```python
import torch

def _softmax(x, axis=-1):
    return torch.softmax(x, dim=axis)
def _gelu(x):
    return torch.nn.functional.gelu(x, approximate="tanh")
def _silu(x):
    return torch.nn.functional.silu(x)
def _sigmoid(x):
    return torch.sigmoid(x)
def _layernorm(x, axis=-1, eps=1e-5):
    m = x.mean(dim=axis, keepdim=True)
    v = x.var(dim=axis, keepdim=True, unbiased=False)
    return (x - m) / torch.sqrt(v + eps)
def _scatter_add(tgt, idx, src, axis=0):
    return tgt.clone().index_add_(axis, idx, src)
def _segment_sum(src, seg, n):
    out = torch.zeros((n,) + src.shape[1:], dtype=src.dtype)
    return out.index_add_(0, seg, src)
def _pad(x, pw):
    flat = tuple(v for pair in reversed(pw) for v in pair)
    return torch.nn.functional.pad(x, flat)

kernel_name = 'concat_heads__gqa_block_cached__mha_block_cached__near_ridge'

def setup_inputs():

    _w = (lambda a: (lambda t: t.to(torch.bfloat16) if t.is_floating_point() else t)(torch.as_tensor(a)))
    def _A_setup():
        B, Sq, D = 8, 192, 640
        Hq, Hkv, Sk, Dh = 32, 32, 512, 128
        assert Hq % Hkv == 0, (Hq, Hkv)
        return {
            "x": _w((lambda *s: torch.randn(*s, dtype=torch.bfloat16) * 0.8)(B, Sq, D)),
            "k_cache": _w((lambda *s: torch.randn(*s, dtype=torch.bfloat16) * 0.8)(B, Hkv, Sk, Dh)),
            "v_cache": _w((lambda *s: torch.randn(*s, dtype=torch.bfloat16) * 0.05)(B, Hkv, Sk, Dh)),
            "wq": _w((lambda *s: torch.randn(*s, dtype=torch.bfloat16) * 0.8)(D, Hq * Dh) * (D ** -0.5)),
            "wo": _w((lambda *s: torch.randn(*s, dtype=torch.bfloat16) * 0.05)(Hq * Dh, D) * ((Hq * Dh) ** -0.5)),
        }
    _A_in = _A_setup()

    def _B_setup():
        B, Sq, D = 8, 192, 640
        H, Sk, Dh = 32, 512, 128
        return {
            "x": _w((lambda *s: torch.randn(*s, dtype=torch.bfloat16) * 0.8)(B, Sq, D)),
            "k_cache": _w((lambda *s: torch.randn(*s, dtype=torch.bfloat16) * 0.8)(B, H, Sk, Dh)),
            "v_cache": _w((lambda *s: torch.randn(*s, dtype=torch.bfloat16) * 0.05)(B, H, Sk, Dh)),
            "wq": _w((lambda *s: torch.randn(*s, dtype=torch.bfloat16) * 0.8)(D, H * Dh) * (D ** -0.5)),
            "wo": _w((lambda *s: torch.randn(*s, dtype=torch.bfloat16) * 0.05)(H * Dh, D) * ((H * Dh) ** -0.5)),
        }
    _B_in = _B_setup()

    return {
        "A_x": _A_in["x"],
        "A_k_cache": _A_in["k_cache"],
        "A_v_cache": _A_in["v_cache"],
        "A_wq": _A_in["wq"],
        "A_wo": _A_in["wo"],
        "B_x": _B_in["x"],
        "B_k_cache": _B_in["k_cache"],
        "B_v_cache": _B_in["v_cache"],
        "B_wq": _B_in["wq"],
        "B_wo": _B_in["wo"],
    }

def reference(A_x, A_k_cache, A_v_cache, A_wq, A_wo, B_x, B_k_cache, B_v_cache, B_wq, B_wo):
    def _A_ref(x, k_cache, v_cache, wq, wo):
        B, Sq, D = x.shape
        Hq, Dh = 32, 128
        Hkv = k_cache.shape[1]
        rep = Hq // Hkv
        q = (lambda x, *d: x.permute(*d))((x @ wq).reshape(B, Sq, Hq, Dh), 0, 2, 1, 3)
        k = (lambda x, n, axis: torch.repeat_interleave(x, n, dim=axis))(k_cache, rep, axis=1)
        v = (lambda x, n, axis: torch.repeat_interleave(x, n, dim=axis))(v_cache, rep, axis=1)
        scores = torch.einsum("bhqd,bhkd->bhqk", q, k) * (Dh ** -0.5)
        p = _softmax(scores, axis=-1)
        o = torch.einsum("bhqk,bhkd->bhqd", p, v)
        return (lambda x, *d: x.permute(*d))(o, 0, 2, 1, 3).reshape(B, Sq, Hq * Dh) @ wo

    def _B_ref(x, k_cache, v_cache, wq, wo):
        B, Sq, D = x.shape
        H, Dh = 32, 128
        q = (lambda x, *d: x.permute(*d))((x @ wq).reshape(B, Sq, H, Dh), 0, 2, 1, 3)
        scores = torch.einsum("bhqd,bhkd->bhqk", q, k_cache) * (Dh ** -0.5)
        p = _softmax(scores, axis=-1)
        o = torch.einsum("bhqk,bhkd->bhqd", p, v_cache)
        return (lambda x, *d: x.permute(*d))(o, 0, 2, 1, 3).reshape(B, Sq, H * Dh) @ wo

    a = _A_ref(A_x, A_k_cache, A_v_cache, A_wq, A_wo)
    b = _B_ref(B_x, B_k_cache, B_v_cache, B_wq, B_wo)
    return torch.concatenate([a, b], axis=-1)
```

```cpp
#include <hip/hip_runtime.h>
#include <hip/hip_bf16.h>

typedef __bf16   bf16x8 __attribute__((ext_vector_type(8)));
typedef __bf16   bf16x4 __attribute__((ext_vector_type(4)));
typedef __bf16   bf16x2 __attribute__((ext_vector_type(2)));
typedef float    f32x4  __attribute__((ext_vector_type(4)));
typedef float    f32x2  __attribute__((ext_vector_type(2)));
typedef unsigned u32x4  __attribute__((ext_vector_type(4)));
typedef unsigned u32x2  __attribute__((ext_vector_type(2)));
#define LAS __attribute__((address_space(3)))
#define DI __device__ __forceinline__
#define SRD3 0x00020000

constexpr int NB = 8, SQ = 192, DM = 640, NH = 32, SK = 512, DH = 128, HD = NH * DH;
constexpr float QSCALE = 0.08838834764831845f * 1.4426950408889634f;
constexpr float THR = 8.0f;

DI unsigned rfl(unsigned x) { return __builtin_amdgcn_readfirstlane(x); }
DI unsigned cvtpk(float lo, float hi) {
  f32x2 v = {lo, hi}; bf16x2 b = __builtin_convertvector(v, bf16x2); return __builtin_bit_cast(unsigned, b);
}
DI float max3f(float a, float b, float c) { return fmaxf(fmaxf(a, b), c); }
DI float max2f(float a, float b) { return fmaxf(a, b); }
DI float fadd_s(float a, float b) { return a + b; }

DI void dma16(unsigned m0v, unsigned voff, __amdgpu_buffer_rsrc_t srd, unsigned soff) {
  asm volatile("s_mov_b32 m0, %0\n\ts_nop 0\n\tbuffer_load_dwordx4 %1, %2, %3 offen lds"
               :: "s"(m0v), "v"(voff), "s"(srd), "s"(soff) : "memory");
}
template <int N> DI void wait_vm() { asm volatile("s_waitcnt vmcnt(%0)" :: "i"(N) : "memory"); }
DI void barrier() { asm volatile("s_waitcnt lgkmcnt(0)\n\ts_barrier" ::: "memory"); }

DI bf16x8 lds_b128(unsigned addr) { return *(const LAS bf16x8*)(size_t)addr; }
DI bf16x8 lds_tr2(unsigned a0, unsigned a1) {
  bf16x4 lo = __builtin_amdgcn_ds_read_tr16_b64_v4bf16((LAS bf16x4*)(size_t)a0);
  bf16x4 hi = __builtin_amdgcn_ds_read_tr16_b64_v4bf16((LAS bf16x4*)(size_t)a1);
  return __builtin_shufflevector(lo, hi, 0, 1, 2, 3, 4, 5, 6, 7);
}
DI f32x4 mfma16(bf16x8 a, bf16x8 b, f32x4 c) { return __builtin_amdgcn_mfma_f32_16x16x32_bf16(a, b, c, 0, 0, 0); }

constexpr unsigned SLOT = 32768u;
constexpr unsigned STG = 65536u, XB0 = STG, WB2 = STG + 24576u, WB0 = WB2 + 16384u, WB1 = WB0 + 16384u, XB1 = WB1 + 16384u;

struct K1Ctx {
  __amdgpu_buffer_rsrc_t srdX, srdW, srdK, srdV;
  unsigned voffX, voffW, voffK, voffV;
  unsigned ldsb, w;
  unsigned xrd0, xrd1, wrd0, wrd1;
};

DI void issueW(const K1Ctx& c, int step, unsigned wbuf) {
  const unsigned m0 = c.ldsb + wbuf + 1024u * c.w, so = (unsigned)step * (32u * 8192u);
  dma16(m0, c.voffW, c.srdW, so);
  dma16(m0 + 8192u, c.voffW, c.srdW, so + 16u * 8192u);
}
DI void issueX(const K1Ctx& c, int chunk, unsigned xbuf) {
  const unsigned m0 = c.ldsb + xbuf + 1024u * c.w, so = (unsigned)chunk * 128u;
  dma16(m0, c.voffX, c.srdX, so);
  dma16(m0 + 8192u, c.voffX, c.srdX, so + 64u * 1280u);
  dma16(m0 + 16384u, c.voffX, c.srdX, so + 128u * 1280u);
}
DI void issueKVp(const K1Ctx& c, int t, int p, unsigned slotoff) {
  const unsigned m0 = c.ldsb + slotoff + 8192u * (unsigned)p + 1024u * c.w;
  const unsigned so = (unsigned)t * 8192u + (unsigned)(p & 1) * 131072u;
  if (p < 2) dma16(m0, c.voffK, c.srdK, so); else dma16(m0, c.voffV, c.srdV, so);
}
DI void issueKV(const K1Ctx& c, int t, unsigned slotoff) {
  issueKVp(c, t, 0, slotoff); issueKVp(c, t, 1, slotoff); issueKVp(c, t, 2, slotoff); issueKVp(c, t, 3, slotoff);
}

DI void qstep(const K1Ctx& c, f32x4 (&acc)[4][2][3], unsigned wbuf, unsigned xbuf, int ks2) {
  bf16x8 xb[3];
  const unsigned xa = (ks2 ? c.xrd1 : c.xrd0) + xbuf;
#pragma unroll
  for (int qb = 0; qb < 3; ++qb) xb[qb] = lds_b128(xa + 2048u * qb);
  const unsigned w0 = c.wrd0 + wbuf, w1 = c.wrd1 + wbuf;
#pragma unroll
  for (int ksq = 0; ksq < 4; ++ksq) {
#pragma unroll
    for (int tt = 0; tt < 2; ++tt) {
      const unsigned a = (tt ? w1 : w0) + 1024u * (ksq >> 1) + 256u * (ksq & 1);
      bf16x8 wa = lds_tr2(a, a + 512u);
#pragma unroll
      for (int qb = 0; qb < 3; ++qb) acc[ksq][tt][qb] = mfma16(wa, xb[qb], acc[ksq][tt][qb]);
    }
  }
}

__global__ __launch_bounds__(512, 2)
void attn_qproj_kernel(const char* __restrict__ xA, const char* __restrict__ kA, const char* __restrict__ vA, const char* __restrict__ wqA,
                       const char* __restrict__ xB, const char* __restrict__ kB, const char* __restrict__ vB, const char* __restrict__ wqB,
                       char* __restrict__ Ows) {
  __shared__ __attribute__((aligned(1024))) char lds[163840];
  const unsigned tid = threadIdx.x, l = tid & 63u;
  const unsigned w = rfl(tid >> 6);
  const unsigned bid = blockIdx.x, xcd = bid & 7u, idx = bid >> 3;
  const unsigned br = xcd >> 2, hp = (xcd & 3u) * 4u + (idx & 3u), b = idx >> 2;
  const unsigned hh = w >> 2, rg = w & 3u;
  const unsigned g = l >> 4, r16 = l & 15u, q4 = (l >> 2) & 3u, p4 = l & 3u;

  K1Ctx c;
  c.ldsb = (unsigned)(size_t)(LAS char*)lds; c.w = w;
  {
    const char* xp = (br ? xB : xA) + (size_t)b * (SQ * DM * 2);
    const char* wp = (br ? wqB : wqA) + (size_t)hp * 512;
    const char* kp = (br ? kB : kA) + (size_t)(b * NH + 2 * hp) * (SK * DH * 2);
    const char* vp = (br ? vB : vA) + (size_t)(b * NH + 2 * hp) * (SK * DH * 2);
    c.srdX = __builtin_amdgcn_make_buffer_rsrc((void*)xp, 0, SQ * DM * 2, SRD3);
    c.srdW = __builtin_amdgcn_make_buffer_rsrc((void*)wp, 0, DM * HD * 2 - (int)hp * 512, SRD3);
    c.srdK = __builtin_amdgcn_make_buffer_rsrc((void*)kp, 0, 2 * SK * DH * 2, SRD3);
    c.srdV = __builtin_amdgcn_make_buffer_rsrc((void*)vp, 0, 2 * SK * DH * 2, SRD3);
  }
  c.voffX = (8u * w + (l >> 3)) * 1280u + 16u * ((l & 7u) ^ (4u * (w & 1u) + (l >> 4)));
  c.voffW = (8u * (w >> 2) + 4u * (l >> 5) + q4) * 8192u + (2u * (w & 3u) + ((l >> 4) & 1u)) * 64u + 16u * (p4 ^ ((w >> 2) & 1u));
  c.voffK = (8u * (w >> 1) + (l >> 3)) * 256u + (w & 1u) * 128u + 16u * ((l & 7u) ^ ((4u * ((w >> 1) & 1u) + (l >> 4)) & 7u));
  c.voffV = (8u * (w >> 1) + ((l >> 2) & 7u)) * 256u + (2u * (w & 1u) + (l >> 5)) * 64u + 16u * (p4 ^ (((l >> 4) & 1u) << 1));
  c.xrd0 = c.ldsb + 1024u * (6u * rg + (r16 >> 3)) + 128u * (r16 & 7u) + 16u * (g ^ (r16 >> 1));
  c.xrd1 = c.xrd0 ^ 64u;
  c.wrd0 = c.ldsb + 4096u * g + 2048u * hh + 64u * q4 + 16u * (p4 ^ (g & 1u)) + 8u * (p4 & 1u);
  c.wrd1 = c.wrd0 ^ 8u;

  f32x4 qacc[4][2][3];
#pragma unroll
  for (int i = 0; i < 4; ++i)
#pragma unroll
    for (int t = 0; t < 2; ++t)
#pragma unroll
      for (int q = 0; q < 3; ++q) qacc[i][t][q] = f32x4{0.f, 0.f, 0.f, 0.f};

  issueW(c, 0, WB0); issueX(c, 0, XB0); issueKVp(c, 0, 0, 0u); issueW(c, 1, WB1);
  unsigned wcur = WB0, wnxt = WB1, wfre = WB2;
  unsigned xcur = XB0, xoth = XB1;
  for (int cch = 0; cch < 7; ++cch) {
    wait_vm<3>(); barrier();
    issueW(c, 2 * cch + 2, wfre); issueX(c, cch + 1, xoth);
    issueKVp(c, (cch + 1) >> 2, (cch + 1) & 3, (unsigned)((cch + 1) >> 2) * SLOT);
    qstep(c, qacc, wcur, xcur, 0);
    wait_vm<6>(); barrier();
    issueW(c, 2 * cch + 3, wcur);
    qstep(c, qacc, wnxt, xcur, 1);
    { unsigned t = wcur; wcur = wfre; wfre = wnxt; wnxt = t; t = xcur; xcur = xoth; xoth = t; }
  }
  wait_vm<3>(); barrier();
  issueW(c, 16, WB1); issueX(c, 8, XB0);
  qstep(c, qacc, WB2, XB1, 0);
  wait_vm<5>(); barrier();
  issueW(c, 17, WB2);
  qstep(c, qacc, WB0, XB1, 1);
  wait_vm<2>(); barrier();
  issueW(c, 18, WB0); issueX(c, 9, XB1);
  qstep(c, qacc, WB1, XB0, 0);
  wait_vm<5>(); barrier();
  issueW(c, 19, WB1);
  qstep(c, qacc, WB2, XB0, 1);
  wait_vm<2>(); barrier();
  issueKV(c, 2, 2u * SLOT);
  qstep(c, qacc, WB0, XB1, 0);
  wait_vm<4>(); barrier();
  qstep(c, qacc, WB1, XB1, 1);

  bf16x8 qf[4][3];
  {
    const bool odd = (g & 1u) != 0u;
#pragma unroll
    for (int ks = 0; ks < 4; ++ks)
#pragma unroll
      for (int qb = 0; qb < 3; ++qb) {
        const f32x4 a = qacc[ks][0][qb], bb = qacc[ks][1][qb];
        const unsigned a0 = cvtpk(a[0] * QSCALE, a[1] * QSCALE), a1 = cvtpk(a[2] * QSCALE, a[3] * QSCALE);
        const unsigned b0 = cvtpk(bb[0] * QSCALE, bb[1] * QSCALE), b1 = cvtpk(bb[2] * QSCALE, bb[3] * QSCALE);
        u32x4 u = {odd ? b0 : a0, odd ? b1 : a1, odd ? a0 : b0, odd ? a1 : b1};
        qf[ks][qb] = __builtin_bit_cast(bf16x8, u);
      }
  }

  const unsigned krd0 = c.ldsb + 8192u * hh + 2048u * (r16 >> 3) + 128u * (r16 & 7u) + 16u * (g ^ (r16 >> 1));
  const unsigned krd1 = krd0 ^ 64u;
  const unsigned vrd0 = c.ldsb + 16384u + 8192u * hh + 2048u * (g >> 1) + 64u * (4u * (g & 1u) + q4) + 16u * ((p4 >> 1) ^ ((g & 1u) << 1)) + 8u * (p4 & 1u);
  const unsigned vrd1 = vrd0 ^ 32u;

  f32x4 oacc[8][3];
#pragma unroll
  for (int i = 0; i < 8; ++i)
#pragma unroll
    for (int q = 0; q < 3; ++q) oacc[i][q] = f32x4{0.f, 0.f, 0.f, 0.f};
  float lsum[3] = {0.f, 0.f, 0.f}, negm[3] = {0.f, 0.f, 0.f};

  unsigned slot = 0u, fslot = 4u * SLOT;
  for (int j = 0; j < 16; ++j) {
    if (j <= 12) wait_vm<12>(); else if (j == 13) wait_vm<8>(); else if (j == 14) wait_vm<4>(); else wait_vm<0>();
    barrier();
    if (j == 0) issueKV(c, 3, 3u * SLOT);
    if (j < 12) issueKV(c, j + 4, fslot);

    f32x4 s[2][3];
    {
      const unsigned k0 = krd0 + slot, k1 = krd1 + slot;
#pragma unroll
      for (int kb = 0; kb < 2; ++kb) {
#pragma unroll
        for (int qb = 0; qb < 3; ++qb) s[kb][qb] = f32x4{negm[qb], negm[qb], negm[qb], negm[qb]};
#pragma unroll
        for (int ks = 0; ks < 4; ++ks) {
          bf16x8 kf = lds_b128(((ks & 1) ? k1 : k0) + 4096u * kb + 1024u * (ks >> 1));
#pragma unroll
          for (int qb = 0; qb < 3; ++qb) s[kb][qb] = mfma16(kf, qf[ks][qb], s[kb][qb]);
        }
      }
    }
    float mx[3];
#pragma unroll
    for (int qb = 0; qb < 3; ++qb) {
      float a = max3f(s[0][qb][0], s[0][qb][1], s[0][qb][2]);
      float bq = max3f(s[0][qb][3], s[1][qb][0], s[1][qb][1]);
      a = max3f(a, s[1][qb][2], s[1][qb][3]);
      mx[qb] = max2f(a, bq);
    }
    const float mall = max3f(mx[0], mx[1], mx[2]);
    if (j == 0 || __any(mall > THR)) {
#pragma unroll
      for (int qb = 0; qb < 3; ++qb) {
        float m = mx[qb];
        m = fmaxf(m, __shfl_xor(m, 16)); m = fmaxf(m, __shfl_xor(m, 32));
        const float dl = (j == 0) ? m : fmaxf(m, 0.f);
        negm[qb] -= dl;
#pragma unroll
        for (int kb = 0; kb < 2; ++kb)
#pragma unroll
          for (int r = 0; r < 4; ++r) s[kb][qb][r] -= dl;
        if (j != 0) {
          const float f = __builtin_amdgcn_exp2f(-dl);
          lsum[qb] *= f;
#pragma unroll
          for (int db = 0; db < 8; ++db) oacc[db][qb] *= f;
        }
      }
    }
    bf16x8 pf[3];
#pragma unroll
    for (int qb = 0; qb < 3; ++qb) {
      float e[8];
#pragma unroll
      for (int r = 0; r < 4; ++r) { e[r] = __builtin_amdgcn_exp2f(s[0][qb][r]); e[4 + r] = __builtin_amdgcn_exp2f(s[1][qb][r]); }
      float t0 = fadd_s(e[0], e[1]), t1 = fadd_s(e[2], e[3]), t2 = fadd_s(e[4], e[5]), t3 = fadd_s(e[6], e[7]);
      t0 = fadd_s(t0, t1); t2 = fadd_s(t2, t3);
      lsum[qb] = fadd_s(lsum[qb], fadd_s(t0, t2));
      u32x4 u = {cvtpk(e[0], e[1]), cvtpk(e[2], e[3]), cvtpk(e[4], e[5]), cvtpk(e[6], e[7])};
      pf[qb] = __builtin_bit_cast(bf16x8, u);
    }
    {
      const unsigned v0 = vrd0 + slot, v1 = vrd1 + slot;
#pragma unroll
      for (int db = 0; db < 8; ++db) {
        const unsigned a = ((db & 1) ? v1 : v0) + 512u * (db >> 1);
        bf16x8 vf = lds_tr2(a, a + 4096u);
#pragma unroll
        for (int qb = 0; qb < 3; ++qb) oacc[db][qb] = mfma16(vf, pf[qb], oacc[db][qb]);
      }
    }
    fslot = slot;
    slot = (slot == 4u * SLOT) ? 0u : slot + SLOT;
  }

  {
    const unsigned head = 2u * hp + hh;
    char* ob = Ows + ((size_t)((br * NB + b) * 12u + 3u * rg) * 128u + head * 4u) * 1024u + 16u * l;
#pragma unroll
    for (int qb = 0; qb < 3; ++qb) {
      float ls = lsum[qb];
      ls += __shfl_xor(ls, 16); ls += __shfl_xor(ls, 32);
      const float inv = 1.0f / ls;
#pragma unroll
      for (int s2 = 0; s2 < 4; ++s2) {
        const f32x4 a = oacc[2 * s2][qb], bb = oacc[2 * s2 + 1][qb];
        u32x4 u = {cvtpk(a[0] * inv, a[1] * inv), cvtpk(a[2] * inv, a[3] * inv), cvtpk(bb[0] * inv, bb[1] * inv), cvtpk(bb[2] * inv, bb[3] * inv)};
        *(u32x4*)(ob + (size_t)qb * (128u * 1024u) + s2 * 1024u) = u;
      }
    }
  }
}

constexpr int K2D = 4;
constexpr unsigned K2STG = 5120u;

DI void gload16(u32x4& dst, unsigned voff, __amdgpu_buffer_rsrc_t srd, unsigned soff) {
  asm volatile("buffer_load_dwordx4 %0, %1, %2, %3 offen" : "=v"(dst) : "v"(voff), "s"(srd), "s"(soff) : "memory");
}

struct K2Ctx {
  __amdgpu_buffer_rsrc_t srdO, srdW;
  unsigned voffO, voffW[5], ldsw, wrd, kbase;
};

DI void k2issue(const K2Ctx& c, u32x4 (&of)[6], int step, int buf) {
  const unsigned ks = c.kbase + (unsigned)step;
#pragma unroll
  for (int i = 0; i < 6; ++i) gload16(of[i], c.voffO, c.srdO, ks * 1024u + (unsigned)i * 131072u);
#pragma unroll
  for (int i = 0; i < 5; ++i) dma16(c.ldsw + (unsigned)buf * K2STG + 1024u * i, c.voffW[i], c.srdW, ks * (32u * 1280u));
}
template <int N> DI void k2wait(u32x4 (&of)[6]) {
  asm volatile("s_waitcnt vmcnt(%6)" : "+v"(of[0]), "+v"(of[1]), "+v"(of[2]), "+v"(of[3]), "+v"(of[4]), "+v"(of[5]) : "i"(N) : "memory");
}
DI void k2comp(const K2Ctx& c, f32x4 (&acc)[5][6], u32x4 (&of)[6], int buf) {
  const unsigned a = c.wrd + (unsigned)buf * K2STG;
#pragma unroll
  for (int nb = 0; nb < 5; ++nb) {
    bf16x8 wf = lds_tr2(a + 32u * nb, a + 32u * nb + 2560u);
#pragma unroll
    for (int qb = 0; qb < 6; ++qb) acc[nb][qb] = mfma16(wf, __builtin_bit_cast(bf16x8, of[qb]), acc[nb][qb]);
  }
}

__global__ __launch_bounds__(256, 1)
void oproj_kernel(const char* __restrict__ Ows, const char* __restrict__ woA, const char* __restrict__ woB, char* __restrict__ out) {
  __shared__ __attribute__((aligned(1024))) char lds[4 * 30720];
  const unsigned tid = threadIdx.x, l = tid & 63u;
  const unsigned w = rfl(tid >> 6);
  const unsigned bid = blockIdx.x, xcd = bid & 7u, idx = bid >> 3;
  const unsigned br = xcd >> 2, mt = (xcd & 3u) * 4u + (idx >> 3), nt = idx & 7u;
  const unsigned g = l >> 4, q4 = (l >> 2) & 3u, p4 = l & 3u;
  const unsigned ldsb = (unsigned)(size_t)(LAS char*)lds;

  K2Ctx c;
  {
    const char* op = Ows + (size_t)(br * 96u + mt * 6u) * 131072u;
    const char* wp = (br ? woB : woA) + (size_t)nt * 160u;
    c.srdO = __builtin_amdgcn_make_buffer_rsrc((void*)op, 0, 6 * 131072, SRD3);
    c.srdW = __builtin_amdgcn_make_buffer_rsrc((void*)wp, 0, HD * DM * 2 - (int)nt * 160, SRD3);
  }
  c.voffO = 16u * l;
#pragma unroll
  for (int i = 0; i < 5; ++i) { const unsigned ci = 64u * i + l, row = ci / 10u, ch = ci - row * 10u; c.voffW[i] = row * 1280u + ch * 16u; }
  c.ldsw = ldsb + w * 30720u;
  c.wrd = c.ldsw + (4u * g + q4) * 160u + 8u * p4;
  c.kbase = w * 32u;

  f32x4 acc[5][6];
#pragma unroll
  for (int i = 0; i < 5; ++i)
#pragma unroll
    for (int q = 0; q < 6; ++q) acc[i][q] = f32x4{0.f, 0.f, 0.f, 0.f};

  u32x4 o0[6], o1[6], o2[6], o3[6];
  k2issue(c, o0, 0, 0); k2issue(c, o1, 1, 1); k2issue(c, o2, 2, 2);
  for (int s = 0; s < 28; s += 4) {
    k2issue(c, o3, s + 3, 3); k2wait<33>(o0); k2comp(c, acc, o0, 0); asm volatile("s_waitcnt lgkmcnt(0)" ::: "memory");
    k2issue(c, o0, s + 4, 0); k2wait<33>(o1); k2comp(c, acc, o1, 1); asm volatile("s_waitcnt lgkmcnt(0)" ::: "memory");
    k2issue(c, o1, s + 5, 1); k2wait<33>(o2); k2comp(c, acc, o2, 2); asm volatile("s_waitcnt lgkmcnt(0)" ::: "memory");
    k2issue(c, o2, s + 6, 2); k2wait<33>(o3); k2comp(c, acc, o3, 3); asm volatile("s_waitcnt lgkmcnt(0)" ::: "memory");
  }
  k2issue(c, o3, 31, 3); k2wait<33>(o0); k2comp(c, acc, o0, 0);
  k2wait<22>(o1); k2comp(c, acc, o1, 1);
  k2wait<11>(o2); k2comp(c, acc, o2, 2);
  k2wait<0>(o3); k2comp(c, acc, o3, 3);
  asm volatile("s_waitcnt lgkmcnt(0)" ::: "memory");

  {
    LAS char* base = (LAS char*)(size_t)c.ldsw;
#pragma unroll
    for (int qb = 0; qb < 6; ++qb)
#pragma unroll
      for (int nb = 0; nb < 5; ++nb)
        *(LAS f32x4*)(base + ((16u * qb + (l & 15u)) * 80u + 16u * nb + 4u * g) * 4u) = acc[nb][qb];
  }
  __syncthreads();
  for (unsigned it = tid; it < 960u; it += 256u) {
    const unsigned q = it / 10u, ch = it - q * 10u;
    const LAS char* src = (const LAS char*)(size_t)(ldsb + (q * 80u + ch * 8u) * 4u);
    f32x4 s0 = *(const LAS f32x4*)src, s1 = *(const LAS f32x4*)(src + 16);
#pragma unroll
    for (int ww = 1; ww < 4; ++ww) { s0 += *(const LAS f32x4*)(src + ww * 30720); s1 += *(const LAS f32x4*)(src + ww * 30720 + 16); }
    u32x4 u = {cvtpk(s0[0], s0[1]), cvtpk(s0[2], s0[3]), cvtpk(s1[0], s1[1]), cvtpk(s1[2], s1[3])};
    *(u32x4*)(out + ((size_t)(mt * 96u + q) * 1280u + br * 640u + nt * 80u + ch * 8u) * 2u) = u;
  }
}

extern "C" void kernel_launch(void* const* d_in, const int* in_sizes, int n_in,
                              void* d_out, int out_size, void* d_ws, size_t ws_size,
                              hipStream_t stream) {
  const char* xA = (const char*)d_in[0]; const char* kA = (const char*)d_in[1]; const char* vA = (const char*)d_in[2];
  const char* wqA = (const char*)d_in[3]; const char* woA = (const char*)d_in[4];
  const char* xB = (const char*)d_in[5]; const char* kB = (const char*)d_in[6]; const char* vB = (const char*)d_in[7];
  const char* wqB = (const char*)d_in[8]; const char* woB = (const char*)d_in[9];
  char* Ows = (char*)d_ws;
  attn_qproj_kernel<<<256, 512, 0, stream>>>(xA, kA, vA, wqA, xB, kB, vB, wqB, Ows);
  oproj_kernel<<<256, 256, 0, stream>>>(Ows, woA, woB, (char*)d_out);
}
```

```cpp
#include <hip/hip_runtime.h>
#include <hip/hip_bf16.h>

typedef __bf16   bf16x8 __attribute__((ext_vector_type(8)));
typedef __bf16   bf16x4 __attribute__((ext_vector_type(4)));
typedef __bf16   bf16x2 __attribute__((ext_vector_type(2)));
typedef float    f32x4  __attribute__((ext_vector_type(4)));
typedef float    f32x2  __attribute__((ext_vector_type(2)));
typedef unsigned u32x4  __attribute__((ext_vector_type(4)));
typedef unsigned u32x2  __attribute__((ext_vector_type(2)));
#define LAS __attribute__((address_space(3)))
#define DI __device__ __forceinline__
#define SRD3 0x00020000

constexpr int NB = 8, SQ = 192, DM = 640, NH = 32, SK = 512, DH = 128, HD = NH * DH;
constexpr float QSCALE = 0.08838834764831845f * 1.4426950408889634f;
constexpr float PBIAS = 7.0f;

#define STAMP(base, k, s) do {} while (0)
#define CSTAMP(base, k, s) do {} while (0)
#define FS(i) do {} while (0)
DI unsigned rfl(unsigned x) { return __builtin_amdgcn_readfirstlane(x); }
DI unsigned cvtpk(float lo, float hi) {
  f32x2 v = {lo, hi}; bf16x2 b = __builtin_convertvector(v, bf16x2); return __builtin_bit_cast(unsigned, b);
}
DI float max3f(float a, float b, float c) { return fmaxf(fmaxf(a, b), c); }
DI float max2f(float a, float b) { return fmaxf(a, b); }
DI float fadd_s(float a, float b) { return a + b; }

DI void dma16(unsigned m0v, unsigned voff, __amdgpu_buffer_rsrc_t srd, unsigned soff) {
  asm volatile("s_mov_b32 m0, %0\n\ts_nop 0\n\tbuffer_load_dwordx4 %1, %2, %3 offen lds"
               :: "s"(m0v), "v"(voff), "s"(srd), "s"(soff) : "memory");
}
DI void dma16nt(unsigned m0v, unsigned voff, __amdgpu_buffer_rsrc_t srd, unsigned soff) {
  asm volatile("s_mov_b32 m0, %0\n\ts_nop 0\n\tbuffer_load_dwordx4 %1, %2, %3 offen nt lds"
               :: "s"(m0v), "v"(voff), "s"(srd), "s"(soff) : "memory");
}
template <int N> DI void wait_vm() { asm volatile("s_waitcnt vmcnt(%0)" :: "i"(N) : "memory"); }
DI void barrier() { asm volatile("s_waitcnt lgkmcnt(0)\n\ts_barrier" ::: "memory"); }

DI bf16x8 lds_b128(unsigned addr) { return *(const LAS bf16x8*)(size_t)addr; }
DI bf16x8 lds_tr2(unsigned a0, unsigned a1) {
  bf16x4 lo = __builtin_amdgcn_ds_read_tr16_b64_v4bf16((LAS bf16x4*)(size_t)a0);
  bf16x4 hi = __builtin_amdgcn_ds_read_tr16_b64_v4bf16((LAS bf16x4*)(size_t)a1);
  return __builtin_shufflevector(lo, hi, 0, 1, 2, 3, 4, 5, 6, 7);
}
DI f32x4 mfma16(bf16x8 a, bf16x8 b, f32x4 c) { return __builtin_amdgcn_mfma_f32_16x16x32_bf16(a, b, c, 0, 0, 0); }

constexpr unsigned SLOT = 32768u;
constexpr unsigned STG = 65536u, XB0 = STG, WB2 = STG + 24576u, WB0 = WB2 + 16384u, WB1 = WB0 + 16384u, XB1 = WB1 + 16384u;

struct K1Ctx {
  __amdgpu_buffer_rsrc_t srdX, srdW, srdK, srdV;
  unsigned voffX, voffW, voffK, voffV;
  unsigned ldsb, w;
  unsigned xrd0, xrd1, wrd0, wrd1;
};

DI void issueW(const K1Ctx& c, int step, unsigned wbuf) {
  const unsigned m0 = c.ldsb + wbuf + 1024u * c.w, so = (unsigned)step * (32u * 8192u);
  dma16(m0, c.voffW, c.srdW, so);
  dma16(m0 + 8192u, c.voffW, c.srdW, so + 16u * 8192u);
}
DI void issueX(const K1Ctx& c, int chunk, unsigned xbuf) {
  const unsigned m0 = c.ldsb + xbuf + 1024u * c.w, so = (unsigned)chunk * 128u;
  dma16(m0, c.voffX, c.srdX, so);
  dma16(m0 + 8192u, c.voffX, c.srdX, so + 64u * 1280u);
  dma16(m0 + 16384u, c.voffX, c.srdX, so + 128u * 1280u);
}
DI void issueKVp(const K1Ctx& c, int t, int p, unsigned slotoff) {
  const unsigned m0 = c.ldsb + slotoff + 8192u * (unsigned)p + 1024u * c.w;
  const unsigned so = (unsigned)t * 8192u + (unsigned)(p & 1) * 131072u;
  if (p < 2) dma16nt(m0, c.voffK, c.srdK, so); else dma16nt(m0, c.voffV, c.srdV, so);
}
DI void issueKV(const K1Ctx& c, int t, unsigned slotoff) {
  issueKVp(c, t, 0, slotoff); issueKVp(c, t, 1, slotoff); issueKVp(c, t, 2, slotoff); issueKVp(c, t, 3, slotoff);
}

#define SGB(mask, n) __builtin_amdgcn_sched_group_barrier(mask, n, 0)
#define SBAR() __builtin_amdgcn_sched_barrier(0)
struct QFr { bf16x8 w[8]; bf16x8 x[3]; };
DI void qread(const K1Ctx& c, QFr& f, unsigned wbuf, unsigned xbuf, int ks2) {
  const unsigned xa = (ks2 ? c.xrd1 : c.xrd0) + xbuf;
#pragma unroll
  for (int qb = 0; qb < 3; ++qb) f.x[qb] = lds_b128(xa + 2048u * qb);
  const unsigned w0 = c.wrd0 + wbuf, w1 = c.wrd1 + wbuf;
#pragma unroll
  for (int i = 0; i < 8; ++i) {
    const int ksq = i >> 1, tt = i & 1;
    const unsigned a = (tt ? w1 : w0) + 1024u * (ksq >> 1) + 256u * (ksq & 1);
    f.w[i] = lds_tr2(a, a + 512u);
  }
}
DI void qmma(f32x4 (&acc)[8][3], const QFr& f) {
#pragma unroll
  for (int i = 0; i < 8; ++i)
#pragma unroll
    for (int qb = 0; qb < 3; ++qb) acc[i][qb] = mfma16(f.w[i], f.x[qb], acc[i][qb]);
}
DI unsigned opq(unsigned x) { asm volatile("" : "+v"(x)); return x; }
#define PRIO(x) __builtin_amdgcn_s_setprio(x)
template <int N, class F>
DI void qstep(const K1Ctx& c, f32x4 (&acc)[8][3], const QFr& cur, QFr& nxt, unsigned wbuf, unsigned xbuf, int ks2, F iss) {
  const unsigned xa = opq((ks2 ? c.xrd1 : c.xrd0) + xbuf);
  const unsigned w0 = opq(c.wrd0 + wbuf), w1 = opq(c.wrd1 + wbuf);
  SBAR();
  wait_vm<N>(); barrier();
#pragma unroll
  for (int gq = 0; gq < 6; ++gq) {
    SBAR();
    if (gq == 0) {
#pragma unroll
      for (int qb = 0; qb < 3; ++qb) nxt.x[qb] = lds_b128(xa + 2048u * qb);
    }
    if (gq < 4) {
#pragma unroll
      for (int i = 2 * gq; i < 2 * gq + 2; ++i) {
        const unsigned a = ((i & 1) ? w1 : w0) + 1024u * (i >> 2) + 256u * ((i >> 1) & 1);
        nxt.w[i] = lds_tr2(a, a + 512u);
      }
    }
#pragma unroll
    for (int m = 4 * gq; m < 4 * gq + 4; ++m) acc[m / 3][m % 3] = mfma16(cur.w[m / 3], cur.x[m % 3], acc[m / 3][m % 3]);
    if (gq == 0) { SGB(0x8, 1); SGB(0x100, 2); SGB(0x8, 1); SGB(0x100, 2); SGB(0x8, 1); SGB(0x100, 2); SGB(0x8, 1); SGB(0x100, 1); }
    else if (gq < 4) { SGB(0x8, 1); SGB(0x100, 1); SGB(0x8, 1); SGB(0x100, 1); SGB(0x8, 1); SGB(0x100, 1); SGB(0x8, 1); SGB(0x100, 1); }
    SBAR();
    if (gq < 5) iss(gq);
  }
}
DI void issueW1(const K1Ctx& c, int step, unsigned wbuf, int i) {
  dma16(c.ldsb + wbuf + 1024u * c.w + 8192u * i, c.voffW, c.srdW, (unsigned)step * (32u * 8192u) + (unsigned)i * (16u * 8192u));
}
DI void issueX1(const K1Ctx& c, int chunk, unsigned xbuf, int i) {
  dma16(c.ldsb + xbuf + 1024u * c.w + 8192u * i, c.voffX, c.srdX, (unsigned)chunk * 128u + (unsigned)i * (64u * 1280u));
}

__global__ __launch_bounds__(512, 2)
void attn_qproj_kernel(const char* __restrict__ xA, const char* __restrict__ kA, const char* __restrict__ vA, const char* __restrict__ wqA,
                       const char* __restrict__ xB, const char* __restrict__ kB, const char* __restrict__ vB, const char* __restrict__ wqB,
                       char* __restrict__ Ows) {
  __shared__ __attribute__((aligned(1024))) char lds[163840];
  const unsigned tid = threadIdx.x, l = tid & 63u;
  const unsigned w = rfl(tid >> 6);
  const unsigned bid = blockIdx.x, xcd = bid & 7u, idx = bid >> 3;
  const unsigned br = xcd >> 2, hp = (xcd & 3u) * 4u + (idx & 3u), b = idx >> 2;
  const unsigned hh = w >> 2, rg = w & 3u;
  const unsigned g = l >> 4, r16 = l & 15u, q4 = (l >> 2) & 3u, p4 = l & 3u;

  STAMP(Ows, 0, 0); CSTAMP(Ows, 0, 0);
  K1Ctx c;
  c.ldsb = (unsigned)(size_t)(LAS char*)lds; c.w = w;
  {
    const char* xp = (br ? xB : xA) + (size_t)b * (SQ * DM * 2);
    const char* wp = (br ? wqB : wqA) + (size_t)hp * 512;
    const char* kp = (br ? kB : kA) + (size_t)(b * NH + 2 * hp) * (SK * DH * 2);
    const char* vp = (br ? vB : vA) + (size_t)(b * NH + 2 * hp) * (SK * DH * 2);
    c.srdX = __builtin_amdgcn_make_buffer_rsrc((void*)xp, 0, SQ * DM * 2, SRD3);
    c.srdW = __builtin_amdgcn_make_buffer_rsrc((void*)wp, 0, DM * HD * 2 - (int)hp * 512, SRD3);
    c.srdK = __builtin_amdgcn_make_buffer_rsrc((void*)kp, 0, 2 * SK * DH * 2, SRD3);
    c.srdV = __builtin_amdgcn_make_buffer_rsrc((void*)vp, 0, 2 * SK * DH * 2, SRD3);
  }
  c.voffX = (8u * w + (l >> 3)) * 1280u + 16u * ((l & 7u) ^ (4u * (w & 1u) + (l >> 4)));
  c.voffW = (8u * (w >> 2) + 4u * (l >> 5) + q4) * 8192u + (2u * (w & 3u) + ((l >> 4) & 1u)) * 64u + 16u * (p4 ^ ((w >> 2) & 1u));
  c.voffK = (8u * (w >> 1) + (l >> 3)) * 256u + (w & 1u) * 128u + 16u * ((l & 7u) ^ ((4u * ((w >> 1) & 1u) + (l >> 4)) & 7u));
  c.voffV = (8u * (w >> 1) + ((l >> 2) & 7u)) * 256u + (2u * (w & 1u) + (l >> 5)) * 64u + 16u * (p4 ^ (((l >> 4) & 1u) << 1));
  c.xrd0 = c.ldsb + 1024u * (6u * rg + (r16 >> 3)) + 128u * (r16 & 7u) + 16u * (g ^ (r16 >> 1));
  c.xrd1 = c.xrd0 ^ 64u;
  c.wrd0 = c.ldsb + 4096u * g + 2048u * hh + 64u * q4 + 16u * (p4 ^ (g & 1u)) + 8u * (p4 & 1u);
  c.wrd1 = c.wrd0 ^ 8u;

  const int rot = (int)((b + 3u * (idx & 3u) + 5u * (xcd & 1u)) % 10u);
#define EXP_NOROT
  auto rc = [&](int ch) { return ch; };
  f32x4 qacc[8][3];
#pragma unroll
  for (int i = 0; i < 8; ++i)
#pragma unroll
    for (int q = 0; q < 3; ++q) qacc[i][q] = f32x4{0.f, 0.f, 0.f, 0.f};

  QFr fa, fb;
  issueW(c, 2 * rc(0), WB0); issueX(c, rc(0), XB0); issueW(c, 2 * rc(0) + 1, WB1);
  issueKVp(c, 0, 0, 0u); issueW(c, 2 * rc(1), WB2); issueX(c, rc(1), XB1);
  wait_vm<8>(); barrier();
  STAMP(Ows, 0, 1); CSTAMP(Ows, 0, 1);
  qread(c, fa, WB0, XB0, 0);
  unsigned w0b = WB0, w1b = WB1, w2b = WB2;
  unsigned xcur = XB0, xoth = XB1;
  const bool ga = (hh == 0u);
  for (int cch = 0; cch < 8; ++cch) {
    const int kp = cch < 7 ? cch + 1 : 7;
    qstep<6>(c, qacc, fa, fb, w1b, xcur, 1, [&](int i) {
      if (i == 0 || i == 2) issueW1(c, 2 * rc(cch + 1) + 1, w0b, i >> 1);
      if (i == 4) issueKVp(c, kp >> 2, kp & 3, (unsigned)(kp >> 2) * SLOT); });
    qstep<3>(c, qacc, fb, fa, w2b, xoth, 0, [&](int i) {
      if (i < 2) issueW1(c, 2 * rc(cch + 2), w1b, i); else issueX1(c, rc(cch + 2), xcur, i - 2); });
    { unsigned t = w0b; w0b = w2b; w2b = w1b; w1b = t; t = xcur; xcur = xoth; xoth = t; }
  }
  qstep<6>(c, qacc, fa, fb, WB2, XB0, 1, [&](int i) { if (i == 0 || i == 2) issueW1(c, 2 * rc(9) + 1, WB1, i >> 1); });
  qstep<2>(c, qacc, fb, fa, WB0, XB1, 0, [&](int i) { if (i < 4) issueKVp(c, 2, i, 2u * SLOT); });
  qstep<4>(c, qacc, fa, fb, WB1, XB1, 1, [&](int i) {});
  barrier();
  issueKV(c, 3, 3u * SLOT);
  qmma(qacc, fb);

  STAMP(Ows, 0, 2); CSTAMP(Ows, 0, 2);
  const unsigned krd0 = c.ldsb + 8192u * hh + 2048u * (r16 >> 3) + 128u * (r16 & 7u) + 16u * (g ^ (r16 >> 1));
  const unsigned krd1 = krd0 ^ 64u;
  const unsigned vrd0 = c.ldsb + 16384u + 8192u * hh + 2048u * (g >> 1) + 64u * (4u * (g & 1u) + q4) + 16u * ((p4 >> 1) ^ ((g & 1u) << 1)) + 8u * (p4 & 1u);
  const unsigned vrd1 = vrd0 ^ 32u;
  bf16x8 kf[8];
#pragma unroll
  for (int i = 0; i < 8; ++i) kf[i] = lds_b128(((i & 1) ? krd1 : krd0) + 4096u * (i >> 2) + 1024u * ((i >> 1) & 1));

  bf16x8 qf[4][3];
  {
    const bool odd = (g & 1u) != 0u;
#pragma unroll
    for (int ks = 0; ks < 4; ++ks)
#pragma unroll
      for (int qb = 0; qb < 3; ++qb) {
        const f32x4 a = qacc[2 * ks][qb], bb = qacc[2 * ks + 1][qb];
        const unsigned a0 = cvtpk(a[0] * QSCALE, a[1] * QSCALE), a1 = cvtpk(a[2] * QSCALE, a[3] * QSCALE);
        const unsigned b0 = cvtpk(bb[0] * QSCALE, bb[1] * QSCALE), b1 = cvtpk(bb[2] * QSCALE, bb[3] * QSCALE);
        u32x4 u = {odd ? b0 : a0, odd ? b1 : a1, odd ? a0 : b0, odd ? a1 : b1};
        qf[ks][qb] = __builtin_bit_cast(bf16x8, u);
      }
  }

  f32x4 oacc[8][3], lacc[3], negm[3];
#pragma unroll
  for (int q = 0; q < 3; ++q) {
#pragma unroll
    for (int i = 0; i < 8; ++i) oacc[i][q] = f32x4{0.f, 0.f, 0.f, 0.f};
    lacc[q] = f32x4{0.f, 0.f, 0.f, 0.f}; negm[q] = f32x4{0.f, 0.f, 0.f, 0.f};
  }
  const u32x4 ones_u = {0x3F803F80u, 0x3F803F80u, 0x3F803F80u, 0x3F803F80u};
  bf16x8 ones = __builtin_bit_cast(bf16x8, ones_u);
  asm volatile("" : "+v"(ones));

  auto sync = [&](int j) {
    if (j <= 12) wait_vm<8>(); else if (j == 13) wait_vm<4>(); else wait_vm<0>();
    barrier();
  };
  unsigned slot = 0u, fslot = 4u * SLOT;
  if (!ga) sync(0);
  for (int j = 0; j < 16; ++j) {
    const unsigned nslot = (slot == 4u * SLOT) ? 0u : slot + SLOT;
    const unsigned v0 = opq(vrd0 + slot), v1 = opq(vrd1 + slot);
    FS(0);
    SBAR();
    if (ga) sync(j);
    FS(1);

    f32x4 s[2][3];
    bf16x8 vf[8];
    PRIO(1);
#pragma unroll
    for (int gq = 0; gq < 4; ++gq) {
      SBAR();
#pragma unroll
      for (int db = 2 * gq; db < 2 * gq + 2; ++db) {
        const unsigned a = ((db & 1) ? v1 : v0) + 512u * (db >> 1);
        vf[db] = lds_tr2(a, a + 4096u);
      }
#pragma unroll
      for (int i = 2 * gq; i < 2 * gq + 2; ++i)
#pragma unroll
        for (int qb = 0; qb < 3; ++qb) s[i >> 2][qb] = mfma16(kf[i], qf[i & 3][qb], (i & 3) == 0 ? negm[qb] : s[i >> 2][qb]);
      SGB(0x8, 3); SGB(0x100, 2); SGB(0x8, 3); SGB(0x100, 2);
      SBAR();
      if (j < 12) issueKVp(c, j + 4, gq, fslot);
    }
    PRIO(0);
    FS(2);
    SBAR();
    if (!ga && j < 15) sync(j + 1);
    SBAR();
    FS(3);
    u32x4 pu[3];
#pragma unroll
    for (int qb = 0; qb < 3; ++qb)
#pragma unroll
      for (int kb = 0; kb < 2; ++kb) {
        pu[qb][2 * kb]     = cvtpk(__builtin_amdgcn_exp2f(s[kb][qb][0]), __builtin_amdgcn_exp2f(s[kb][qb][1]));
        pu[qb][2 * kb + 1] = cvtpk(__builtin_amdgcn_exp2f(s[kb][qb][2]), __builtin_amdgcn_exp2f(s[kb][qb][3]));
      }
    const unsigned por = (pu[0][0] | pu[0][1] | pu[0][2]) | (pu[0][3] | pu[1][0] | pu[1][1]) | (pu[1][2] | pu[1][3] | pu[2][0]) | (pu[2][1] | pu[2][2] | pu[2][3]);
    if (j == 0 || __any((por & 0x40004000u) != 0u)) {
#pragma unroll
      for (int qb = 0; qb < 3; ++qb) {
        float m = max3f(max3f(s[0][qb][0], s[0][qb][1], s[0][qb][2]), max3f(s[0][qb][3], s[1][qb][0], s[1][qb][1]), max2f(s[1][qb][2], s[1][qb][3]));
        m = fmaxf(m, __shfl_xor(m, 16)); m = fmaxf(m, __shfl_xor(m, 32));
        m += PBIAS;
        const float dl = (j == 0) ? m : fmaxf(m, 0.f);
        negm[qb] -= dl;
#pragma unroll
        for (int kb = 0; kb < 2; ++kb) {
          s[kb][qb] -= dl;
          pu[qb][2 * kb]     = cvtpk(__builtin_amdgcn_exp2f(s[kb][qb][0]), __builtin_amdgcn_exp2f(s[kb][qb][1]));
          pu[qb][2 * kb + 1] = cvtpk(__builtin_amdgcn_exp2f(s[kb][qb][2]), __builtin_amdgcn_exp2f(s[kb][qb][3]));
        }
        if (j != 0) {
          const float f = __builtin_amdgcn_exp2f(-dl);
          lacc[qb] *= f;
#pragma unroll
          for (int db = 0; db < 8; ++db) oacc[db][qb] *= f;
        }
      }
    }
    FS(4);
    FS(5);
    {
      const unsigned k0 = opq(krd0 + nslot), k1 = opq(krd1 + nslot);
      SBAR();
      PRIO(1);
#pragma unroll
      for (int db = 0; db < 8; ++db)
#pragma unroll
        for (int qb = 0; qb < 3; ++qb) oacc[db][qb] = mfma16(vf[db], __builtin_bit_cast(bf16x8, pu[qb]), oacc[db][qb]);
#pragma unroll
      for (int qb = 0; qb < 3; ++qb) lacc[qb] = mfma16(ones, __builtin_bit_cast(bf16x8, pu[qb]), lacc[qb]);
#pragma unroll
      for (int i = 0; i < 8; ++i) kf[i] = lds_b128(((i & 1) ? k1 : k0) + 4096u * (i >> 2) + 1024u * ((i >> 1) & 1));
#pragma unroll
      for (int i = 0; i < 8; ++i) { SGB(0x8, 3); SGB(0x100, 1); }
      SGB(0x8, 3);
    }
    SBAR();
    PRIO(0);
    FS(6);
    fslot = slot;
    slot = nslot;
  }

  STAMP(Ows, 0, 3); CSTAMP(Ows, 0, 3);
  {
    const unsigned head = 2u * hp + hh;
    char* ob = Ows + ((size_t)((br * NB + b) * 12u + 3u * rg) * 128u + head * 4u) * 1024u + 16u * l;
#pragma unroll
    for (int qb = 0; qb < 3; ++qb) {
      const float inv = 1.0f / lacc[qb][0];
#pragma unroll
      for (int s2 = 0; s2 < 4; ++s2) {
        const f32x4 a = oacc[2 * s2][qb], bb = oacc[2 * s2 + 1][qb];
        u32x4 u = {cvtpk(a[0] * inv, a[1] * inv), cvtpk(a[2] * inv, a[3] * inv), cvtpk(bb[0] * inv, bb[1] * inv), cvtpk(bb[2] * inv, bb[3] * inv)};
        asm volatile("global_store_dwordx4 %0, %1, off sc1\n\ts_nop 1" :: "v"(ob + (size_t)qb * (128u * 1024u) + s2 * 1024u), "v"(u) : "memory");
      }
    }
  }
  STAMP(Ows, 0, 4); CSTAMP(Ows, 0, 4);
}

#define K2ROT (mt & 3u)
constexpr unsigned K2STG = 5120u;

DI void gload16(u32x4& dst, unsigned voff, __amdgpu_buffer_rsrc_t srd, unsigned soff) {
  asm volatile("buffer_load_dwordx4 %0, %1, %2, %3 offen" : "=v"(dst) : "v"(voff), "s"(srd), "s"(soff) : "memory");
}

struct K2Ctx {
  __amdgpu_buffer_rsrc_t srdO, srdW;
  unsigned voffO, voffW[5], ldsw, wrd, kbase, rot;
};

DI void k2issue(const K2Ctx& c, u32x4 (&of)[6], int step, int buf) {
  const unsigned ks = c.kbase + (((unsigned)step + c.rot) & 15u);
#pragma unroll
  for (int i = 0; i < 5; ++i) dma16(c.ldsw + (unsigned)buf * K2STG + 1024u * i, c.voffW[i], c.srdW, ks * (32u * 1280u));
#pragma unroll
  for (int i = 0; i < 6; ++i) gload16(of[i], c.voffO, c.srdO, ks * 1024u + (unsigned)i * 131072u);
}
template <int N> DI void k2wait(u32x4 (&of)[6]) {
  asm volatile("s_waitcnt vmcnt(%6)" : "+v"(of[0]), "+v"(of[1]), "+v"(of[2]), "+v"(of[3]), "+v"(of[4]), "+v"(of[5]) : "i"(N) : "memory");
}
DI void k2comp(const K2Ctx& c, f32x4 (&acc)[5][6], u32x4 (&of)[6], int buf) {
  const unsigned a = c.wrd + (unsigned)buf * K2STG;
  bf16x8 wf[5];
#pragma unroll
  for (int nb = 0; nb < 5; ++nb) wf[nb] = lds_tr2(a + 32u * nb, a + 32u * nb + 2560u);
  SBAR();
#pragma unroll
  for (int nb = 0; nb < 5; ++nb)
#pragma unroll
    for (int qb = 0; qb < 6; ++qb) acc[nb][qb] = mfma16(wf[nb], __builtin_bit_cast(bf16x8, of[qb]), acc[nb][qb]);
  SBAR();
}

__global__ __launch_bounds__(512, 2)
void oproj_kernel(const char* __restrict__ Ows, const char* __restrict__ woA, const char* __restrict__ woB, char* __restrict__ out) {
  __shared__ __attribute__((aligned(1024))) char lds[4 * 30720];
  const unsigned tid = threadIdx.x, l = tid & 63u;
  const unsigned w = rfl(tid >> 6);
  const unsigned bid = blockIdx.x, xcd = bid & 7u, idx = bid >> 3;
  const unsigned br = xcd >> 2, mt = (xcd & 3u) * 4u + (idx >> 3), nt = idx & 7u;
  const unsigned g = l >> 4, q4 = (l >> 2) & 3u, p4 = l & 3u;
  const unsigned ldsb = (unsigned)(size_t)(LAS char*)lds;

  STAMP(Ows, 1, 0); CSTAMP(Ows, 1, 0);
  K2Ctx c;
  {
    const char* op = Ows + (size_t)(br * 96u + mt * 6u) * 131072u;
    const char* wp = (br ? woB : woA) + (size_t)nt * 160u;
    c.srdO = __builtin_amdgcn_make_buffer_rsrc((void*)op, 0, 6 * 131072, SRD3);
    c.srdW = __builtin_amdgcn_make_buffer_rsrc((void*)wp, 0, HD * DM * 2 - (int)nt * 160, SRD3);
  }
  c.voffO = 16u * l;
#pragma unroll
  for (int i = 0; i < 5; ++i) { const unsigned ci = 64u * i + l, row = ci / 10u, ch = ci - row * 10u; c.voffW[i] = row * 1280u + ch * 16u; }
  c.ldsw = ldsb + w * (3u * K2STG);
  c.wrd = c.ldsw + (4u * g + q4) * 160u + 8u * p4;
  c.kbase = w * 16u;
  c.rot = K2ROT;

  f32x4 acc[5][6];
#pragma unroll
  for (int i = 0; i < 5; ++i)
#pragma unroll
    for (int q = 0; q < 6; ++q) acc[i][q] = f32x4{0.f, 0.f, 0.f, 0.f};

  u32x4 o0[6], o1[6], o2[6];
  k2issue(c, o0, 0, 0); k2issue(c, o1, 1, 1);
  for (int s = 0; s < 12; s += 3) {
    k2issue(c, o2, s + 2, 2); k2wait<22>(o0); k2comp(c, acc, o0, 0);
    k2issue(c, o0, s + 3, 0); k2wait<22>(o1); k2comp(c, acc, o1, 1);
    k2issue(c, o1, s + 4, 1); k2wait<22>(o2); k2comp(c, acc, o2, 2);
  }
  k2issue(c, o2, 14, 2); k2wait<22>(o0); k2comp(c, acc, o0, 0);
  k2issue(c, o0, 15, 0); k2wait<22>(o1); k2comp(c, acc, o1, 1);
  k2wait<11>(o2); k2comp(c, acc, o2, 2);
  k2wait<0>(o0); k2comp(c, acc, o0, 0);

  STAMP(Ows, 1, 1); CSTAMP(Ows, 1, 1);
  __syncthreads();
  LAS char* base = (LAS char*)(size_t)(ldsb + (w & 3u) * 30720u + ((l & 15u) * 80u + 4u * g) * 4u);
  if (w >= 4u) {
#pragma unroll
    for (int qb = 0; qb < 6; ++qb)
#pragma unroll
      for (int nb = 0; nb < 5; ++nb) *(LAS f32x4*)(base + (16u * qb * 80u + 16u * nb) * 4u) = acc[nb][qb];
  }
  __syncthreads();
  if (w < 4u) {
#pragma unroll
    for (int qb = 0; qb < 6; ++qb)
#pragma unroll
      for (int nb = 0; nb < 5; ++nb) {
        LAS f32x4* p = (LAS f32x4*)(base + (16u * qb * 80u + 16u * nb) * 4u);
        *p = *p + acc[nb][qb];
      }
  }
  __syncthreads();
  for (unsigned it = tid; it < 960u; it += 512u) {
    const unsigned q = it / 10u, ch = it - q * 10u;
    const LAS char* src = (const LAS char*)(size_t)(ldsb + (q * 80u + ch * 8u) * 4u);
    f32x4 s0 = *(const LAS f32x4*)src, s1 = *(const LAS f32x4*)(src + 16);
#pragma unroll
    for (int ww = 1; ww < 4; ++ww) { s0 += *(const LAS f32x4*)(src + ww * 30720); s1 += *(const LAS f32x4*)(src + ww * 30720 + 16); }
    u32x4 u = {cvtpk(s0[0], s0[1]), cvtpk(s0[2], s0[3]), cvtpk(s1[0], s1[1]), cvtpk(s1[2], s1[3])};
    *(u32x4*)(out + ((size_t)(mt * 96u + q) * 1280u + br * 640u + nt * 80u + ch * 8u) * 2u) = u;
  }
  STAMP(Ows, 1, 2); CSTAMP(Ows, 1, 2);
}

extern "C" void kernel_launch(void* const* d_in, const int* in_sizes, int n_in,
                              void* d_out, int out_size, void* d_ws, size_t ws_size,
                              hipStream_t stream) {
  const char* xA = (const char*)d_in[0]; const char* kA = (const char*)d_in[1]; const char* vA = (const char*)d_in[2];
  const char* wqA = (const char*)d_in[3]; const char* woA = (const char*)d_in[4];
  const char* xB = (const char*)d_in[5]; const char* kB = (const char*)d_in[6]; const char* vB = (const char*)d_in[7];
  const char* wqB = (const char*)d_in[8]; const char* woB = (const char*)d_in[9];
  char* Ows = (char*)d_ws;
  attn_qproj_kernel<<<256, 512, 0, stream>>>(xA, kA, vA, wqA, xB, kB, vB, wqB, Ows);
  oproj_kernel<<<256, 512, 0, stream>>>(Ows, woA, woB, (char*)d_out);
}
```

```cpp
#include <hip/hip_runtime.h>
#include <hip/hip_bf16.h>

typedef __bf16   bf16x2 __attribute__((ext_vector_type(2)));
typedef float    f32x4  __attribute__((ext_vector_type(4)));
typedef float    f32x2  __attribute__((ext_vector_type(2)));
typedef unsigned u32x4  __attribute__((ext_vector_type(4)));
typedef unsigned u32x2  __attribute__((ext_vector_type(2)));
typedef unsigned u32x8  __attribute__((ext_vector_type(8)));
typedef float    f32x32 __attribute__((ext_vector_type(32)));
#define LAS __attribute__((address_space(3)))
#define DI __device__ __forceinline__
#define SRD3 0x00020000

constexpr int NB = 8, SQ = 192, DM = 640, NH = 32, SK = 512, DH = 128, HD = NH * DH;

DI unsigned rfl(unsigned x) { return __builtin_amdgcn_readfirstlane(x); }
DI unsigned cvtpk(float lo, float hi) {
  f32x2 v = {lo, hi}; bf16x2 b = __builtin_convertvector(v, bf16x2); return __builtin_bit_cast(unsigned, b);
}

DI void setm0(unsigned m0v) { asm volatile("s_mov_b32 m0, %0\n\ts_nop 0" :: "s"(m0v) : "memory"); }
template <int OFF, bool NT> DI void dma(unsigned voff, __amdgpu_buffer_rsrc_t srd, unsigned soff) {
  if constexpr (NT) asm volatile("buffer_load_dwordx4 %0, %1, %2 offen offset:%3 nt lds" :: "v"(voff), "s"(srd), "s"(soff), "i"(OFF) : "memory");
  else              asm volatile("buffer_load_dwordx4 %0, %1, %2 offen offset:%3 lds" :: "v"(voff), "s"(srd), "s"(soff), "i"(OFF) : "memory");
}
DI void barrier() { asm volatile("s_waitcnt lgkmcnt(0)\n\ts_barrier" ::: "memory"); }
template <int N> DI void wait_bar() { asm volatile("s_waitcnt vmcnt(%0) lgkmcnt(0)\n\ts_barrier" :: "i"(N) : "memory"); }

#define SBAR() __builtin_amdgcn_sched_barrier(0)

constexpr size_t WS_O = 32ul << 20, WS_MB = 0, WS_PX = 1ul << 20;
DI unsigned peer_bid(unsigned bid, unsigned k) {
  return k < 4u ? ((bid & ~24u) | (k << 3)) : ((bid & ~3u) | (k - 4u));
}
DI unsigned mb_load(const char* ws, unsigned p, unsigned word) {
  unsigned r;
  asm volatile("global_load_dword %0, %1, off sc1" : "=v"(r) : "v"(ws + WS_MB + p * 128u + word * 4u) : "memory");
  return r;
}
DI void mb_store(char* ws, unsigned p, unsigned word, unsigned v) {
  asm volatile("global_store_dword %0, %1, off sc1" :: "v"(ws + WS_MB + p * 128u + word * 4u), "v"(v) : "memory");
}
DI void store16_sc1(char* p, u32x4 u) { asm volatile("global_store_dwordx4 %0, %1, off sc1\n\ts_nop 1" :: "v"(p), "v"(u) : "memory"); }
constexpr unsigned SPIN_CAP = 1u << 18;
template <bool ECHO>
DI void handoff_wait(char* ws, unsigned bid, unsigned G, unsigned myslot, unsigned nonce, unsigned l, unsigned& echoed) {
  const unsigned p = peer_bid(bid, 4u * G + (l & 3u));
  for (unsigned spin = 0; spin < SPIN_CAP; ++spin) {
    unsigned a = mb_load(ws, p, 4u + 4u * G + myslot), n = 0u;
    if (ECHO) n = mb_load(ws, p, 0u);
    asm volatile("s_waitcnt vmcnt(0)" : "+v"(a), "+v"(n) :: "memory");
    if (ECHO) {
      if (__any(n != echoed)) { echoed = n; if (l < 4u) mb_store(ws, bid, 4u + 4u * G + l, (n << 8) | 1u); }
    }
    if (__all(a == ((nonce << 8) | 1u) || p == bid)) break;
    __builtin_amdgcn_s_sleep(2);
  }
}

#define KVROT (((bid >> 3) + (bid >> 7) * 5u + xcd * 3u) & 15u)
constexpr unsigned XBASE = 32768u;
constexpr unsigned P3O = 65536u, P3S = 12288u;

__global__ __launch_bounds__(512, 2)
void fused_attn_kernel(const char* __restrict__ xA, const char* __restrict__ kA, const char* __restrict__ vA, const char* __restrict__ wqA,
                       const char* __restrict__ woA,
                       const char* __restrict__ xB, const char* __restrict__ kB, const char* __restrict__ vB, const char* __restrict__ wqB,
                       const char* __restrict__ woB, char* __restrict__ out, char* Ows) {
  __shared__ __attribute__((aligned(1024))) char lds[163840];
  const unsigned tid = threadIdx.x, l = tid & 63u;
  const unsigned w = rfl(tid >> 6);
  const unsigned bid = blockIdx.x, xcd = bid & 7u, idx = bid >> 3;
  const unsigned br = xcd >> 2, hg = xcd & 3u, jq = idx & 3u, hp = hg * 4u + jq, b = idx >> 2;
  const unsigned hh = w >> 2, rg = w & 3u;
  const unsigned g = l >> 4, r16 = l & 15u, q4 = (l >> 2) & 3u, p4 = l & 3u;
  const unsigned ldsb = (unsigned)(size_t)(LAS char*)lds;
  const __amdgpu_buffer_rsrc_t srdX = __builtin_amdgcn_make_buffer_rsrc((void*)((br ? xB : xA) + (size_t)b * (SQ * DM * 2)), 0, SQ * DM * 2, SRD3);
  const __amdgpu_buffer_rsrc_t srdW = __builtin_amdgcn_make_buffer_rsrc((void*)((br ? wqB : wqA) + (size_t)hp * 512), 0, DM * HD * 2 - (int)hp * 512, SRD3);
  const unsigned voffW = (8u * (w >> 2) + 4u * (l >> 5) + q4) * 8192u + (2u * (w & 3u) + ((l >> 4) & 1u)) * 64u + 16u * (p4 ^ ((w >> 2) & 1u));
  const unsigned voffX = (l >> 3) * 1280u + 16u * ((l & 7u) ^ (4u * (w & 1u) + (l >> 4)));
  setm0(ldsb + 81920u + 2048u * w);
  dma<0, false>(voffW, srdW, 0u); dma<1024, false>(voffW, srdW, 131072u - 1024u);
  setm0(ldsb + XBASE + 3072u * w);
  dma<0, false>(voffX, srdX, w * 30720u); dma<1024, false>(voffX ^ 64u, srdX, w * 30720u + 9216u); dma<2048, false>(voffX, srdX, w * 30720u + 18432u);
  SBAR();
  const __amdgpu_buffer_rsrc_t srdM = __builtin_amdgcn_make_buffer_rsrc((void*)(Ows + WS_MB), 0, (1 << 20) + 256 * 65536, SRD3);
  const __amdgpu_buffer_rsrc_t srdWo = __builtin_amdgcn_make_buffer_rsrc((void*)((br ? woB : woA) + (size_t)hg * (1024u * 1280u) + 320u * jq), 0, 1024 * 1280 - 320 * (int)jq, SRD3);
  char* ogrp = Ows + WS_O + (size_t)((br * NB + b) * 4u + hg) * (32u * P3S);
  const __amdgpu_buffer_rsrc_t srdO = __builtin_amdgcn_make_buffer_rsrc((void*)ogrp, 0, 32 * P3S, SRD3);
  unsigned nonce, ln16;
  {
    const char* kp = (br ? kB : kA) + (size_t)(b * NH + 2 * hp) * (SK * DH * 2);
    const char* vp = (br ? vB : vA) + (size_t)(b * NH + 2 * hp) * (SK * DH * 2);
    const __amdgpu_buffer_rsrc_t srdK = __builtin_amdgcn_make_buffer_rsrc((void*)kp, 0, 2 * SK * DH * 2, SRD3);
    const __amdgpu_buffer_rsrc_t srdV = __builtin_amdgcn_make_buffer_rsrc((void*)(vp - 2048), 0, 2 * SK * DH * 2 + 2048, SRD3);
    u32x8 qa, ka;
    qa[0] = ldsb + 81920u + 8192u * (g & 1u) + 4096u * hh + 1024u * (g >> 1) + 64u * q4 + 16u * (p4 ^ (g & 1u)) + 8u * (p4 & 1u);
    qa[1] = qa[0] ^ 8u;
    qa[2] = ldsb + XBASE + 1024u * (6u * rg + (r16 >> 3)) + 128u * (r16 & 7u) + 16u * (g ^ (r16 >> 1));
    qa[3] = qa[2] ^ 64u;
    qa[4] = voffW;
    qa[5] = voffX;
    qa[6] = qa[5] ^ 64u;
    qa[7] = ldsb + XBASE + 4096u * w + 16u * l;
    ka[0] = ldsb + 8192u * (r16 >> 3) + 1024u * hh + 128u * (r16 & 7u) + 16u * (g ^ (r16 >> 1));
    ka[1] = ldsb + 8192u * (g >> 1) + 2048u + 1024u * hh + 64u * (4u * (g & 1u) + q4) + 16u * ((p4 >> 1) ^ ((g & 1u) << 1)) + 8u * (p4 & 1u);
    ka[2] = l * 3u;
    ka[3] = peer_bid(bid, l & 7u) * 128u + 4u * ((l & 4u) ? 8u + hg : 4u + jq);
    ka[4] = tid == 0u ? bid * 128u : 0x40000000u;
    ka[5] = (8u + l) & 31u;
    ka[6] = (8u * (w >> 1) + (l >> 3)) * 256u + (w & 1u) * 128u + 16u * ((l & 7u) ^ ((4u * ((w >> 1) & 1u) + (l >> 4)) & 7u));
    ka[7] = (8u * (w >> 1) + ((l >> 2) & 7u)) * 256u + (2u * (w & 1u) + (l >> 5)) * 64u + 16u * (p4 ^ (((l >> 4) & 1u) << 1));
    unsigned vtch = l < 16u ? w * 1024u + (l & 7u) * 128u + (l >> 3) * 131072u : 0x40000000u;
    const unsigned pr0 = w < 4u ? 4u * jq + 3u + 2u * b : 4u * jq;
    unsigned wm0 = w < 4u ? ldsb + 61440u + 1024u * rg : ldsb + 5120u * rg;
    const unsigned ws0 = (pr0 & 15u) * 81920u + rg * 20480u, ws1 = ((pr0 + 1u) & 15u) * 81920u + rg * 20480u, ws2 = ((pr0 + 2u) & 15u) * 81920u + rg * 20480u;
    asm volatile(
    "s_nop 3\n"
    "buffer_load_dword v242, v243, s[76:79], 0 offen sc1\n"
    "s_add_u32 m0, s57, 98304\n"
    "s_mov_b32 s60, 262144\n"
    "buffer_load_dwordx4 v140, s[40:43], s60 offen offset:0 lds\n"
    "s_mov_b32 s60, 392192\n"
    "buffer_load_dwordx4 v140, s[40:43], s60 offen offset:1024 lds\n"
    "s_add_u32 m0, s57, 114688\n"
    "s_mov_b32 s60, 524288\n"
    "buffer_load_dwordx4 v140, s[40:43], s60 offen offset:0 lds\n"
    "s_mov_b32 s60, 654336\n"
    "buffer_load_dwordx4 v140, s[40:43], s60 offen offset:1024 lds\n"
    "s_add_u32 m0, s58, 57344\n"
    "s_add_u32 s60, s59, 128\n"
    "buffer_load_dwordx4 v141, s[36:39], s60 offen offset:0 lds\n"
    "s_add_u32 s60, s59, 9344\n"
    "buffer_load_dwordx4 v142, s[36:39], s60 offen offset:1024 lds\n"
    "s_add_u32 s60, s59, 18560\n"
    "buffer_load_dwordx4 v141, s[36:39], s60 offen offset:2048 lds\n"
    "s_mov_b32 s74, 0xffff0000\n"
    "s_mov_b32 s75, 0xffff0000\n"
    "s_mov_b32 s63, 0x40004000\n"
    "s_waitcnt vmcnt(8)\n"
    "s_waitcnt vmcnt(8) lgkmcnt(0)\n"
    "s_barrier\n"
    "ds_read_b128 v[176:179], v138 offset:0\n"
    "ds_read_b128 v[180:183], v138 offset:2048\n"
    "ds_read_b128 v[184:187], v138 offset:4096\n"
    "ds_read_b64_tr_b16 v[144:145], v136 offset:0\n"
    "ds_read_b64_tr_b16 v[146:147], v136 offset:512\n"
    "ds_read_b64_tr_b16 v[148:149], v137 offset:0\n"
    "ds_read_b64_tr_b16 v[150:151], v137 offset:512\n"
    "ds_read_b64_tr_b16 v[152:153], v136 offset:256\n"
    "ds_read_b64_tr_b16 v[154:155], v136 offset:768\n"
    "ds_read_b64_tr_b16 v[156:157], v137 offset:256\n"
    "ds_read_b64_tr_b16 v[158:159], v137 offset:768\n"
    "ds_read_b64_tr_b16 v[160:161], v136 offset:2048\n"
    "ds_read_b64_tr_b16 v[162:163], v136 offset:2560\n"
    "ds_read_b64_tr_b16 v[164:165], v137 offset:2048\n"
    "ds_read_b64_tr_b16 v[166:167], v137 offset:2560\n"
    "ds_read_b64_tr_b16 v[168:169], v136 offset:2304\n"
    "ds_read_b64_tr_b16 v[170:171], v136 offset:2816\n"
    "ds_read_b64_tr_b16 v[172:173], v137 offset:2304\n"
    "ds_read_b64_tr_b16 v[174:175], v137 offset:2816\n"
    "s_waitcnt lgkmcnt(14)\n"
    "v_mfma_f32_16x16x32_bf16 v[0:3], v[144:147], v[176:179], 0\n"
    "v_mfma_f32_16x16x32_bf16 v[4:7], v[144:147], v[180:183], 0\n"
    "v_mfma_f32_16x16x32_bf16 v[8:11], v[144:147], v[184:187], 0\n"
    "s_waitcnt lgkmcnt(12)\n"
    "v_mfma_f32_16x16x32_bf16 v[12:15], v[148:151], v[176:179], 0\n"
    "s_sleep 3\n"
    "v_mfma_f32_16x16x32_bf16 v[16:19], v[148:151], v[180:183], 0\n"
    "v_mfma_f32_16x16x32_bf16 v[20:23], v[148:151], v[184:187], 0\n"
    "s_waitcnt lgkmcnt(10)\n"
    "v_mfma_f32_16x16x32_bf16 v[24:27], v[152:155], v[176:179], 0\n"
    "v_mfma_f32_16x16x32_bf16 v[28:31], v[152:155], v[180:183], 0\n"
    "v_mfma_f32_16x16x32_bf16 v[32:35], v[152:155], v[184:187], 0\n"
    "s_waitcnt lgkmcnt(8)\n"
    "v_mfma_f32_16x16x32_bf16 v[36:39], v[156:159], v[176:179], 0\n"
    "s_sleep 3\n"
    "v_mfma_f32_16x16x32_bf16 v[40:43], v[156:159], v[180:183], 0\n"
    "v_mfma_f32_16x16x32_bf16 v[44:47], v[156:159], v[184:187], 0\n"
    "s_waitcnt vmcnt(5) lgkmcnt(0)\n"
    "s_barrier\n"
    "ds_read_b128 v[188:191], v139 offset:0\n"
    "v_mfma_f32_16x16x32_bf16 v[48:51], v[160:163], v[176:179], 0\n"
    "ds_read_b128 v[192:195], v139 offset:2048\n"
    "ds_read_b64_tr_b16 v[144:145], v136 offset:16384\n"
    "ds_read_b64_tr_b16 v[146:147], v136 offset:16896\n"
    "s_add_u32 m0, s57, 81920\n"
    "s_mov_b32 s60, 786432\n"
    "buffer_load_dwordx4 v140, s[40:43], s60 offen offset:0 lds\n"
    "s_mov_b32 s60, 916480\n"
    "v_mfma_f32_16x16x32_bf16 v[52:55], v[160:163], v[180:183], 0\n"
    "ds_read_b128 v[196:199], v139 offset:4096\n"
    "ds_read_b64_tr_b16 v[148:149], v137 offset:16384\n"
    "ds_read_b64_tr_b16 v[150:151], v137 offset:16896\n"
    "v_mfma_f32_16x16x32_bf16 v[56:59], v[160:163], v[184:187], 0\n"
    "ds_read_b64_tr_b16 v[160:161], v136 offset:18432\n"
    "ds_read_b64_tr_b16 v[162:163], v136 offset:18944\n"
    "v_mfma_f32_16x16x32_bf16 v[60:63], v[164:167], v[176:179], 0\n"
    "s_sleep 3\n"
    "ds_read_b64_tr_b16 v[152:153], v136 offset:16640\n"
    "ds_read_b64_tr_b16 v[154:155], v136 offset:17152\n"
    "buffer_load_dwordx4 v140, s[40:43], s60 offen offset:1024 lds\n"
    "v_mfma_f32_16x16x32_bf16 v[64:67], v[164:167], v[180:183], 0\n"
    "ds_read_b64_tr_b16 v[156:157], v137 offset:16640\n"
    "ds_read_b64_tr_b16 v[158:159], v137 offset:17152\n"
    "v_mfma_f32_16x16x32_bf16 v[68:71], v[164:167], v[184:187], 0\n"
    "ds_read_b64_tr_b16 v[164:165], v137 offset:18432\n"
    "ds_read_b64_tr_b16 v[166:167], v137 offset:18944\n"
    "v_mfma_f32_16x16x32_bf16 v[72:75], v[168:171], v[176:179], 0\n"
    "s_add_u32 m0, s56, 131072\n"
    "s_add_u32 s61, s65, 0\n"
    "s_and_b32 s61, s61, 0x1ffff\n"
    "buffer_load_dwordx4 v246, s[44:47], s61 offen offset:0 nt lds\n"
    "v_mfma_f32_16x16x32_bf16 v[76:79], v[168:171], v[180:183], 0\n"
    "v_mfma_f32_16x16x32_bf16 v[80:83], v[168:171], v[184:187], 0\n"
    "v_lshrrev_b32_e32 v242, v245, v242\n"
    "v_and_b32_e32 v242, 1, v242\n"
    "v_cmp_eq_u32_e32 vcc, 0, v242\n"
    "s_nop 3\n"
    "s_and_b32 s80, vcc_lo, 0xff\n"
    "v_mov_b32_e32 v242, s80\n"
    "buffer_store_dword v242, v244, s[76:79], 0 offen sc1\n"
    "ds_read_b64_tr_b16 v[168:169], v136 offset:18688\n"
    "ds_read_b64_tr_b16 v[170:171], v136 offset:19200\n"
    "v_mfma_f32_16x16x32_bf16 v[84:87], v[172:175], v[176:179], 0\n"
    "s_sleep 3\n"
    "v_mfma_f32_16x16x32_bf16 v[88:91], v[172:175], v[180:183], 0\n"
    "v_mfma_f32_16x16x32_bf16 v[92:95], v[172:175], v[184:187], 0\n"
    "ds_read_b64_tr_b16 v[172:173], v137 offset:18688\n"
    "ds_read_b64_tr_b16 v[174:175], v137 offset:19200\n"
    "s_waitcnt lgkmcnt(14)\n"
    "v_mfma_f32_16x16x32_bf16 v[0:3], v[144:147], v[188:191], v[0:3]\n"
    "v_mfma_f32_16x16x32_bf16 v[4:7], v[144:147], v[192:195], v[4:7]\n"
    "v_mfma_f32_16x16x32_bf16 v[8:11], v[144:147], v[196:199], v[8:11]\n"
    "s_waitcnt lgkmcnt(12)\n"
    "v_mfma_f32_16x16x32_bf16 v[12:15], v[148:151], v[188:191], v[12:15]\n"
    "s_sleep 2\n"
    "v_mfma_f32_16x16x32_bf16 v[16:19], v[148:151], v[192:195], v[16:19]\n"
    "v_mfma_f32_16x16x32_bf16 v[20:23], v[148:151], v[196:199], v[20:23]\n"
    "s_waitcnt lgkmcnt(8)\n"
    "v_mfma_f32_16x16x32_bf16 v[24:27], v[152:155], v[188:191], v[24:27]\n"
    "v_mfma_f32_16x16x32_bf16 v[28:31], v[152:155], v[192:195], v[28:31]\n"
    "v_mfma_f32_16x16x32_bf16 v[32:35], v[152:155], v[196:199], v[32:35]\n"
    "s_waitcnt lgkmcnt(6)\n"
    "v_mfma_f32_16x16x32_bf16 v[36:39], v[156:159], v[188:191], v[36:39]\n"
    "s_sleep 2\n"
    "v_mfma_f32_16x16x32_bf16 v[40:43], v[156:159], v[192:195], v[40:43]\n"
    "v_mfma_f32_16x16x32_bf16 v[44:47], v[156:159], v[196:199], v[44:47]\n"
    "s_waitcnt vmcnt(4) lgkmcnt(0)\n"
    "s_barrier\n"
    "ds_read_b128 v[176:179], v138 offset:24576\n"
    "v_mfma_f32_16x16x32_bf16 v[48:51], v[160:163], v[188:191], v[48:51]\n"
    "ds_read_b128 v[180:183], v138 offset:26624\n"
    "ds_read_b64_tr_b16 v[144:145], v136 offset:32768\n"
    "ds_read_b64_tr_b16 v[146:147], v136 offset:33280\n"
    "s_add_u32 m0, s57, 98304\n"
    "s_mov_b32 s60, 1048576\n"
    "buffer_load_dwordx4 v140, s[40:43], s60 offen offset:0 lds\n"
    "s_mov_b32 s60, 1178624\n"
    "v_mfma_f32_16x16x32_bf16 v[52:55], v[160:163], v[192:195], v[52:55]\n"
    "ds_read_b128 v[184:187], v138 offset:28672\n"
    "ds_read_b64_tr_b16 v[148:149], v137 offset:32768\n"
    "ds_read_b64_tr_b16 v[150:151], v137 offset:33280\n"
    "v_mfma_f32_16x16x32_bf16 v[56:59], v[160:163], v[196:199], v[56:59]\n"
    "ds_read_b64_tr_b16 v[160:161], v136 offset:34816\n"
    "ds_read_b64_tr_b16 v[162:163], v136 offset:35328\n"
    "v_mfma_f32_16x16x32_bf16 v[60:63], v[164:167], v[188:191], v[60:63]\n"
    "s_sleep 2\n"
    "ds_read_b64_tr_b16 v[152:153], v136 offset:33024\n"
    "ds_read_b64_tr_b16 v[154:155], v136 offset:33536\n"
    "buffer_load_dwordx4 v140, s[40:43], s60 offen offset:1024 lds\n"
    "v_mfma_f32_16x16x32_bf16 v[64:67], v[164:167], v[192:195], v[64:67]\n"
    "ds_read_b64_tr_b16 v[156:157], v137 offset:33024\n"
    "ds_read_b64_tr_b16 v[158:159], v137 offset:33536\n"
    "v_mfma_f32_16x16x32_bf16 v[68:71], v[164:167], v[196:199], v[68:71]\n"
    "ds_read_b64_tr_b16 v[164:165], v137 offset:34816\n"
    "ds_read_b64_tr_b16 v[166:167], v137 offset:35328\n"
    "v_mfma_f32_16x16x32_bf16 v[72:75], v[168:171], v[188:191], v[72:75]\n"
    "s_add_u32 m0, s58, 32768\n"
    "s_add_u32 s60, s59, 256\n"
    "buffer_load_dwordx4 v141, s[36:39], s60 offen offset:0 lds\n"
    "s_add_u32 s60, s59, 9472\n"
    "v_mfma_f32_16x16x32_bf16 v[76:79], v[168:171], v[192:195], v[76:79]\n"
    "v_mfma_f32_16x16x32_bf16 v[80:83], v[168:171], v[196:199], v[80:83]\n"
    "ds_read_b64_tr_b16 v[168:169], v136 offset:35072\n"
    "ds_read_b64_tr_b16 v[170:171], v136 offset:35584\n"
    "v_mfma_f32_16x16x32_bf16 v[84:87], v[172:175], v[188:191], v[84:87]\n"
    "s_sleep 2\n"
    "buffer_load_dwordx4 v142, s[36:39], s60 offen offset:1024 lds\n"
    "s_add_u32 s60, s59, 18688\n"
    "v_mfma_f32_16x16x32_bf16 v[88:91], v[172:175], v[192:195], v[88:91]\n"
    "v_mfma_f32_16x16x32_bf16 v[92:95], v[172:175], v[196:199], v[92:95]\n"
    "ds_read_b64_tr_b16 v[172:173], v137 offset:35072\n"
    "ds_read_b64_tr_b16 v[174:175], v137 offset:35584\n"
    "s_waitcnt lgkmcnt(14)\n"
    "v_mfma_f32_16x16x32_bf16 v[0:3], v[144:147], v[176:179], v[0:3]\n"
    "buffer_load_dwordx4 v141, s[36:39], s60 offen offset:2048 lds\n"
    "v_mfma_f32_16x16x32_bf16 v[4:7], v[144:147], v[180:183], v[4:7]\n"
    "v_mfma_f32_16x16x32_bf16 v[8:11], v[144:147], v[184:187], v[8:11]\n"
    "s_waitcnt lgkmcnt(12)\n"
    "v_mfma_f32_16x16x32_bf16 v[12:15], v[148:151], v[176:179], v[12:15]\n"
    "s_sleep 2\n"
    "s_add_u32 m0, s56, 131072\n"
    "s_add_u32 s61, s65, 0\n"
    "s_and_b32 s61, s61, 0x1ffff\n"
    "s_add_u32 s61, s61, 130048\n"
    "buffer_load_dwordx4 v246, s[44:47], s61 offen offset:1024 nt lds\n"
    "v_mfma_f32_16x16x32_bf16 v[16:19], v[148:151], v[180:183], v[16:19]\n"
    "v_mfma_f32_16x16x32_bf16 v[20:23], v[148:151], v[184:187], v[20:23]\n"
    "s_waitcnt lgkmcnt(8)\n"
    "v_mfma_f32_16x16x32_bf16 v[24:27], v[152:155], v[176:179], v[24:27]\n"
    "v_mfma_f32_16x16x32_bf16 v[28:31], v[152:155], v[180:183], v[28:31]\n"
    "v_mfma_f32_16x16x32_bf16 v[32:35], v[152:155], v[184:187], v[32:35]\n"
    "s_waitcnt lgkmcnt(6)\n"
    "v_mfma_f32_16x16x32_bf16 v[36:39], v[156:159], v[176:179], v[36:39]\n"
    "s_sleep 2\n"
    "v_mfma_f32_16x16x32_bf16 v[40:43], v[156:159], v[180:183], v[40:43]\n"
    "v_mfma_f32_16x16x32_bf16 v[44:47], v[156:159], v[184:187], v[44:47]\n"
    "s_waitcnt vmcnt(8) lgkmcnt(0)\n"
    "s_barrier\n"
    "ds_read_b128 v[188:191], v139 offset:24576\n"
    "v_mfma_f32_16x16x32_bf16 v[48:51], v[160:163], v[176:179], v[48:51]\n"
    "ds_read_b128 v[192:195], v139 offset:26624\n"
    "ds_read_b64_tr_b16 v[144:145], v136 offset:0\n"
    "ds_read_b64_tr_b16 v[146:147], v136 offset:512\n"
    "s_add_u32 m0, s57, 114688\n"
    "s_mov_b32 s60, 1310720\n"
    "buffer_load_dwordx4 v140, s[40:43], s60 offen offset:0 lds\n"
    "s_mov_b32 s60, 1440768\n"
    "v_mfma_f32_16x16x32_bf16 v[52:55], v[160:163], v[180:183], v[52:55]\n"
    "ds_read_b128 v[196:199], v139 offset:28672\n"
    "ds_read_b64_tr_b16 v[148:149], v137 offset:0\n"
    "ds_read_b64_tr_b16 v[150:151], v137 offset:512\n"
    "v_mfma_f32_16x16x32_bf16 v[56:59], v[160:163], v[184:187], v[56:59]\n"
    "ds_read_b64_tr_b16 v[160:161], v136 offset:2048\n"
    "ds_read_b64_tr_b16 v[162:163], v136 offset:2560\n"
    "v_mfma_f32_16x16x32_bf16 v[60:63], v[164:167], v[176:179], v[60:63]\n"
    "s_sleep 2\n"
    "ds_read_b64_tr_b16 v[152:153], v136 offset:256\n"
    "ds_read_b64_tr_b16 v[154:155], v136 offset:768\n"
    "buffer_load_dwordx4 v140, s[40:43], s60 offen offset:1024 lds\n"
    "v_mfma_f32_16x16x32_bf16 v[64:67], v[164:167], v[180:183], v[64:67]\n"
    "ds_read_b64_tr_b16 v[156:157], v137 offset:256\n"
    "ds_read_b64_tr_b16 v[158:159], v137 offset:768\n"
    "v_mfma_f32_16x16x32_bf16 v[68:71], v[164:167], v[184:187], v[68:71]\n"
    "ds_read_b64_tr_b16 v[164:165], v137 offset:2048\n"
    "ds_read_b64_tr_b16 v[166:167], v137 offset:2560\n"
    "v_mfma_f32_16x16x32_bf16 v[72:75], v[168:171], v[176:179], v[72:75]\n"
    "s_add_u32 m0, s56, 131072\n"
    "s_add_u32 s61, s65, 0\n"
    "s_and_b32 s61, s61, 0x1ffff\n"
    "buffer_load_dwordx4 v247, s[48:51], s61 offen offset:2048 nt lds\n"
    "v_mfma_f32_16x16x32_bf16 v[76:79], v[168:171], v[180:183], v[76:79]\n"
    "v_mfma_f32_16x16x32_bf16 v[80:83], v[168:171], v[184:187], v[80:83]\n"
    "ds_read_b64_tr_b16 v[168:169], v136 offset:2304\n"
    "ds_read_b64_tr_b16 v[170:171], v136 offset:2816\n"
    "v_mfma_f32_16x16x32_bf16 v[84:87], v[172:175], v[176:179], v[84:87]\n"
    "s_sleep 2\n"
    "v_mfma_f32_16x16x32_bf16 v[88:91], v[172:175], v[180:183], v[88:91]\n"
    "v_mfma_f32_16x16x32_bf16 v[92:95], v[172:175], v[184:187], v[92:95]\n"
    "ds_read_b64_tr_b16 v[172:173], v137 offset:2304\n"
    "ds_read_b64_tr_b16 v[174:175], v137 offset:2816\n"
    "s_waitcnt lgkmcnt(14)\n"
    "v_mfma_f32_16x16x32_bf16 v[0:3], v[144:147], v[188:191], v[0:3]\n"
    "v_mfma_f32_16x16x32_bf16 v[4:7], v[144:147], v[192:195], v[4:7]\n"
    "v_mfma_f32_16x16x32_bf16 v[8:11], v[144:147], v[196:199], v[8:11]\n"
    "s_waitcnt lgkmcnt(12)\n"
    "v_mfma_f32_16x16x32_bf16 v[12:15], v[148:151], v[188:191], v[12:15]\n"
    "s_sleep 1\n"
    "v_mfma_f32_16x16x32_bf16 v[16:19], v[148:151], v[192:195], v[16:19]\n"
    "v_mfma_f32_16x16x32_bf16 v[20:23], v[148:151], v[196:199], v[20:23]\n"
    "s_waitcnt lgkmcnt(8)\n"
    "v_mfma_f32_16x16x32_bf16 v[24:27], v[152:155], v[188:191], v[24:27]\n"
    "v_mfma_f32_16x16x32_bf16 v[28:31], v[152:155], v[192:195], v[28:31]\n"
    "v_mfma_f32_16x16x32_bf16 v[32:35], v[152:155], v[196:199], v[32:35]\n"
    "s_waitcnt lgkmcnt(6)\n"
    "v_mfma_f32_16x16x32_bf16 v[36:39], v[156:159], v[188:191], v[36:39]\n"
    "s_sleep 1\n"
    "v_mfma_f32_16x16x32_bf16 v[40:43], v[156:159], v[192:195], v[40:43]\n"
    "v_mfma_f32_16x16x32_bf16 v[44:47], v[156:159], v[196:199], v[44:47]\n"
    "s_waitcnt vmcnt(4) lgkmcnt(0)\n"
    "s_barrier\n"
    "ds_read_b128 v[176:179], v138 offset:0\n"
    "v_mfma_f32_16x16x32_bf16 v[48:51], v[160:163], v[188:191], v[48:51]\n"
    "ds_read_b128 v[180:183], v138 offset:2048\n"
    "ds_read_b64_tr_b16 v[144:145], v136 offset:16384\n"
    "ds_read_b64_tr_b16 v[146:147], v136 offset:16896\n"
    "s_add_u32 m0, s57, 81920\n"
    "s_mov_b32 s60, 1572864\n"
    "buffer_load_dwordx4 v140, s[40:43], s60 offen offset:0 lds\n"
    "s_mov_b32 s60, 1702912\n"
    "v_mfma_f32_16x16x32_bf16 v[52:55], v[160:163], v[192:195], v[52:55]\n"
    "ds_read_b128 v[184:187], v138 offset:4096\n"
    "ds_read_b64_tr_b16 v[148:149], v137 offset:16384\n"
    "ds_read_b64_tr_b16 v[150:151], v137 offset:16896\n"
    "v_mfma_f32_16x16x32_bf16 v[56:59], v[160:163], v[196:199], v[56:59]\n"
    "ds_read_b64_tr_b16 v[160:161], v136 offset:18432\n"
    "ds_read_b64_tr_b16 v[162:163], v136 offset:18944\n"
    "v_mfma_f32_16x16x32_bf16 v[60:63], v[164:167], v[188:191], v[60:63]\n"
    "s_sleep 1\n"
    "ds_read_b64_tr_b16 v[152:153], v136 offset:16640\n"
    "ds_read_b64_tr_b16 v[154:155], v136 offset:17152\n"
    "buffer_load_dwordx4 v140, s[40:43], s60 offen offset:1024 lds\n"
    "v_mfma_f32_16x16x32_bf16 v[64:67], v[164:167], v[192:195], v[64:67]\n"
    "ds_read_b64_tr_b16 v[156:157], v137 offset:16640\n"
    "ds_read_b64_tr_b16 v[158:159], v137 offset:17152\n"
    "v_mfma_f32_16x16x32_bf16 v[68:71], v[164:167], v[196:199], v[68:71]\n"
    "ds_read_b64_tr_b16 v[164:165], v137 offset:18432\n"
    "ds_read_b64_tr_b16 v[166:167], v137 offset:18944\n"
    "v_mfma_f32_16x16x32_bf16 v[72:75], v[168:171], v[188:191], v[72:75]\n"
    "s_add_u32 m0, s58, 57344\n"
    "s_add_u32 s60, s59, 384\n"
    "buffer_load_dwordx4 v141, s[36:39], s60 offen offset:0 lds\n"
    "s_add_u32 s60, s59, 9600\n"
    "v_mfma_f32_16x16x32_bf16 v[76:79], v[168:171], v[192:195], v[76:79]\n"
    "v_mfma_f32_16x16x32_bf16 v[80:83], v[168:171], v[196:199], v[80:83]\n"
    "ds_read_b64_tr_b16 v[168:169], v136 offset:18688\n"
    "ds_read_b64_tr_b16 v[170:171], v136 offset:19200\n"
    "v_mfma_f32_16x16x32_bf16 v[84:87], v[172:175], v[188:191], v[84:87]\n"
    "s_sleep 1\n"
    "buffer_load_dwordx4 v142, s[36:39], s60 offen offset:1024 lds\n"
    "s_add_u32 s60, s59, 18816\n"
    "v_mfma_f32_16x16x32_bf16 v[88:91], v[172:175], v[192:195], v[88:91]\n"
    "v_mfma_f32_16x16x32_bf16 v[92:95], v[172:175], v[196:199], v[92:95]\n"
    "ds_read_b64_tr_b16 v[172:173], v137 offset:18688\n"
    "ds_read_b64_tr_b16 v[174:175], v137 offset:19200\n"
    "s_waitcnt lgkmcnt(14)\n"
    "v_mfma_f32_16x16x32_bf16 v[0:3], v[144:147], v[176:179], v[0:3]\n"
    "buffer_load_dwordx4 v141, s[36:39], s60 offen offset:2048 lds\n"
    "v_mfma_f32_16x16x32_bf16 v[4:7], v[144:147], v[180:183], v[4:7]\n"
    "v_mfma_f32_16x16x32_bf16 v[8:11], v[144:147], v[184:187], v[8:11]\n"
    "s_waitcnt lgkmcnt(12)\n"
    "v_mfma_f32_16x16x32_bf16 v[12:15], v[148:151], v[176:179], v[12:15]\n"
    "s_sleep 1\n"
    "s_add_u32 m0, s56, 131072\n"
    "s_add_u32 s61, s65, 0\n"
    "s_and_b32 s61, s61, 0x1ffff\n"
    "s_add_u32 s61, s61, 130048\n"
    "buffer_load_dwordx4 v247, s[48:51], s61 offen offset:3072 nt lds\n"
    "v_mfma_f32_16x16x32_bf16 v[16:19], v[148:151], v[180:183], v[16:19]\n"
    "v_mfma_f32_16x16x32_bf16 v[20:23], v[148:151], v[184:187], v[20:23]\n"
    "s_waitcnt lgkmcnt(8)\n"
    "v_mfma_f32_16x16x32_bf16 v[24:27], v[152:155], v[176:179], v[24:27]\n"
    "v_mfma_f32_16x16x32_bf16 v[28:31], v[152:155], v[180:183], v[28:31]\n"
    "v_mfma_f32_16x16x32_bf16 v[32:35], v[152:155], v[184:187], v[32:35]\n"
    "s_waitcnt lgkmcnt(6)\n"
    "v_mfma_f32_16x16x32_bf16 v[36:39], v[156:159], v[176:179], v[36:39]\n"
    "s_sleep 1\n"
    "v_mfma_f32_16x16x32_bf16 v[40:43], v[156:159], v[180:183], v[40:43]\n"
    "v_mfma_f32_16x16x32_bf16 v[44:47], v[156:159], v[184:187], v[44:47]\n"
    "s_waitcnt vmcnt(7) lgkmcnt(0)\n"
    "s_barrier\n"
    "ds_read_b128 v[188:191], v139 offset:0\n"
    "v_mfma_f32_16x16x32_bf16 v[48:51], v[160:163], v[176:179], v[48:51]\n"
    "ds_read_b128 v[192:195], v139 offset:2048\n"
    "ds_read_b64_tr_b16 v[144:145], v136 offset:32768\n"
    "ds_read_b64_tr_b16 v[146:147], v136 offset:33280\n"
    "s_add_u32 m0, s57, 98304\n"
    "s_mov_b32 s60, 1835008\n"
    "buffer_load_dwordx4 v140, s[40:43], s60 offen offset:0 lds\n"
    "s_mov_b32 s60, 1965056\n"
    "v_mfma_f32_16x16x32_bf16 v[52:55], v[160:163], v[180:183], v[52:55]\n"
    "ds_read_b128 v[196:199], v139 offset:4096\n"
    "ds_read_b64_tr_b16 v[148:149], v137 offset:32768\n"
    "ds_read_b64_tr_b16 v[150:151], v137 offset:33280\n"
    "v_mfma_f32_16x16x32_bf16 v[56:59], v[160:163], v[184:187], v[56:59]\n"
    "ds_read_b64_tr_b16 v[160:161], v136 offset:34816\n"
    "ds_read_b64_tr_b16 v[162:163], v136 offset:35328\n"
    "v_mfma_f32_16x16x32_bf16 v[60:63], v[164:167], v[176:179], v[60:63]\n"
    "s_sleep 1\n"
    "ds_read_b64_tr_b16 v[152:153], v136 offset:33024\n"
    "ds_read_b64_tr_b16 v[154:155], v136 offset:33536\n"
    "buffer_load_dwordx4 v140, s[40:43], s60 offen offset:1024 lds\n"
    "v_mfma_f32_16x16x32_bf16 v[64:67], v[164:167], v[180:183], v[64:67]\n"
    "ds_read_b64_tr_b16 v[156:157], v137 offset:33024\n"
    "ds_read_b64_tr_b16 v[158:159], v137 offset:33536\n"
    "v_mfma_f32_16x16x32_bf16 v[68:71], v[164:167], v[184:187], v[68:71]\n"
    "ds_read_b64_tr_b16 v[164:165], v137 offset:34816\n"
    "ds_read_b64_tr_b16 v[166:167], v137 offset:35328\n"
    "v_mfma_f32_16x16x32_bf16 v[72:75], v[168:171], v[176:179], v[72:75]\n"
    "s_add_u32 m0, s56, 0\n"
    "s_add_u32 s61, s65, 8192\n"
    "s_and_b32 s61, s61, 0x1ffff\n"
    "buffer_load_dwordx4 v246, s[44:47], s61 offen offset:0 nt lds\n"
    "v_mfma_f32_16x16x32_bf16 v[76:79], v[168:171], v[180:183], v[76:79]\n"
    "v_mfma_f32_16x16x32_bf16 v[80:83], v[168:171], v[184:187], v[80:83]\n"
    "ds_read_b64_tr_b16 v[168:169], v136 offset:35072\n"
    "ds_read_b64_tr_b16 v[170:171], v136 offset:35584\n"
    "v_mfma_f32_16x16x32_bf16 v[84:87], v[172:175], v[176:179], v[84:87]\n"
    "s_sleep 1\n"
    "v_mfma_f32_16x16x32_bf16 v[88:91], v[172:175], v[180:183], v[88:91]\n"
    "v_mfma_f32_16x16x32_bf16 v[92:95], v[172:175], v[184:187], v[92:95]\n"
    "ds_read_b64_tr_b16 v[172:173], v137 offset:35072\n"
    "ds_read_b64_tr_b16 v[174:175], v137 offset:35584\n"
    "s_waitcnt lgkmcnt(14)\n"
    "v_mfma_f32_16x16x32_bf16 v[0:3], v[144:147], v[188:191], v[0:3]\n"
    "v_mfma_f32_16x16x32_bf16 v[4:7], v[144:147], v[192:195], v[4:7]\n"
    "v_mfma_f32_16x16x32_bf16 v[8:11], v[144:147], v[196:199], v[8:11]\n"
    "s_waitcnt lgkmcnt(12)\n"
    "v_mfma_f32_16x16x32_bf16 v[12:15], v[148:151], v[188:191], v[12:15]\n"
    "s_sleep 1\n"
    "v_mfma_f32_16x16x32_bf16 v[16:19], v[148:151], v[192:195], v[16:19]\n"
    "v_mfma_f32_16x16x32_bf16 v[20:23], v[148:151], v[196:199], v[20:23]\n"
    "s_waitcnt lgkmcnt(8)\n"
    "v_mfma_f32_16x16x32_bf16 v[24:27], v[152:155], v[188:191], v[24:27]\n"
    "v_mfma_f32_16x16x32_bf16 v[28:31], v[152:155], v[192:195], v[28:31]\n"
    "v_mfma_f32_16x16x32_bf16 v[32:35], v[152:155], v[196:199], v[32:35]\n"
    "s_waitcnt lgkmcnt(6)\n"
    "v_mfma_f32_16x16x32_bf16 v[36:39], v[156:159], v[188:191], v[36:39]\n"
    "s_sleep 1\n"
    "v_mfma_f32_16x16x32_bf16 v[40:43], v[156:159], v[192:195], v[40:43]\n"
    "v_mfma_f32_16x16x32_bf16 v[44:47], v[156:159], v[196:199], v[44:47]\n"
    "s_waitcnt vmcnt(4) lgkmcnt(0)\n"
    "s_barrier\n"
    "ds_read_b128 v[176:179], v138 offset:24576\n"
    "v_mfma_f32_16x16x32_bf16 v[48:51], v[160:163], v[188:191], v[48:51]\n"
    "ds_read_b128 v[180:183], v138 offset:26624\n"
    "ds_read_b64_tr_b16 v[144:145], v136 offset:0\n"
    "ds_read_b64_tr_b16 v[146:147], v136 offset:512\n"
    "s_add_u32 m0, s57, 114688\n"
    "s_mov_b32 s60, 2097152\n"
    "buffer_load_dwordx4 v140, s[40:43], s60 offen offset:0 lds\n"
    "s_mov_b32 s60, 2227200\n"
    "v_mfma_f32_16x16x32_bf16 v[52:55], v[160:163], v[192:195], v[52:55]\n"
    "ds_read_b128 v[184:187], v138 offset:28672\n"
    "ds_read_b64_tr_b16 v[148:149], v137 offset:0\n"
    "ds_read_b64_tr_b16 v[150:151], v137 offset:512\n"
    "v_mfma_f32_16x16x32_bf16 v[56:59], v[160:163], v[196:199], v[56:59]\n"
    "ds_read_b64_tr_b16 v[160:161], v136 offset:2048\n"
    "ds_read_b64_tr_b16 v[162:163], v136 offset:2560\n"
    "v_mfma_f32_16x16x32_bf16 v[60:63], v[164:167], v[188:191], v[60:63]\n"
    "s_sleep 1\n"
    "ds_read_b64_tr_b16 v[152:153], v136 offset:256\n"
    "ds_read_b64_tr_b16 v[154:155], v136 offset:768\n"
    "buffer_load_dwordx4 v140, s[40:43], s60 offen offset:1024 lds\n"
    "v_mfma_f32_16x16x32_bf16 v[64:67], v[164:167], v[192:195], v[64:67]\n"
    "ds_read_b64_tr_b16 v[156:157], v137 offset:256\n"
    "ds_read_b64_tr_b16 v[158:159], v137 offset:768\n"
    "v_mfma_f32_16x16x32_bf16 v[68:71], v[164:167], v[196:199], v[68:71]\n"
    "ds_read_b64_tr_b16 v[164:165], v137 offset:2048\n"
    "ds_read_b64_tr_b16 v[166:167], v137 offset:2560\n"
    "v_mfma_f32_16x16x32_bf16 v[72:75], v[168:171], v[188:191], v[72:75]\n"
    "s_add_u32 m0, s58, 32768\n"
    "s_add_u32 s60, s59, 512\n"
    "buffer_load_dwordx4 v141, s[36:39], s60 offen offset:0 lds\n"
    "s_add_u32 s60, s59, 9728\n"
    "v_mfma_f32_16x16x32_bf16 v[76:79], v[168:171], v[192:195], v[76:79]\n"
    "v_mfma_f32_16x16x32_bf16 v[80:83], v[168:171], v[196:199], v[80:83]\n"
    "ds_read_b64_tr_b16 v[168:169], v136 offset:2304\n"
    "ds_read_b64_tr_b16 v[170:171], v136 offset:2816\n"
    "v_mfma_f32_16x16x32_bf16 v[84:87], v[172:175], v[188:191], v[84:87]\n"
    "s_sleep 1\n"
    "buffer_load_dwordx4 v142, s[36:39], s60 offen offset:1024 lds\n"
    "s_add_u32 s60, s59, 18944\n"
    "v_mfma_f32_16x16x32_bf16 v[88:91], v[172:175], v[192:195], v[88:91]\n"
    "v_mfma_f32_16x16x32_bf16 v[92:95], v[172:175], v[196:199], v[92:95]\n"
    "ds_read_b64_tr_b16 v[172:173], v137 offset:2304\n"
    "ds_read_b64_tr_b16 v[174:175], v137 offset:2816\n"
    "s_waitcnt lgkmcnt(14)\n"
    "v_mfma_f32_16x16x32_bf16 v[0:3], v[144:147], v[176:179], v[0:3]\n"
    "buffer_load_dwordx4 v141, s[36:39], s60 offen offset:2048 lds\n"
    "v_mfma_f32_16x16x32_bf16 v[4:7], v[144:147], v[180:183], v[4:7]\n"
    "v_mfma_f32_16x16x32_bf16 v[8:11], v[144:147], v[184:187], v[8:11]\n"
    "s_waitcnt lgkmcnt(12)\n"
    "v_mfma_f32_16x16x32_bf16 v[12:15], v[148:151], v[176:179], v[12:15]\n"
    "s_sleep 1\n"
    "s_add_u32 m0, s56, 0\n"
    "s_add_u32 s61, s65, 8192\n"
    "s_and_b32 s61, s61, 0x1ffff\n"
    "s_add_u32 s61, s61, 130048\n"
    "buffer_load_dwordx4 v246, s[44:47], s61 offen offset:1024 nt lds\n"
    "v_mfma_f32_16x16x32_bf16 v[16:19], v[148:151], v[180:183], v[16:19]\n"
    "v_mfma_f32_16x16x32_bf16 v[20:23], v[148:151], v[184:187], v[20:23]\n"
    "s_waitcnt lgkmcnt(8)\n"
    "v_mfma_f32_16x16x32_bf16 v[24:27], v[152:155], v[176:179], v[24:27]\n"
    "v_mfma_f32_16x16x32_bf16 v[28:31], v[152:155], v[180:183], v[28:31]\n"
    "v_mfma_f32_16x16x32_bf16 v[32:35], v[152:155], v[184:187], v[32:35]\n"
    "s_waitcnt lgkmcnt(6)\n"
    "v_mfma_f32_16x16x32_bf16 v[36:39], v[156:159], v[176:179], v[36:39]\n"
    "s_sleep 1\n"
    "v_mfma_f32_16x16x32_bf16 v[40:43], v[156:159], v[180:183], v[40:43]\n"
    "v_mfma_f32_16x16x32_bf16 v[44:47], v[156:159], v[184:187], v[44:47]\n"
    "s_waitcnt vmcnt(7) lgkmcnt(0)\n"
    "s_barrier\n"
    "ds_read_b128 v[188:191], v139 offset:24576\n"
    "v_mfma_f32_16x16x32_bf16 v[48:51], v[160:163], v[176:179], v[48:51]\n"
    "ds_read_b128 v[192:195], v139 offset:26624\n"
    "ds_read_b64_tr_b16 v[144:145], v136 offset:16384\n"
    "ds_read_b64_tr_b16 v[146:147], v136 offset:16896\n"
    "s_add_u32 m0, s57, 81920\n"
    "s_mov_b32 s60, 2359296\n"
    "buffer_load_dwordx4 v140, s[40:43], s60 offen offset:0 lds\n"
    "s_mov_b32 s60, 2489344\n"
    "v_mfma_f32_16x16x32_bf16 v[52:55], v[160:163], v[180:183], v[52:55]\n"
    "ds_read_b128 v[196:199], v139 offset:28672\n"
    "ds_read_b64_tr_b16 v[148:149], v137 offset:16384\n"
    "ds_read_b64_tr_b16 v[150:151], v137 offset:16896\n"
    "v_mfma_f32_16x16x32_bf16 v[56:59], v[160:163], v[184:187], v[56:59]\n"
    "ds_read_b64_tr_b16 v[160:161], v136 offset:18432\n"
    "ds_read_b64_tr_b16 v[162:163], v136 offset:18944\n"
    "v_mfma_f32_16x16x32_bf16 v[60:63], v[164:167], v[176:179], v[60:63]\n"
    "s_sleep 1\n"
    "ds_read_b64_tr_b16 v[152:153], v136 offset:16640\n"
    "ds_read_b64_tr_b16 v[154:155], v136 offset:17152\n"
    "buffer_load_dwordx4 v140, s[40:43], s60 offen offset:1024 lds\n"
    "v_mfma_f32_16x16x32_bf16 v[64:67], v[164:167], v[180:183], v[64:67]\n"
    "ds_read_b64_tr_b16 v[156:157], v137 offset:16640\n"
    "ds_read_b64_tr_b16 v[158:159], v137 offset:17152\n"
    "v_mfma_f32_16x16x32_bf16 v[68:71], v[164:167], v[184:187], v[68:71]\n"
    "ds_read_b64_tr_b16 v[164:165], v137 offset:18432\n"
    "ds_read_b64_tr_b16 v[166:167], v137 offset:18944\n"
    "v_mfma_f32_16x16x32_bf16 v[72:75], v[168:171], v[176:179], v[72:75]\n"
    "s_add_u32 m0, s56, 0\n"
    "s_add_u32 s61, s65, 8192\n"
    "s_and_b32 s61, s61, 0x1ffff\n"
    "buffer_load_dwordx4 v247, s[48:51], s61 offen offset:2048 nt lds\n"
    "v_mfma_f32_16x16x32_bf16 v[76:79], v[168:171], v[180:183], v[76:79]\n"
    "v_mfma_f32_16x16x32_bf16 v[80:83], v[168:171], v[184:187], v[80:83]\n"
    "ds_read_b64_tr_b16 v[168:169], v136 offset:18688\n"
    "ds_read_b64_tr_b16 v[170:171], v136 offset:19200\n"
    "v_mfma_f32_16x16x32_bf16 v[84:87], v[172:175], v[176:179], v[84:87]\n"
    "s_sleep 1\n"
    "v_mfma_f32_16x16x32_bf16 v[88:91], v[172:175], v[180:183], v[88:91]\n"
    "v_mfma_f32_16x16x32_bf16 v[92:95], v[172:175], v[184:187], v[92:95]\n"
    "ds_read_b64_tr_b16 v[172:173], v137 offset:18688\n"
    "ds_read_b64_tr_b16 v[174:175], v137 offset:19200\n"
    "s_waitcnt lgkmcnt(14)\n"
    "v_mfma_f32_16x16x32_bf16 v[0:3], v[144:147], v[188:191], v[0:3]\n"
    "v_mfma_f32_16x16x32_bf16 v[4:7], v[144:147], v[192:195], v[4:7]\n"
    "v_mfma_f32_16x16x32_bf16 v[8:11], v[144:147], v[196:199], v[8:11]\n"
    "s_waitcnt lgkmcnt(12)\n"
    "v_mfma_f32_16x16x32_bf16 v[12:15], v[148:151], v[188:191], v[12:15]\n"
    "s_sleep 1\n"
    "v_mfma_f32_16x16x32_bf16 v[16:19], v[148:151], v[192:195], v[16:19]\n"
    "v_mfma_f32_16x16x32_bf16 v[20:23], v[148:151], v[196:199], v[20:23]\n"
    "s_waitcnt lgkmcnt(8)\n"
    "v_mfma_f32_16x16x32_bf16 v[24:27], v[152:155], v[188:191], v[24:27]\n"
    "v_mfma_f32_16x16x32_bf16 v[28:31], v[152:155], v[192:195], v[28:31]\n"
    "v_mfma_f32_16x16x32_bf16 v[32:35], v[152:155], v[196:199], v[32:35]\n"
    "s_waitcnt lgkmcnt(6)\n"
    "v_mfma_f32_16x16x32_bf16 v[36:39], v[156:159], v[188:191], v[36:39]\n"
    "s_sleep 1\n"
    "v_mfma_f32_16x16x32_bf16 v[40:43], v[156:159], v[192:195], v[40:43]\n"
    "v_mfma_f32_16x16x32_bf16 v[44:47], v[156:159], v[196:199], v[44:47]\n"
    "s_waitcnt vmcnt(4) lgkmcnt(0)\n"
    "s_barrier\n"
    "ds_read_b128 v[176:179], v138 offset:0\n"
    "v_mfma_f32_16x16x32_bf16 v[48:51], v[160:163], v[188:191], v[48:51]\n"
    "ds_read_b128 v[180:183], v138 offset:2048\n"
    "ds_read_b64_tr_b16 v[144:145], v136 offset:32768\n"
    "ds_read_b64_tr_b16 v[146:147], v136 offset:33280\n"
    "s_add_u32 m0, s57, 98304\n"
    "s_mov_b32 s60, 2621440\n"
    "buffer_load_dwordx4 v140, s[40:43], s60 offen offset:0 lds\n"
    "s_mov_b32 s60, 2751488\n"
    "v_mfma_f32_16x16x32_bf16 v[52:55], v[160:163], v[192:195], v[52:55]\n"
    "ds_read_b128 v[184:187], v138 offset:4096\n"
    "ds_read_b64_tr_b16 v[148:149], v137 offset:32768\n"
    "ds_read_b64_tr_b16 v[150:151], v137 offset:33280\n"
    "v_mfma_f32_16x16x32_bf16 v[56:59], v[160:163], v[196:199], v[56:59]\n"
    "ds_read_b64_tr_b16 v[160:161], v136 offset:34816\n"
    "ds_read_b64_tr_b16 v[162:163], v136 offset:35328\n"
    "v_mfma_f32_16x16x32_bf16 v[60:63], v[164:167], v[188:191], v[60:63]\n"
    "s_sleep 1\n"
    "ds_read_b64_tr_b16 v[152:153], v136 offset:33024\n"
    "ds_read_b64_tr_b16 v[154:155], v136 offset:33536\n"
    "buffer_load_dwordx4 v140, s[40:43], s60 offen offset:1024 lds\n"
    "v_mfma_f32_16x16x32_bf16 v[64:67], v[164:167], v[192:195], v[64:67]\n"
    "ds_read_b64_tr_b16 v[156:157], v137 offset:33024\n"
    "ds_read_b64_tr_b16 v[158:159], v137 offset:33536\n"
    "v_mfma_f32_16x16x32_bf16 v[68:71], v[164:167], v[196:199], v[68:71]\n"
    "ds_read_b64_tr_b16 v[164:165], v137 offset:34816\n"
    "ds_read_b64_tr_b16 v[166:167], v137 offset:35328\n"
    "v_mfma_f32_16x16x32_bf16 v[72:75], v[168:171], v[188:191], v[72:75]\n"
    "s_add_u32 m0, s58, 57344\n"
    "s_add_u32 s60, s59, 640\n"
    "buffer_load_dwordx4 v141, s[36:39], s60 offen offset:0 lds\n"
    "s_add_u32 s60, s59, 9856\n"
    "v_mfma_f32_16x16x32_bf16 v[76:79], v[168:171], v[192:195], v[76:79]\n"
    "v_mfma_f32_16x16x32_bf16 v[80:83], v[168:171], v[196:199], v[80:83]\n"
    "ds_read_b64_tr_b16 v[168:169], v136 offset:35072\n"
    "ds_read_b64_tr_b16 v[170:171], v136 offset:35584\n"
    "v_mfma_f32_16x16x32_bf16 v[84:87], v[172:175], v[188:191], v[84:87]\n"
    "s_sleep 1\n"
    "buffer_load_dwordx4 v142, s[36:39], s60 offen offset:1024 lds\n"
    "s_add_u32 s60, s59, 19072\n"
    "v_mfma_f32_16x16x32_bf16 v[88:91], v[172:175], v[192:195], v[88:91]\n"
    "v_mfma_f32_16x16x32_bf16 v[92:95], v[172:175], v[196:199], v[92:95]\n"
    "ds_read_b64_tr_b16 v[172:173], v137 offset:35072\n"
    "ds_read_b64_tr_b16 v[174:175], v137 offset:35584\n"
    "s_waitcnt lgkmcnt(14)\n"
    "v_mfma_f32_16x16x32_bf16 v[0:3], v[144:147], v[176:179], v[0:3]\n"
    "buffer_load_dwordx4 v141, s[36:39], s60 offen offset:2048 lds\n"
    "v_mfma_f32_16x16x32_bf16 v[4:7], v[144:147], v[180:183], v[4:7]\n"
    "v_mfma_f32_16x16x32_bf16 v[8:11], v[144:147], v[184:187], v[8:11]\n"
    "s_waitcnt lgkmcnt(12)\n"
    "v_mfma_f32_16x16x32_bf16 v[12:15], v[148:151], v[176:179], v[12:15]\n"
    "s_sleep 1\n"
    "s_add_u32 m0, s56, 0\n"
    "s_add_u32 s61, s65, 8192\n"
    "s_and_b32 s61, s61, 0x1ffff\n"
    "s_add_u32 s61, s61, 130048\n"
    "buffer_load_dwordx4 v247, s[48:51], s61 offen offset:3072 nt lds\n"
    "v_mfma_f32_16x16x32_bf16 v[16:19], v[148:151], v[180:183], v[16:19]\n"
    "v_mfma_f32_16x16x32_bf16 v[20:23], v[148:151], v[184:187], v[20:23]\n"
    "s_waitcnt lgkmcnt(8)\n"
    "v_mfma_f32_16x16x32_bf16 v[24:27], v[152:155], v[176:179], v[24:27]\n"
    "v_mfma_f32_16x16x32_bf16 v[28:31], v[152:155], v[180:183], v[28:31]\n"
    "v_mfma_f32_16x16x32_bf16 v[32:35], v[152:155], v[184:187], v[32:35]\n"
    "s_waitcnt lgkmcnt(6)\n"
    "v_mfma_f32_16x16x32_bf16 v[36:39], v[156:159], v[176:179], v[36:39]\n"
    "s_sleep 1\n"
    "v_mfma_f32_16x16x32_bf16 v[40:43], v[156:159], v[180:183], v[40:43]\n"
    "v_mfma_f32_16x16x32_bf16 v[44:47], v[156:159], v[184:187], v[44:47]\n"
    "s_waitcnt vmcnt(7) lgkmcnt(0)\n"
    "s_barrier\n"
    "ds_read_b128 v[188:191], v139 offset:0\n"
    "v_mfma_f32_16x16x32_bf16 v[48:51], v[160:163], v[176:179], v[48:51]\n"
    "ds_read_b128 v[192:195], v139 offset:2048\n"
    "ds_read_b64_tr_b16 v[144:145], v136 offset:0\n"
    "ds_read_b64_tr_b16 v[146:147], v136 offset:512\n"
    "s_add_u32 m0, s57, 114688\n"
    "s_mov_b32 s60, 2883584\n"
    "buffer_load_dwordx4 v140, s[40:43], s60 offen offset:0 lds\n"
    "s_mov_b32 s60, 3013632\n"
    "v_mfma_f32_16x16x32_bf16 v[52:55], v[160:163], v[180:183], v[52:55]\n"
    "ds_read_b128 v[196:199], v139 offset:4096\n"
    "ds_read_b64_tr_b16 v[148:149], v137 offset:0\n"
    "ds_read_b64_tr_b16 v[150:151], v137 offset:512\n"
    "v_mfma_f32_16x16x32_bf16 v[56:59], v[160:163], v[184:187], v[56:59]\n"
    "ds_read_b64_tr_b16 v[160:161], v136 offset:2048\n"
    "ds_read_b64_tr_b16 v[162:163], v136 offset:2560\n"
    "v_mfma_f32_16x16x32_bf16 v[60:63], v[164:167], v[176:179], v[60:63]\n"
    "s_sleep 1\n"
    "ds_read_b64_tr_b16 v[152:153], v136 offset:256\n"
    "ds_read_b64_tr_b16 v[154:155], v136 offset:768\n"
    "buffer_load_dwordx4 v140, s[40:43], s60 offen offset:1024 lds\n"
    "v_mfma_f32_16x16x32_bf16 v[64:67], v[164:167], v[180:183], v[64:67]\n"
    "ds_read_b64_tr_b16 v[156:157], v137 offset:256\n"
    "ds_read_b64_tr_b16 v[158:159], v137 offset:768\n"
    "v_mfma_f32_16x16x32_bf16 v[68:71], v[164:167], v[184:187], v[68:71]\n"
    "ds_read_b64_tr_b16 v[164:165], v137 offset:2048\n"
    "ds_read_b64_tr_b16 v[166:167], v137 offset:2560\n"
    "v_mfma_f32_16x16x32_bf16 v[72:75], v[168:171], v[176:179], v[72:75]\n"
    "s_add_u32 s61, s65, 16384\n"
    "s_and_b32 s61, s61, 0x1ffff\n"
    "buffer_load_dwordx4 v[200:203], v246, s[44:47], s61 offen offset:0 nt\n"
    "v_mfma_f32_16x16x32_bf16 v[76:79], v[168:171], v[180:183], v[76:79]\n"
    "v_mfma_f32_16x16x32_bf16 v[80:83], v[168:171], v[184:187], v[80:83]\n"
    "ds_read_b64_tr_b16 v[168:169], v136 offset:2304\n"
    "ds_read_b64_tr_b16 v[170:171], v136 offset:2816\n"
    "v_mfma_f32_16x16x32_bf16 v[84:87], v[172:175], v[176:179], v[84:87]\n"
    "s_sleep 1\n"
    "v_mfma_f32_16x16x32_bf16 v[88:91], v[172:175], v[180:183], v[88:91]\n"
    "v_mfma_f32_16x16x32_bf16 v[92:95], v[172:175], v[184:187], v[92:95]\n"
    "ds_read_b64_tr_b16 v[172:173], v137 offset:2304\n"
    "ds_read_b64_tr_b16 v[174:175], v137 offset:2816\n"
    "s_waitcnt lgkmcnt(14)\n"
    "v_mfma_f32_16x16x32_bf16 v[0:3], v[144:147], v[188:191], v[0:3]\n"
    "v_mfma_f32_16x16x32_bf16 v[4:7], v[144:147], v[192:195], v[4:7]\n"
    "v_mfma_f32_16x16x32_bf16 v[8:11], v[144:147], v[196:199], v[8:11]\n"
    "s_waitcnt lgkmcnt(12)\n"
    "v_mfma_f32_16x16x32_bf16 v[12:15], v[148:151], v[188:191], v[12:15]\n"
    "s_sleep 1\n"
    "v_mfma_f32_16x16x32_bf16 v[16:19], v[148:151], v[192:195], v[16:19]\n"
    "v_mfma_f32_16x16x32_bf16 v[20:23], v[148:151], v[196:199], v[20:23]\n"
    "s_waitcnt lgkmcnt(8)\n"
    "v_mfma_f32_16x16x32_bf16 v[24:27], v[152:155], v[188:191], v[24:27]\n"
    "v_mfma_f32_16x16x32_bf16 v[28:31], v[152:155], v[192:195], v[28:31]\n"
    "v_mfma_f32_16x16x32_bf16 v[32:35], v[152:155], v[196:199], v[32:35]\n"
    "s_waitcnt lgkmcnt(6)\n"
    "v_mfma_f32_16x16x32_bf16 v[36:39], v[156:159], v[188:191], v[36:39]\n"
    "s_sleep 1\n"
    "v_mfma_f32_16x16x32_bf16 v[40:43], v[156:159], v[192:195], v[40:43]\n"
    "v_mfma_f32_16x16x32_bf16 v[44:47], v[156:159], v[196:199], v[44:47]\n"
    "s_waitcnt vmcnt(4) lgkmcnt(0)\n"
    "s_barrier\n"
    "ds_read_b128 v[176:179], v138 offset:24576\n"
    "v_mfma_f32_16x16x32_bf16 v[48:51], v[160:163], v[188:191], v[48:51]\n"
    "ds_read_b128 v[180:183], v138 offset:26624\n"
    "ds_read_b64_tr_b16 v[144:145], v136 offset:16384\n"
    "ds_read_b64_tr_b16 v[146:147], v136 offset:16896\n"
    "s_add_u32 m0, s57, 81920\n"
    "s_mov_b32 s60, 3145728\n"
    "buffer_load_dwordx4 v140, s[40:43], s60 offen offset:0 lds\n"
    "s_mov_b32 s60, 3275776\n"
    "v_mfma_f32_16x16x32_bf16 v[52:55], v[160:163], v[192:195], v[52:55]\n"
    "ds_read_b128 v[184:187], v138 offset:28672\n"
    "ds_read_b64_tr_b16 v[148:149], v137 offset:16384\n"
    "ds_read_b64_tr_b16 v[150:151], v137 offset:16896\n"
    "v_mfma_f32_16x16x32_bf16 v[56:59], v[160:163], v[196:199], v[56:59]\n"
    "ds_read_b64_tr_b16 v[160:161], v136 offset:18432\n"
    "ds_read_b64_tr_b16 v[162:163], v136 offset:18944\n"
    "v_mfma_f32_16x16x32_bf16 v[60:63], v[164:167], v[188:191], v[60:63]\n"
    "s_sleep 1\n"
    "ds_read_b64_tr_b16 v[152:153], v136 offset:16640\n"
    "ds_read_b64_tr_b16 v[154:155], v136 offset:17152\n"
    "buffer_load_dwordx4 v140, s[40:43], s60 offen offset:1024 lds\n"
    "v_mfma_f32_16x16x32_bf16 v[64:67], v[164:167], v[192:195], v[64:67]\n"
    "ds_read_b64_tr_b16 v[156:157], v137 offset:16640\n"
    "ds_read_b64_tr_b16 v[158:159], v137 offset:17152\n"
    "v_mfma_f32_16x16x32_bf16 v[68:71], v[164:167], v[196:199], v[68:71]\n"
    "ds_read_b64_tr_b16 v[164:165], v137 offset:18432\n"
    "ds_read_b64_tr_b16 v[166:167], v137 offset:18944\n"
    "v_mfma_f32_16x16x32_bf16 v[72:75], v[168:171], v[188:191], v[72:75]\n"
    "s_add_u32 m0, s58, 32768\n"
    "s_add_u32 s60, s59, 768\n"
    "buffer_load_dwordx4 v141, s[36:39], s60 offen offset:0 lds\n"
    "s_add_u32 s60, s59, 9984\n"
    "v_mfma_f32_16x16x32_bf16 v[76:79], v[168:171], v[192:195], v[76:79]\n"
    "v_mfma_f32_16x16x32_bf16 v[80:83], v[168:171], v[196:199], v[80:83]\n"
    "ds_read_b64_tr_b16 v[168:169], v136 offset:18688\n"
    "ds_read_b64_tr_b16 v[170:171], v136 offset:19200\n"
    "v_mfma_f32_16x16x32_bf16 v[84:87], v[172:175], v[188:191], v[84:87]\n"
    "s_sleep 1\n"
    "buffer_load_dwordx4 v142, s[36:39], s60 offen offset:1024 lds\n"
    "s_add_u32 s60, s59, 19200\n"
    "v_mfma_f32_16x16x32_bf16 v[88:91], v[172:175], v[192:195], v[88:91]\n"
    "v_mfma_f32_16x16x32_bf16 v[92:95], v[172:175], v[196:199], v[92:95]\n"
    "ds_read_b64_tr_b16 v[172:173], v137 offset:18688\n"
    "ds_read_b64_tr_b16 v[174:175], v137 offset:19200\n"
    "s_waitcnt lgkmcnt(14)\n"
    "v_mfma_f32_16x16x32_bf16 v[0:3], v[144:147], v[176:179], v[0:3]\n"
    "buffer_load_dwordx4 v141, s[36:39], s60 offen offset:2048 lds\n"
    "v_mfma_f32_16x16x32_bf16 v[4:7], v[144:147], v[180:183], v[4:7]\n"
    "v_mfma_f32_16x16x32_bf16 v[8:11], v[144:147], v[184:187], v[8:11]\n"
    "s_waitcnt lgkmcnt(12)\n"
    "v_mfma_f32_16x16x32_bf16 v[12:15], v[148:151], v[176:179], v[12:15]\n"
    "s_sleep 1\n"
    "s_add_u32 s61, s65, 16384\n"
    "s_and_b32 s61, s61, 0x1ffff\n"
    "s_add_u32 s61, s61, 130048\n"
    "buffer_load_dwordx4 v[204:207], v246, s[44:47], s61 offen offset:1024 nt\n"
    "v_mfma_f32_16x16x32_bf16 v[16:19], v[148:151], v[180:183], v[16:19]\n"
    "v_mfma_f32_16x16x32_bf16 v[20:23], v[148:151], v[184:187], v[20:23]\n"
    "s_waitcnt lgkmcnt(8)\n"
    "v_mfma_f32_16x16x32_bf16 v[24:27], v[152:155], v[176:179], v[24:27]\n"
    "v_mfma_f32_16x16x32_bf16 v[28:31], v[152:155], v[180:183], v[28:31]\n"
    "v_mfma_f32_16x16x32_bf16 v[32:35], v[152:155], v[184:187], v[32:35]\n"
    "s_waitcnt lgkmcnt(6)\n"
    "v_mfma_f32_16x16x32_bf16 v[36:39], v[156:159], v[176:179], v[36:39]\n"
    "s_sleep 1\n"
    "v_mfma_f32_16x16x32_bf16 v[40:43], v[156:159], v[180:183], v[40:43]\n"
    "v_mfma_f32_16x16x32_bf16 v[44:47], v[156:159], v[184:187], v[44:47]\n"
    "s_waitcnt vmcnt(7) lgkmcnt(0)\n"
    "s_barrier\n"
    "ds_read_b128 v[188:191], v139 offset:24576\n"
    "v_mfma_f32_16x16x32_bf16 v[48:51], v[160:163], v[176:179], v[48:51]\n"
    "ds_read_b128 v[192:195], v139 offset:26624\n"
    "ds_read_b64_tr_b16 v[144:145], v136 offset:32768\n"
    "ds_read_b64_tr_b16 v[146:147], v136 offset:33280\n"
    "s_add_u32 m0, s57, 98304\n"
    "s_mov_b32 s60, 3407872\n"
    "buffer_load_dwordx4 v140, s[40:43], s60 offen offset:0 lds\n"
    "s_mov_b32 s60, 3537920\n"
    "v_mfma_f32_16x16x32_bf16 v[52:55], v[160:163], v[180:183], v[52:55]\n"
    "ds_read_b128 v[196:199], v139 offset:28672\n"
    "ds_read_b64_tr_b16 v[148:149], v137 offset:32768\n"
    "ds_read_b64_tr_b16 v[150:151], v137 offset:33280\n"
    "v_mfma_f32_16x16x32_bf16 v[56:59], v[160:163], v[184:187], v[56:59]\n"
    "ds_read_b64_tr_b16 v[160:161], v136 offset:34816\n"
    "ds_read_b64_tr_b16 v[162:163], v136 offset:35328\n"
    "v_mfma_f32_16x16x32_bf16 v[60:63], v[164:167], v[176:179], v[60:63]\n"
    "s_sleep 1\n"
    "ds_read_b64_tr_b16 v[152:153], v136 offset:33024\n"
    "ds_read_b64_tr_b16 v[154:155], v136 offset:33536\n"
    "buffer_load_dwordx4 v140, s[40:43], s60 offen offset:1024 lds\n"
    "v_mfma_f32_16x16x32_bf16 v[64:67], v[164:167], v[180:183], v[64:67]\n"
    "ds_read_b64_tr_b16 v[156:157], v137 offset:33024\n"
    "ds_read_b64_tr_b16 v[158:159], v137 offset:33536\n"
    "v_mfma_f32_16x16x32_bf16 v[68:71], v[164:167], v[184:187], v[68:71]\n"
    "ds_read_b64_tr_b16 v[164:165], v137 offset:34816\n"
    "ds_read_b64_tr_b16 v[166:167], v137 offset:35328\n"
    "v_mfma_f32_16x16x32_bf16 v[72:75], v[168:171], v[176:179], v[72:75]\n"
    "s_add_u32 s61, s65, 16384\n"
    "s_and_b32 s61, s61, 0x1ffff\n"
    "buffer_load_dwordx4 v[208:211], v247, s[48:51], s61 offen offset:2048 nt\n"
    "v_mfma_f32_16x16x32_bf16 v[76:79], v[168:171], v[180:183], v[76:79]\n"
    "v_mfma_f32_16x16x32_bf16 v[80:83], v[168:171], v[184:187], v[80:83]\n"
    "ds_read_b64_tr_b16 v[168:169], v136 offset:35072\n"
    "ds_read_b64_tr_b16 v[170:171], v136 offset:35584\n"
    "v_mfma_f32_16x16x32_bf16 v[84:87], v[172:175], v[176:179], v[84:87]\n"
    "s_sleep 1\n"
    "v_mfma_f32_16x16x32_bf16 v[88:91], v[172:175], v[180:183], v[88:91]\n"
    "v_mfma_f32_16x16x32_bf16 v[92:95], v[172:175], v[184:187], v[92:95]\n"
    "ds_read_b64_tr_b16 v[172:173], v137 offset:35072\n"
    "ds_read_b64_tr_b16 v[174:175], v137 offset:35584\n"
    "s_waitcnt lgkmcnt(14)\n"
    "v_mfma_f32_16x16x32_bf16 v[0:3], v[144:147], v[188:191], v[0:3]\n"
    "v_mfma_f32_16x16x32_bf16 v[4:7], v[144:147], v[192:195], v[4:7]\n"
    "v_mfma_f32_16x16x32_bf16 v[8:11], v[144:147], v[196:199], v[8:11]\n"
    "s_waitcnt lgkmcnt(12)\n"
    "v_mfma_f32_16x16x32_bf16 v[12:15], v[148:151], v[188:191], v[12:15]\n"
    "s_sleep 1\n"
    "v_mfma_f32_16x16x32_bf16 v[16:19], v[148:151], v[192:195], v[16:19]\n"
    "v_mfma_f32_16x16x32_bf16 v[20:23], v[148:151], v[196:199], v[20:23]\n"
    "s_waitcnt lgkmcnt(8)\n"
    "v_mfma_f32_16x16x32_bf16 v[24:27], v[152:155], v[188:191], v[24:27]\n"
    "v_mfma_f32_16x16x32_bf16 v[28:31], v[152:155], v[192:195], v[28:31]\n"
    "v_mfma_f32_16x16x32_bf16 v[32:35], v[152:155], v[196:199], v[32:35]\n"
    "s_waitcnt lgkmcnt(6)\n"
    "v_mfma_f32_16x16x32_bf16 v[36:39], v[156:159], v[188:191], v[36:39]\n"
    "s_sleep 1\n"
    "v_mfma_f32_16x16x32_bf16 v[40:43], v[156:159], v[192:195], v[40:43]\n"
    "v_mfma_f32_16x16x32_bf16 v[44:47], v[156:159], v[196:199], v[44:47]\n"
    "s_waitcnt vmcnt(4) lgkmcnt(0)\n"
    "s_barrier\n"
    "ds_read_b128 v[176:179], v138 offset:0\n"
    "v_mfma_f32_16x16x32_bf16 v[48:51], v[160:163], v[188:191], v[48:51]\n"
    "ds_read_b128 v[180:183], v138 offset:2048\n"
    "ds_read_b64_tr_b16 v[144:145], v136 offset:0\n"
    "ds_read_b64_tr_b16 v[146:147], v136 offset:512\n"
    "s_add_u32 m0, s57, 114688\n"
    "s_mov_b32 s60, 3670016\n"
    "buffer_load_dwordx4 v140, s[40:43], s60 offen offset:0 lds\n"
    "s_mov_b32 s60, 3800064\n"
    "v_mfma_f32_16x16x32_bf16 v[52:55], v[160:163], v[192:195], v[52:55]\n"
    "ds_read_b128 v[184:187], v138 offset:4096\n"
    "ds_read_b64_tr_b16 v[148:149], v137 offset:0\n"
    "ds_read_b64_tr_b16 v[150:151], v137 offset:512\n"
    "v_mfma_f32_16x16x32_bf16 v[56:59], v[160:163], v[196:199], v[56:59]\n"
    "ds_read_b64_tr_b16 v[160:161], v136 offset:2048\n"
    "ds_read_b64_tr_b16 v[162:163], v136 offset:2560\n"
    "v_mfma_f32_16x16x32_bf16 v[60:63], v[164:167], v[188:191], v[60:63]\n"
    "s_sleep 1\n"
    "ds_read_b64_tr_b16 v[152:153], v136 offset:256\n"
    "ds_read_b64_tr_b16 v[154:155], v136 offset:768\n"
    "buffer_load_dwordx4 v140, s[40:43], s60 offen offset:1024 lds\n"
    "v_mfma_f32_16x16x32_bf16 v[64:67], v[164:167], v[192:195], v[64:67]\n"
    "ds_read_b64_tr_b16 v[156:157], v137 offset:256\n"
    "ds_read_b64_tr_b16 v[158:159], v137 offset:768\n"
    "v_mfma_f32_16x16x32_bf16 v[68:71], v[164:167], v[196:199], v[68:71]\n"
    "ds_read_b64_tr_b16 v[164:165], v137 offset:2048\n"
    "ds_read_b64_tr_b16 v[166:167], v137 offset:2560\n"
    "v_mfma_f32_16x16x32_bf16 v[72:75], v[168:171], v[188:191], v[72:75]\n"
    "s_add_u32 m0, s58, 57344\n"
    "s_add_u32 s60, s59, 896\n"
    "buffer_load_dwordx4 v141, s[36:39], s60 offen offset:0 lds\n"
    "s_add_u32 s60, s59, 10112\n"
    "v_mfma_f32_16x16x32_bf16 v[76:79], v[168:171], v[192:195], v[76:79]\n"
    "v_mfma_f32_16x16x32_bf16 v[80:83], v[168:171], v[196:199], v[80:83]\n"
    "ds_read_b64_tr_b16 v[168:169], v136 offset:2304\n"
    "ds_read_b64_tr_b16 v[170:171], v136 offset:2816\n"
    "v_mfma_f32_16x16x32_bf16 v[84:87], v[172:175], v[188:191], v[84:87]\n"
    "s_sleep 1\n"
    "buffer_load_dwordx4 v142, s[36:39], s60 offen offset:1024 lds\n"
    "s_add_u32 s60, s59, 19328\n"
    "v_mfma_f32_16x16x32_bf16 v[88:91], v[172:175], v[192:195], v[88:91]\n"
    "v_mfma_f32_16x16x32_bf16 v[92:95], v[172:175], v[196:199], v[92:95]\n"
    "ds_read_b64_tr_b16 v[172:173], v137 offset:2304\n"
    "ds_read_b64_tr_b16 v[174:175], v137 offset:2816\n"
    "s_waitcnt lgkmcnt(14)\n"
    "v_mfma_f32_16x16x32_bf16 v[0:3], v[144:147], v[176:179], v[0:3]\n"
    "buffer_load_dwordx4 v141, s[36:39], s60 offen offset:2048 lds\n"
    "v_mfma_f32_16x16x32_bf16 v[4:7], v[144:147], v[180:183], v[4:7]\n"
    "v_mfma_f32_16x16x32_bf16 v[8:11], v[144:147], v[184:187], v[8:11]\n"
    "s_waitcnt lgkmcnt(12)\n"
    "v_mfma_f32_16x16x32_bf16 v[12:15], v[148:151], v[176:179], v[12:15]\n"
    "s_sleep 1\n"
    "s_add_u32 s61, s65, 16384\n"
    "s_and_b32 s61, s61, 0x1ffff\n"
    "s_add_u32 s61, s61, 130048\n"
    "buffer_load_dwordx4 v[212:215], v247, s[48:51], s61 offen offset:3072 nt\n"
    "v_mfma_f32_16x16x32_bf16 v[16:19], v[148:151], v[180:183], v[16:19]\n"
    "v_mfma_f32_16x16x32_bf16 v[20:23], v[148:151], v[184:187], v[20:23]\n"
    "s_waitcnt lgkmcnt(8)\n"
    "v_mfma_f32_16x16x32_bf16 v[24:27], v[152:155], v[176:179], v[24:27]\n"
    "v_mfma_f32_16x16x32_bf16 v[28:31], v[152:155], v[180:183], v[28:31]\n"
    "v_mfma_f32_16x16x32_bf16 v[32:35], v[152:155], v[184:187], v[32:35]\n"
    "s_waitcnt lgkmcnt(6)\n"
    "v_mfma_f32_16x16x32_bf16 v[36:39], v[156:159], v[176:179], v[36:39]\n"
    "s_sleep 1\n"
    "v_mfma_f32_16x16x32_bf16 v[40:43], v[156:159], v[180:183], v[40:43]\n"
    "v_mfma_f32_16x16x32_bf16 v[44:47], v[156:159], v[184:187], v[44:47]\n"
    "s_waitcnt vmcnt(7) lgkmcnt(0)\n"
    "s_barrier\n"
    "ds_read_b128 v[188:191], v139 offset:0\n"
    "v_mfma_f32_16x16x32_bf16 v[48:51], v[160:163], v[176:179], v[48:51]\n"
    "ds_read_b128 v[192:195], v139 offset:2048\n"
    "ds_read_b64_tr_b16 v[144:145], v136 offset:16384\n"
    "ds_read_b64_tr_b16 v[146:147], v136 offset:16896\n"
    "s_add_u32 m0, s57, 81920\n"
    "s_mov_b32 s60, 3932160\n"
    "buffer_load_dwordx4 v140, s[40:43], s60 offen offset:0 lds\n"
    "s_mov_b32 s60, 4062208\n"
    "v_mfma_f32_16x16x32_bf16 v[52:55], v[160:163], v[180:183], v[52:55]\n"
    "ds_read_b128 v[196:199], v139 offset:4096\n"
    "ds_read_b64_tr_b16 v[148:149], v137 offset:16384\n"
    "ds_read_b64_tr_b16 v[150:151], v137 offset:16896\n"
    "v_mfma_f32_16x16x32_bf16 v[56:59], v[160:163], v[184:187], v[56:59]\n"
    "ds_read_b64_tr_b16 v[160:161], v136 offset:18432\n"
    "ds_read_b64_tr_b16 v[162:163], v136 offset:18944\n"
    "v_mfma_f32_16x16x32_bf16 v[60:63], v[164:167], v[176:179], v[60:63]\n"
    "s_sleep 1\n"
    "ds_read_b64_tr_b16 v[152:153], v136 offset:16640\n"
    "ds_read_b64_tr_b16 v[154:155], v136 offset:17152\n"
    "buffer_load_dwordx4 v140, s[40:43], s60 offen offset:1024 lds\n"
    "v_mfma_f32_16x16x32_bf16 v[64:67], v[164:167], v[180:183], v[64:67]\n"
    "ds_read_b64_tr_b16 v[156:157], v137 offset:16640\n"
    "ds_read_b64_tr_b16 v[158:159], v137 offset:17152\n"
    "v_mfma_f32_16x16x32_bf16 v[68:71], v[164:167], v[184:187], v[68:71]\n"
    "ds_read_b64_tr_b16 v[164:165], v137 offset:18432\n"
    "ds_read_b64_tr_b16 v[166:167], v137 offset:18944\n"
    "v_mfma_f32_16x16x32_bf16 v[72:75], v[168:171], v[176:179], v[72:75]\n"
    "s_add_u32 s61, s65, 24576\n"
    "s_and_b32 s61, s61, 0x1ffff\n"
    "buffer_load_dwordx4 v[216:219], v246, s[44:47], s61 offen offset:0 nt\n"
    "v_mfma_f32_16x16x32_bf16 v[76:79], v[168:171], v[180:183], v[76:79]\n"
    "v_mfma_f32_16x16x32_bf16 v[80:83], v[168:171], v[184:187], v[80:83]\n"
    "ds_read_b64_tr_b16 v[168:169], v136 offset:18688\n"
    "ds_read_b64_tr_b16 v[170:171], v136 offset:19200\n"
    "v_mfma_f32_16x16x32_bf16 v[84:87], v[172:175], v[176:179], v[84:87]\n"
    "s_sleep 1\n"
    "v_mfma_f32_16x16x32_bf16 v[88:91], v[172:175], v[180:183], v[88:91]\n"
    "v_mfma_f32_16x16x32_bf16 v[92:95], v[172:175], v[184:187], v[92:95]\n"
    "ds_read_b64_tr_b16 v[172:173], v137 offset:18688\n"
    "ds_read_b64_tr_b16 v[174:175], v137 offset:19200\n"
    "s_waitcnt lgkmcnt(14)\n"
    "v_mfma_f32_16x16x32_bf16 v[0:3], v[144:147], v[188:191], v[0:3]\n"
    "v_mfma_f32_16x16x32_bf16 v[4:7], v[144:147], v[192:195], v[4:7]\n"
    "v_mfma_f32_16x16x32_bf16 v[8:11], v[144:147], v[196:199], v[8:11]\n"
    "s_waitcnt lgkmcnt(12)\n"
    "v_mfma_f32_16x16x32_bf16 v[12:15], v[148:151], v[188:191], v[12:15]\n"
    "s_sleep 1\n"
    "v_mfma_f32_16x16x32_bf16 v[16:19], v[148:151], v[192:195], v[16:19]\n"
    "v_mfma_f32_16x16x32_bf16 v[20:23], v[148:151], v[196:199], v[20:23]\n"
    "s_waitcnt lgkmcnt(8)\n"
    "v_mfma_f32_16x16x32_bf16 v[24:27], v[152:155], v[188:191], v[24:27]\n"
    "v_mfma_f32_16x16x32_bf16 v[28:31], v[152:155], v[192:195], v[28:31]\n"
    "v_mfma_f32_16x16x32_bf16 v[32:35], v[152:155], v[196:199], v[32:35]\n"
    "s_waitcnt lgkmcnt(6)\n"
    "v_mfma_f32_16x16x32_bf16 v[36:39], v[156:159], v[188:191], v[36:39]\n"
    "s_sleep 1\n"
    "v_mfma_f32_16x16x32_bf16 v[40:43], v[156:159], v[192:195], v[40:43]\n"
    "v_mfma_f32_16x16x32_bf16 v[44:47], v[156:159], v[196:199], v[44:47]\n"
    "s_waitcnt vmcnt(4) lgkmcnt(0)\n"
    "s_barrier\n"
    "ds_read_b128 v[176:179], v138 offset:24576\n"
    "v_mfma_f32_16x16x32_bf16 v[48:51], v[160:163], v[188:191], v[48:51]\n"
    "ds_read_b128 v[180:183], v138 offset:26624\n"
    "ds_read_b64_tr_b16 v[144:145], v136 offset:32768\n"
    "ds_read_b64_tr_b16 v[146:147], v136 offset:33280\n"
    "s_add_u32 m0, s57, 98304\n"
    "s_mov_b32 s60, 4194304\n"
    "buffer_load_dwordx4 v140, s[40:43], s60 offen offset:0 lds\n"
    "s_mov_b32 s60, 4324352\n"
    "v_mfma_f32_16x16x32_bf16 v[52:55], v[160:163], v[192:195], v[52:55]\n"
    "ds_read_b128 v[184:187], v138 offset:28672\n"
    "ds_read_b64_tr_b16 v[148:149], v137 offset:32768\n"
    "ds_read_b64_tr_b16 v[150:151], v137 offset:33280\n"
    "v_mfma_f32_16x16x32_bf16 v[56:59], v[160:163], v[196:199], v[56:59]\n"
    "ds_read_b64_tr_b16 v[160:161], v136 offset:34816\n"
    "ds_read_b64_tr_b16 v[162:163], v136 offset:35328\n"
    "v_mfma_f32_16x16x32_bf16 v[60:63], v[164:167], v[188:191], v[60:63]\n"
    "s_sleep 1\n"
    "ds_read_b64_tr_b16 v[152:153], v136 offset:33024\n"
    "ds_read_b64_tr_b16 v[154:155], v136 offset:33536\n"
    "buffer_load_dwordx4 v140, s[40:43], s60 offen offset:1024 lds\n"
    "v_mfma_f32_16x16x32_bf16 v[64:67], v[164:167], v[192:195], v[64:67]\n"
    "ds_read_b64_tr_b16 v[156:157], v137 offset:33024\n"
    "ds_read_b64_tr_b16 v[158:159], v137 offset:33536\n"
    "v_mfma_f32_16x16x32_bf16 v[68:71], v[164:167], v[196:199], v[68:71]\n"
    "ds_read_b64_tr_b16 v[164:165], v137 offset:34816\n"
    "ds_read_b64_tr_b16 v[166:167], v137 offset:35328\n"
    "v_mfma_f32_16x16x32_bf16 v[72:75], v[168:171], v[188:191], v[72:75]\n"
    "s_add_u32 m0, s58, 32768\n"
    "s_add_u32 s60, s59, 1024\n"
    "buffer_load_dwordx4 v141, s[36:39], s60 offen offset:0 lds\n"
    "s_add_u32 s60, s59, 10240\n"
    "v_mfma_f32_16x16x32_bf16 v[76:79], v[168:171], v[192:195], v[76:79]\n"
    "v_mfma_f32_16x16x32_bf16 v[80:83], v[168:171], v[196:199], v[80:83]\n"
    "ds_read_b64_tr_b16 v[168:169], v136 offset:35072\n"
    "ds_read_b64_tr_b16 v[170:171], v136 offset:35584\n"
    "v_mfma_f32_16x16x32_bf16 v[84:87], v[172:175], v[188:191], v[84:87]\n"
    "s_sleep 1\n"
    "buffer_load_dwordx4 v142, s[36:39], s60 offen offset:1024 lds\n"
    "s_add_u32 s60, s59, 19456\n"
    "v_mfma_f32_16x16x32_bf16 v[88:91], v[172:175], v[192:195], v[88:91]\n"
    "v_mfma_f32_16x16x32_bf16 v[92:95], v[172:175], v[196:199], v[92:95]\n"
    "ds_read_b64_tr_b16 v[172:173], v137 offset:35072\n"
    "ds_read_b64_tr_b16 v[174:175], v137 offset:35584\n"
    "s_waitcnt lgkmcnt(14)\n"
    "v_mfma_f32_16x16x32_bf16 v[0:3], v[144:147], v[176:179], v[0:3]\n"
    "buffer_load_dwordx4 v141, s[36:39], s60 offen offset:2048 lds\n"
    "v_mfma_f32_16x16x32_bf16 v[4:7], v[144:147], v[180:183], v[4:7]\n"
    "v_mfma_f32_16x16x32_bf16 v[8:11], v[144:147], v[184:187], v[8:11]\n"
    "s_waitcnt lgkmcnt(12)\n"
    "v_mfma_f32_16x16x32_bf16 v[12:15], v[148:151], v[176:179], v[12:15]\n"
    "s_sleep 1\n"
    "s_add_u32 s61, s65, 24576\n"
    "s_and_b32 s61, s61, 0x1ffff\n"
    "s_add_u32 s61, s61, 130048\n"
    "buffer_load_dwordx4 v[220:223], v246, s[44:47], s61 offen offset:1024 nt\n"
    "v_mfma_f32_16x16x32_bf16 v[16:19], v[148:151], v[180:183], v[16:19]\n"
    "v_mfma_f32_16x16x32_bf16 v[20:23], v[148:151], v[184:187], v[20:23]\n"
    "s_waitcnt lgkmcnt(8)\n"
    "v_mfma_f32_16x16x32_bf16 v[24:27], v[152:155], v[176:179], v[24:27]\n"
    "v_mfma_f32_16x16x32_bf16 v[28:31], v[152:155], v[180:183], v[28:31]\n"
    "v_mfma_f32_16x16x32_bf16 v[32:35], v[152:155], v[184:187], v[32:35]\n"
    "s_waitcnt lgkmcnt(6)\n"
    "v_mfma_f32_16x16x32_bf16 v[36:39], v[156:159], v[176:179], v[36:39]\n"
    "s_sleep 1\n"
    "v_mfma_f32_16x16x32_bf16 v[40:43], v[156:159], v[180:183], v[40:43]\n"
    "v_mfma_f32_16x16x32_bf16 v[44:47], v[156:159], v[184:187], v[44:47]\n"
    "s_waitcnt vmcnt(7) lgkmcnt(0)\n"
    "s_barrier\n"
    "ds_read_b128 v[188:191], v139 offset:24576\n"
    "v_mfma_f32_16x16x32_bf16 v[48:51], v[160:163], v[176:179], v[48:51]\n"
    "ds_read_b128 v[192:195], v139 offset:26624\n"
    "ds_read_b64_tr_b16 v[144:145], v136 offset:0\n"
    "ds_read_b64_tr_b16 v[146:147], v136 offset:512\n"
    "s_add_u32 m0, s57, 114688\n"
    "s_mov_b32 s60, 4456448\n"
    "buffer_load_dwordx4 v140, s[40:43], s60 offen offset:0 lds\n"
    "s_mov_b32 s60, 4586496\n"
    "v_mfma_f32_16x16x32_bf16 v[52:55], v[160:163], v[180:183], v[52:55]\n"
    "ds_read_b128 v[196:199], v139 offset:28672\n"
    "ds_read_b64_tr_b16 v[148:149], v137 offset:0\n"
    "ds_read_b64_tr_b16 v[150:151], v137 offset:512\n"
    "v_mfma_f32_16x16x32_bf16 v[56:59], v[160:163], v[184:187], v[56:59]\n"
    "ds_read_b64_tr_b16 v[160:161], v136 offset:2048\n"
    "ds_read_b64_tr_b16 v[162:163], v136 offset:2560\n"
    "v_mfma_f32_16x16x32_bf16 v[60:63], v[164:167], v[176:179], v[60:63]\n"
    "s_sleep 1\n"
    "ds_read_b64_tr_b16 v[152:153], v136 offset:256\n"
    "ds_read_b64_tr_b16 v[154:155], v136 offset:768\n"
    "buffer_load_dwordx4 v140, s[40:43], s60 offen offset:1024 lds\n"
    "v_mfma_f32_16x16x32_bf16 v[64:67], v[164:167], v[180:183], v[64:67]\n"
    "ds_read_b64_tr_b16 v[156:157], v137 offset:256\n"
    "ds_read_b64_tr_b16 v[158:159], v137 offset:768\n"
    "v_mfma_f32_16x16x32_bf16 v[68:71], v[164:167], v[184:187], v[68:71]\n"
    "ds_read_b64_tr_b16 v[164:165], v137 offset:2048\n"
    "ds_read_b64_tr_b16 v[166:167], v137 offset:2560\n"
    "v_mfma_f32_16x16x32_bf16 v[72:75], v[168:171], v[176:179], v[72:75]\n"
    "s_add_u32 s61, s65, 24576\n"
    "s_and_b32 s61, s61, 0x1ffff\n"
    "buffer_load_dwordx4 v[224:227], v247, s[48:51], s61 offen offset:2048 nt\n"
    "v_mfma_f32_16x16x32_bf16 v[76:79], v[168:171], v[180:183], v[76:79]\n"
    "v_mfma_f32_16x16x32_bf16 v[80:83], v[168:171], v[184:187], v[80:83]\n"
    "ds_read_b64_tr_b16 v[168:169], v136 offset:2304\n"
    "ds_read_b64_tr_b16 v[170:171], v136 offset:2816\n"
    "v_mfma_f32_16x16x32_bf16 v[84:87], v[172:175], v[176:179], v[84:87]\n"
    "s_sleep 1\n"
    "v_mfma_f32_16x16x32_bf16 v[88:91], v[172:175], v[180:183], v[88:91]\n"
    "v_mfma_f32_16x16x32_bf16 v[92:95], v[172:175], v[184:187], v[92:95]\n"
    "ds_read_b64_tr_b16 v[172:173], v137 offset:2304\n"
    "ds_read_b64_tr_b16 v[174:175], v137 offset:2816\n"
    "s_waitcnt lgkmcnt(14)\n"
    "v_mfma_f32_16x16x32_bf16 v[0:3], v[144:147], v[188:191], v[0:3]\n"
    "v_mfma_f32_16x16x32_bf16 v[4:7], v[144:147], v[192:195], v[4:7]\n"
    "v_mfma_f32_16x16x32_bf16 v[8:11], v[144:147], v[196:199], v[8:11]\n"
    "s_waitcnt lgkmcnt(12)\n"
    "v_mfma_f32_16x16x32_bf16 v[12:15], v[148:151], v[188:191], v[12:15]\n"
    "s_sleep 1\n"
    "v_mfma_f32_16x16x32_bf16 v[16:19], v[148:151], v[192:195], v[16:19]\n"
    "v_mfma_f32_16x16x32_bf16 v[20:23], v[148:151], v[196:199], v[20:23]\n"
    "s_waitcnt lgkmcnt(8)\n"
    "v_mfma_f32_16x16x32_bf16 v[24:27], v[152:155], v[188:191], v[24:27]\n"
    "v_mfma_f32_16x16x32_bf16 v[28:31], v[152:155], v[192:195], v[28:31]\n"
    "v_mfma_f32_16x16x32_bf16 v[32:35], v[152:155], v[196:199], v[32:35]\n"
    "s_waitcnt lgkmcnt(6)\n"
    "v_mfma_f32_16x16x32_bf16 v[36:39], v[156:159], v[188:191], v[36:39]\n"
    "s_sleep 1\n"
    "v_mfma_f32_16x16x32_bf16 v[40:43], v[156:159], v[192:195], v[40:43]\n"
    "v_mfma_f32_16x16x32_bf16 v[44:47], v[156:159], v[196:199], v[44:47]\n"
    "s_waitcnt vmcnt(4) lgkmcnt(0)\n"
    "s_barrier\n"
    "ds_read_b128 v[176:179], v138 offset:0\n"
    "v_mfma_f32_16x16x32_bf16 v[48:51], v[160:163], v[188:191], v[48:51]\n"
    "ds_read_b128 v[180:183], v138 offset:2048\n"
    "ds_read_b64_tr_b16 v[144:145], v136 offset:16384\n"
    "ds_read_b64_tr_b16 v[146:147], v136 offset:16896\n"
    "s_add_u32 m0, s57, 81920\n"
    "s_mov_b32 s60, 4718592\n"
    "buffer_load_dwordx4 v140, s[40:43], s60 offen offset:0 lds\n"
    "s_mov_b32 s60, 4848640\n"
    "v_mfma_f32_16x16x32_bf16 v[52:55], v[160:163], v[192:195], v[52:55]\n"
    "ds_read_b128 v[184:187], v138 offset:4096\n"
    "ds_read_b64_tr_b16 v[148:149], v137 offset:16384\n"
    "ds_read_b64_tr_b16 v[150:151], v137 offset:16896\n"
    "v_mfma_f32_16x16x32_bf16 v[56:59], v[160:163], v[196:199], v[56:59]\n"
    "ds_read_b64_tr_b16 v[160:161], v136 offset:18432\n"
    "ds_read_b64_tr_b16 v[162:163], v136 offset:18944\n"
    "v_mfma_f32_16x16x32_bf16 v[60:63], v[164:167], v[188:191], v[60:63]\n"
    "s_sleep 1\n"
    "ds_read_b64_tr_b16 v[152:153], v136 offset:16640\n"
    "ds_read_b64_tr_b16 v[154:155], v136 offset:17152\n"
    "buffer_load_dwordx4 v140, s[40:43], s60 offen offset:1024 lds\n"
    "v_mfma_f32_16x16x32_bf16 v[64:67], v[164:167], v[192:195], v[64:67]\n"
    "ds_read_b64_tr_b16 v[156:157], v137 offset:16640\n"
    "ds_read_b64_tr_b16 v[158:159], v137 offset:17152\n"
    "v_mfma_f32_16x16x32_bf16 v[68:71], v[164:167], v[196:199], v[68:71]\n"
    "ds_read_b64_tr_b16 v[164:165], v137 offset:18432\n"
    "ds_read_b64_tr_b16 v[166:167], v137 offset:18944\n"
    "v_mfma_f32_16x16x32_bf16 v[72:75], v[168:171], v[188:191], v[72:75]\n"
    "s_add_u32 m0, s58, 57344\n"
    "s_add_u32 s60, s59, 1152\n"
    "buffer_load_dwordx4 v141, s[36:39], s60 offen offset:0 lds\n"
    "s_add_u32 s60, s59, 10368\n"
    "v_mfma_f32_16x16x32_bf16 v[76:79], v[168:171], v[192:195], v[76:79]\n"
    "v_mfma_f32_16x16x32_bf16 v[80:83], v[168:171], v[196:199], v[80:83]\n"
    "ds_read_b64_tr_b16 v[168:169], v136 offset:18688\n"
    "ds_read_b64_tr_b16 v[170:171], v136 offset:19200\n"
    "v_mfma_f32_16x16x32_bf16 v[84:87], v[172:175], v[188:191], v[84:87]\n"
    "s_sleep 1\n"
    "buffer_load_dwordx4 v142, s[36:39], s60 offen offset:1024 lds\n"
    "s_add_u32 s60, s59, 19584\n"
    "v_mfma_f32_16x16x32_bf16 v[88:91], v[172:175], v[192:195], v[88:91]\n"
    "v_mfma_f32_16x16x32_bf16 v[92:95], v[172:175], v[196:199], v[92:95]\n"
    "ds_read_b64_tr_b16 v[172:173], v137 offset:18688\n"
    "ds_read_b64_tr_b16 v[174:175], v137 offset:19200\n"
    "s_waitcnt lgkmcnt(14)\n"
    "v_mfma_f32_16x16x32_bf16 v[0:3], v[144:147], v[176:179], v[0:3]\n"
    "buffer_load_dwordx4 v141, s[36:39], s60 offen offset:2048 lds\n"
    "v_mfma_f32_16x16x32_bf16 v[4:7], v[144:147], v[180:183], v[4:7]\n"
    "v_mfma_f32_16x16x32_bf16 v[8:11], v[144:147], v[184:187], v[8:11]\n"
    "s_waitcnt lgkmcnt(12)\n"
    "v_mfma_f32_16x16x32_bf16 v[12:15], v[148:151], v[176:179], v[12:15]\n"
    "s_sleep 1\n"
    "s_add_u32 s61, s65, 24576\n"
    "s_and_b32 s61, s61, 0x1ffff\n"
    "s_add_u32 s61, s61, 130048\n"
    "buffer_load_dwordx4 v[228:231], v247, s[48:51], s61 offen offset:3072 nt\n"
    "v_mfma_f32_16x16x32_bf16 v[16:19], v[148:151], v[180:183], v[16:19]\n"
    "v_mfma_f32_16x16x32_bf16 v[20:23], v[148:151], v[184:187], v[20:23]\n"
    "s_waitcnt lgkmcnt(8)\n"
    "v_mfma_f32_16x16x32_bf16 v[24:27], v[152:155], v[176:179], v[24:27]\n"
    "v_mfma_f32_16x16x32_bf16 v[28:31], v[152:155], v[180:183], v[28:31]\n"
    "v_mfma_f32_16x16x32_bf16 v[32:35], v[152:155], v[184:187], v[32:35]\n"
    "s_waitcnt lgkmcnt(6)\n"
    "v_mfma_f32_16x16x32_bf16 v[36:39], v[156:159], v[176:179], v[36:39]\n"
    "s_sleep 1\n"
    "v_mfma_f32_16x16x32_bf16 v[40:43], v[156:159], v[180:183], v[40:43]\n"
    "v_mfma_f32_16x16x32_bf16 v[44:47], v[156:159], v[184:187], v[44:47]\n"
    "s_waitcnt vmcnt(7) lgkmcnt(0)\n"
    "s_barrier\n"
    "ds_read_b128 v[188:191], v139 offset:0\n"
    "v_mfma_f32_16x16x32_bf16 v[48:51], v[160:163], v[176:179], v[48:51]\n"
    "ds_read_b128 v[192:195], v139 offset:2048\n"
    "ds_read_b64_tr_b16 v[144:145], v136 offset:32768\n"
    "ds_read_b64_tr_b16 v[146:147], v136 offset:33280\n"
    "s_add_u32 m0, s57, 98304\n"
    "s_mov_b32 s60, 4980736\n"
    "buffer_load_dwordx4 v140, s[40:43], s60 offen offset:0 lds\n"
    "s_mov_b32 s60, 5110784\n"
    "v_mfma_f32_16x16x32_bf16 v[52:55], v[160:163], v[180:183], v[52:55]\n"
    "ds_read_b128 v[196:199], v139 offset:4096\n"
    "ds_read_b64_tr_b16 v[148:149], v137 offset:32768\n"
    "ds_read_b64_tr_b16 v[150:151], v137 offset:33280\n"
    "v_mfma_f32_16x16x32_bf16 v[56:59], v[160:163], v[184:187], v[56:59]\n"
    "ds_read_b64_tr_b16 v[160:161], v136 offset:34816\n"
    "ds_read_b64_tr_b16 v[162:163], v136 offset:35328\n"
    "v_mfma_f32_16x16x32_bf16 v[60:63], v[164:167], v[176:179], v[60:63]\n"
    "s_sleep 1\n"
    "ds_read_b64_tr_b16 v[152:153], v136 offset:33024\n"
    "ds_read_b64_tr_b16 v[154:155], v136 offset:33536\n"
    "buffer_load_dwordx4 v140, s[40:43], s60 offen offset:1024 lds\n"
    "v_mfma_f32_16x16x32_bf16 v[64:67], v[164:167], v[180:183], v[64:67]\n"
    "ds_read_b64_tr_b16 v[156:157], v137 offset:33024\n"
    "ds_read_b64_tr_b16 v[158:159], v137 offset:33536\n"
    "v_mfma_f32_16x16x32_bf16 v[68:71], v[164:167], v[184:187], v[68:71]\n"
    "ds_read_b64_tr_b16 v[164:165], v137 offset:34816\n"
    "ds_read_b64_tr_b16 v[166:167], v137 offset:35328\n"
    "v_mfma_f32_16x16x32_bf16 v[72:75], v[168:171], v[176:179], v[72:75]\n"
    "s_add_u32 s61, s65, 32768\n"
    "s_and_b32 s61, s61, 0x1ffff\n"
    "buffer_load_dwordx4 v[232:235], v246, s[44:47], s61 offen offset:0 nt\n"
    "v_mfma_f32_16x16x32_bf16 v[76:79], v[168:171], v[180:183], v[76:79]\n"
    "v_mfma_f32_16x16x32_bf16 v[80:83], v[168:171], v[184:187], v[80:83]\n"
    "ds_read_b64_tr_b16 v[168:169], v136 offset:35072\n"
    "ds_read_b64_tr_b16 v[170:171], v136 offset:35584\n"
    "v_mfma_f32_16x16x32_bf16 v[84:87], v[172:175], v[176:179], v[84:87]\n"
    "s_sleep 1\n"
    "s_add_u32 s61, s65, 32768\n"
    "s_and_b32 s61, s61, 0x1ffff\n"
    "s_add_u32 s61, s61, 130048\n"
    "buffer_load_dwordx4 v[236:239], v246, s[44:47], s61 offen offset:1024 nt\n"
    "v_mfma_f32_16x16x32_bf16 v[88:91], v[172:175], v[180:183], v[88:91]\n"
    "v_mfma_f32_16x16x32_bf16 v[92:95], v[172:175], v[184:187], v[92:95]\n"
    "ds_read_b64_tr_b16 v[172:173], v137 offset:35072\n"
    "ds_read_b64_tr_b16 v[174:175], v137 offset:35584\n"
    "s_waitcnt lgkmcnt(14)\n"
    "v_mfma_f32_16x16x32_bf16 v[0:3], v[144:147], v[188:191], v[0:3]\n"
    "v_mfma_f32_16x16x32_bf16 v[4:7], v[144:147], v[192:195], v[4:7]\n"
    "v_mfma_f32_16x16x32_bf16 v[8:11], v[144:147], v[196:199], v[8:11]\n"
    "s_waitcnt lgkmcnt(12)\n"
    "v_mfma_f32_16x16x32_bf16 v[12:15], v[148:151], v[188:191], v[12:15]\n"
    "s_sleep 1\n"
    "v_mfma_f32_16x16x32_bf16 v[16:19], v[148:151], v[192:195], v[16:19]\n"
    "v_mfma_f32_16x16x32_bf16 v[20:23], v[148:151], v[196:199], v[20:23]\n"
    "s_waitcnt lgkmcnt(8)\n"
    "v_mfma_f32_16x16x32_bf16 v[24:27], v[152:155], v[188:191], v[24:27]\n"
    "v_mfma_f32_16x16x32_bf16 v[28:31], v[152:155], v[192:195], v[28:31]\n"
    "v_mfma_f32_16x16x32_bf16 v[32:35], v[152:155], v[196:199], v[32:35]\n"
    "s_waitcnt lgkmcnt(6)\n"
    "v_mfma_f32_16x16x32_bf16 v[36:39], v[156:159], v[188:191], v[36:39]\n"
    "s_sleep 1\n"
    "v_mfma_f32_16x16x32_bf16 v[40:43], v[156:159], v[192:195], v[40:43]\n"
    "v_mfma_f32_16x16x32_bf16 v[44:47], v[156:159], v[196:199], v[44:47]\n"
    "s_waitcnt vmcnt(5) lgkmcnt(0)\n"
    "s_barrier\n"
    "ds_read_b128 v[176:179], v138 offset:24576\n"
    "v_mfma_f32_16x16x32_bf16 v[48:51], v[160:163], v[188:191], v[48:51]\n"
    "ds_read_b128 v[180:183], v138 offset:26624\n"
    "ds_read_b64_tr_b16 v[144:145], v136 offset:0\n"
    "ds_read_b64_tr_b16 v[146:147], v136 offset:512\n"
    "v_mfma_f32_16x16x32_bf16 v[52:55], v[160:163], v[192:195], v[52:55]\n"
    "ds_read_b128 v[184:187], v138 offset:28672\n"
    "ds_read_b64_tr_b16 v[148:149], v137 offset:0\n"
    "ds_read_b64_tr_b16 v[150:151], v137 offset:512\n"
    "v_mfma_f32_16x16x32_bf16 v[56:59], v[160:163], v[196:199], v[56:59]\n"
    "ds_read_b64_tr_b16 v[160:161], v136 offset:2048\n"
    "ds_read_b64_tr_b16 v[162:163], v136 offset:2560\n"
    "v_mfma_f32_16x16x32_bf16 v[60:63], v[164:167], v[188:191], v[60:63]\n"
    "s_sleep 1\n"
    "ds_read_b64_tr_b16 v[152:153], v136 offset:256\n"
    "ds_read_b64_tr_b16 v[154:155], v136 offset:768\n"
    "v_mfma_f32_16x16x32_bf16 v[64:67], v[164:167], v[192:195], v[64:67]\n"
    "ds_read_b64_tr_b16 v[156:157], v137 offset:256\n"
    "ds_read_b64_tr_b16 v[158:159], v137 offset:768\n"
    "v_mfma_f32_16x16x32_bf16 v[68:71], v[164:167], v[196:199], v[68:71]\n"
    "ds_read_b64_tr_b16 v[164:165], v137 offset:2048\n"
    "ds_read_b64_tr_b16 v[166:167], v137 offset:2560\n"
    "v_mfma_f32_16x16x32_bf16 v[72:75], v[168:171], v[188:191], v[72:75]\n"
    "v_mfma_f32_16x16x32_bf16 v[76:79], v[168:171], v[192:195], v[76:79]\n"
    "v_mfma_f32_16x16x32_bf16 v[80:83], v[168:171], v[196:199], v[80:83]\n"
    "ds_read_b64_tr_b16 v[168:169], v136 offset:2304\n"
    "ds_read_b64_tr_b16 v[170:171], v136 offset:2816\n"
    "v_mfma_f32_16x16x32_bf16 v[84:87], v[172:175], v[188:191], v[84:87]\n"
    "s_sleep 1\n"
    "v_mfma_f32_16x16x32_bf16 v[88:91], v[172:175], v[192:195], v[88:91]\n"
    "v_mfma_f32_16x16x32_bf16 v[92:95], v[172:175], v[196:199], v[92:95]\n"
    "ds_read_b64_tr_b16 v[172:173], v137 offset:2304\n"
    "ds_read_b64_tr_b16 v[174:175], v137 offset:2816\n"
    "s_waitcnt lgkmcnt(14)\n"
    "v_mfma_f32_16x16x32_bf16 v[0:3], v[144:147], v[176:179], v[0:3]\n"
    "v_mfma_f32_16x16x32_bf16 v[4:7], v[144:147], v[180:183], v[4:7]\n"
    "v_mfma_f32_16x16x32_bf16 v[8:11], v[144:147], v[184:187], v[8:11]\n"
    "v_add_u32_e32 v242, 131072, v240\n"
    "s_waitcnt lgkmcnt(12)\n"
    "v_mfma_f32_16x16x32_bf16 v[12:15], v[148:151], v[176:179], v[12:15]\n"
    "v_xor_b32_e32 v243, 64, v242\n"
    "v_mfma_f32_16x16x32_bf16 v[16:19], v[148:151], v[180:183], v[16:19]\n"
    "v_add_u32_e32 v244, 131072, v241\n"
    "v_mfma_f32_16x16x32_bf16 v[20:23], v[148:151], v[184:187], v[20:23]\n"
    "v_xor_b32_e32 v245, 32, v244\n"
    "s_waitcnt lgkmcnt(8)\n"
    "v_mfma_f32_16x16x32_bf16 v[24:27], v[152:155], v[176:179], v[24:27]\n"
    "v_mov_b32_e32 v240, v135\n"
    "v_mfma_f32_16x16x32_bf16 v[28:31], v[152:155], v[180:183], v[28:31]\n"
    "v_mfma_f32_16x16x32_bf16 v[32:35], v[152:155], v[184:187], v[32:35]\n"
    "s_add_u32 s61, s65, 32768\n"
    "s_and_b32 s61, s61, 0x1ffff\n"
    "buffer_load_dword v241, v240, s[48:51], s61 offen offset:2048\n"
    "s_waitcnt lgkmcnt(6)\n"
    "v_mfma_f32_16x16x32_bf16 v[36:39], v[156:159], v[176:179], v[36:39]\n"
    "v_mfma_f32_16x16x32_bf16 v[40:43], v[156:159], v[180:183], v[40:43]\n"
    "v_mfma_f32_16x16x32_bf16 v[44:47], v[156:159], v[184:187], v[44:47]\n"
    "s_waitcnt vmcnt(3) lgkmcnt(0)\n"
    "s_barrier\n"
    "s_mov_b32 s62, 0x3e0293ee\n"
    "ds_read_b128 v[188:191], v139 offset:24576\n"
    "ds_read_b64_tr_b16 v[144:145], v136 offset:16384\n"
    "ds_read_b64_tr_b16 v[146:147], v136 offset:16896\n"
    "v_mfma_f32_16x16x32_bf16 v[48:51], v[160:163], v[176:179], v[48:51]\n"
    "ds_read_b128 v[192:195], v139 offset:26624\n"
    "ds_read_b64_tr_b16 v[148:149], v137 offset:16384\n"
    "ds_read_b64_tr_b16 v[150:151], v137 offset:16896\n"
    "v_mfma_f32_16x16x32_bf16 v[52:55], v[160:163], v[180:183], v[52:55]\n"
    "ds_read_b128 v[196:199], v139 offset:28672\n"
    "ds_read_b64_tr_b16 v[152:153], v136 offset:16640\n"
    "ds_read_b64_tr_b16 v[154:155], v136 offset:17152\n"
    "v_mfma_f32_16x16x32_bf16 v[56:59], v[160:163], v[184:187], v[56:59]\n"
    "ds_read_b64_tr_b16 v[160:161], v136 offset:18432\n"
    "ds_read_b64_tr_b16 v[162:163], v136 offset:18944\n"
    "v_mfma_f32_16x16x32_bf16 v[60:63], v[164:167], v[176:179], v[60:63]\n"
    "ds_read_b64_tr_b16 v[156:157], v137 offset:16640\n"
    "ds_read_b64_tr_b16 v[158:159], v137 offset:17152\n"
    "v_mfma_f32_16x16x32_bf16 v[64:67], v[164:167], v[180:183], v[64:67]\n"
    "v_mfma_f32_16x16x32_bf16 v[68:71], v[164:167], v[184:187], v[68:71]\n"
    "ds_read_b64_tr_b16 v[164:165], v137 offset:18432\n"
    "ds_read_b64_tr_b16 v[166:167], v137 offset:18944\n"
    "s_waitcnt lgkmcnt(12)\n"
    "v_mfma_f32_16x16x32_bf16 v[0:3], v[144:147], v[188:191], v[0:3]\n"
    "s_waitcnt lgkmcnt(11)\n"
    "v_mfma_f32_16x16x32_bf16 v[4:7], v[144:147], v[192:195], v[4:7]\n"
    "s_waitcnt lgkmcnt(8)\n"
    "v_mfma_f32_16x16x32_bf16 v[8:11], v[144:147], v[196:199], v[8:11]\n"
    "ds_read_b128 v[144:147], v242 offset:0\n"
    "v_mfma_f32_16x16x32_bf16 v[12:15], v[148:151], v[188:191], v[12:15]\n"
    "v_mfma_f32_16x16x32_bf16 v[16:19], v[148:151], v[192:195], v[16:19]\n"
    "v_mfma_f32_16x16x32_bf16 v[20:23], v[148:151], v[196:199], v[20:23]\n"
    "ds_read_b128 v[148:151], v243 offset:0\n"
    "v_mfma_f32_16x16x32_bf16 v[72:75], v[168:171], v[176:179], v[72:75]\n"
    "v_mfma_f32_16x16x32_bf16 v[76:79], v[168:171], v[180:183], v[76:79]\n"
    "v_mfma_f32_16x16x32_bf16 v[80:83], v[168:171], v[184:187], v[80:83]\n"
    "ds_read_b64_tr_b16 v[168:169], v136 offset:18688\n"
    "ds_read_b64_tr_b16 v[170:171], v136 offset:19200\n"
    "v_mul_f32_e32 v0, s62, v0\n"
    "v_mul_f32_e32 v1, s62, v1\n"
    "v_mul_f32_e32 v2, s62, v2\n"
    "v_mul_f32_e32 v3, s62, v3\n"
    "s_waitcnt lgkmcnt(10)\n"
    "v_mfma_f32_16x16x32_bf16 v[24:27], v[152:155], v[188:191], v[24:27]\n"
    "v_mul_f32_e32 v12, s62, v12\n"
    "v_mul_f32_e32 v13, s62, v13\n"
    "v_mul_f32_e32 v14, s62, v14\n"
    "v_mul_f32_e32 v15, s62, v15\n"
    "v_mfma_f32_16x16x32_bf16 v[28:31], v[152:155], v[192:195], v[28:31]\n"
    "v_cvt_pk_bf16_f32 v0, v0, v1\n"
    "v_cvt_pk_bf16_f32 v1, v2, v3\n"
    "v_cvt_pk_bf16_f32 v12, v12, v13\n"
    "v_cvt_pk_bf16_f32 v13, v14, v15\n"
    "v_mfma_f32_16x16x32_bf16 v[32:35], v[152:155], v[196:199], v[32:35]\n"
    "ds_read_b128 v[152:155], v242 offset:4096\n"
    "v_cndmask_b32_e64 v96, v0, v12, s[74:75]\n"
    "v_cndmask_b32_e64 v98, v12, v0, s[74:75]\n"
    "v_cndmask_b32_e64 v97, v1, v13, s[74:75]\n"
    "v_cndmask_b32_e64 v99, v13, v1, s[74:75]\n"
    "v_mfma_f32_16x16x32_bf16 v[84:87], v[172:175], v[176:179], v[84:87]\n"
    "v_mul_f32_e32 v4, s62, v4\n"
    "v_mul_f32_e32 v5, s62, v5\n"
    "v_mul_f32_e32 v6, s62, v6\n"
    "v_mul_f32_e32 v7, s62, v7\n"
    "v_mfma_f32_16x16x32_bf16 v[88:91], v[172:175], v[180:183], v[88:91]\n"
    "v_mul_f32_e32 v16, s62, v16\n"
    "v_mul_f32_e32 v17, s62, v17\n"
    "v_mul_f32_e32 v18, s62, v18\n"
    "v_mul_f32_e32 v19, s62, v19\n"
    "v_mfma_f32_16x16x32_bf16 v[92:95], v[172:175], v[184:187], v[92:95]\n"
    "ds_read_b64_tr_b16 v[172:173], v137 offset:18688\n"
    "ds_read_b64_tr_b16 v[174:175], v137 offset:19200\n"
    "v_cvt_pk_bf16_f32 v4, v4, v5\n"
    "v_cvt_pk_bf16_f32 v5, v6, v7\n"
    "v_cvt_pk_bf16_f32 v16, v16, v17\n"
    "v_cvt_pk_bf16_f32 v17, v18, v19\n"
    "s_waitcnt lgkmcnt(9)\n"
    "v_mfma_f32_16x16x32_bf16 v[36:39], v[156:159], v[188:191], v[36:39]\n"
    "v_cndmask_b32_e64 v100, v4, v16, s[74:75]\n"
    "v_cndmask_b32_e64 v102, v16, v4, s[74:75]\n"
    "v_cndmask_b32_e64 v101, v5, v17, s[74:75]\n"
    "v_cndmask_b32_e64 v103, v17, v5, s[74:75]\n"
    "v_mfma_f32_16x16x32_bf16 v[40:43], v[156:159], v[192:195], v[40:43]\n"
    "v_mul_f32_e32 v8, s62, v8\n"
    "v_mul_f32_e32 v9, s62, v9\n"
    "v_mul_f32_e32 v10, s62, v10\n"
    "v_mul_f32_e32 v11, s62, v11\n"
    "v_mfma_f32_16x16x32_bf16 v[44:47], v[156:159], v[196:199], v[44:47]\n"
    "ds_read_b128 v[156:159], v243 offset:4096\n"
    "v_mul_f32_e32 v20, s62, v20\n"
    "v_mul_f32_e32 v21, s62, v21\n"
    "v_mul_f32_e32 v22, s62, v22\n"
    "v_mul_f32_e32 v23, s62, v23\n"
    "v_mfma_f32_16x16x32_bf16 v[48:51], v[160:163], v[188:191], v[48:51]\n"
    "v_cvt_pk_bf16_f32 v8, v8, v9\n"
    "v_cvt_pk_bf16_f32 v9, v10, v11\n"
    "v_cvt_pk_bf16_f32 v20, v20, v21\n"
    "v_cvt_pk_bf16_f32 v21, v22, v23\n"
    "v_mfma_f32_16x16x32_bf16 v[52:55], v[160:163], v[192:195], v[52:55]\n"
    "s_add_u32 s61, s65, 40960\n"
    "s_and_b32 s61, s61, 0x1ffff\n"
    "buffer_load_dword v241, v240, s[44:47], s61 offen offset:0\n"
    "v_cndmask_b32_e64 v104, v8, v20, s[74:75]\n"
    "v_cndmask_b32_e64 v106, v20, v8, s[74:75]\n"
    "v_cndmask_b32_e64 v105, v9, v21, s[74:75]\n"
    "v_cndmask_b32_e64 v107, v21, v9, s[74:75]\n"
    "v_mfma_f32_16x16x32_bf16 v[56:59], v[160:163], v[196:199], v[56:59]\n"
    "s_waitcnt vmcnt(2) lgkmcnt(0)\n"
    "s_barrier\n"
    "ds_read_b128 v[160:163], v242 offset:16384\n"
    "v_add_u32_e32 v136, 65536, v143\n"
    "v_mul_f32_e32 v24, s62, v24\n"
    "v_mul_f32_e32 v25, s62, v25\n"
    "v_mul_f32_e32 v26, s62, v26\n"
    "v_mul_f32_e32 v27, s62, v27\n"
    "v_mfma_f32_16x16x32_bf16 v[60:63], v[164:167], v[188:191], v[60:63]\n"
    "ds_write_b128 v143, v[200:203] offset:0\n"
    "ds_write_b128 v143, v[204:207] offset:1024\n"
    "v_mul_f32_e32 v36, s62, v36\n"
    "v_mul_f32_e32 v37, s62, v37\n"
    "v_mul_f32_e32 v38, s62, v38\n"
    "v_mfma_f32_16x16x32_bf16 v[64:67], v[164:167], v[192:195], v[64:67]\n"
    "ds_write_b128 v143, v[208:211] offset:2048\n"
    "v_mul_f32_e32 v39, s62, v39\n"
    "v_cvt_pk_bf16_f32 v24, v24, v25\n"
    "v_cvt_pk_bf16_f32 v25, v26, v27\n"
    "v_mfma_f32_16x16x32_bf16 v[68:71], v[164:167], v[196:199], v[68:71]\n"
    "ds_read_b128 v[164:167], v243 offset:16384\n"
    "ds_write_b128 v143, v[212:215] offset:3072\n"
    "v_cvt_pk_bf16_f32 v36, v36, v37\n"
    "v_cvt_pk_bf16_f32 v37, v38, v39\n"
    "v_cndmask_b32_e64 v108, v24, v36, s[74:75]\n"
    "v_mfma_f32_16x16x32_bf16 v[72:75], v[168:171], v[188:191], v[72:75]\n"
    "ds_write_b128 v143, v[216:219] offset:32768\n"
    "v_cndmask_b32_e64 v110, v36, v24, s[74:75]\n"
    "v_cndmask_b32_e64 v109, v25, v37, s[74:75]\n"
    "v_cndmask_b32_e64 v111, v37, v25, s[74:75]\n"
    "v_mfma_f32_16x16x32_bf16 v[76:79], v[168:171], v[192:195], v[76:79]\n"
    "ds_write_b128 v143, v[220:223] offset:33792\n"
    "ds_write_b128 v143, v[224:227] offset:34816\n"
    "v_mul_f32_e32 v28, s62, v28\n"
    "v_mul_f32_e32 v29, s62, v29\n"
    "v_mul_f32_e32 v30, s62, v30\n"
    "v_mul_f32_e32 v31, s62, v31\n"
    "v_mfma_f32_16x16x32_bf16 v[80:83], v[168:171], v[196:199], v[80:83]\n"
    "ds_read_b128 v[168:171], v242 offset:20480\n"
    "ds_write_b128 v143, v[228:231] offset:35840\n"
    "v_mul_f32_e32 v40, s62, v40\n"
    "v_mul_f32_e32 v41, s62, v41\n"
    "v_mul_f32_e32 v42, s62, v42\n"
    "v_mfma_f32_16x16x32_bf16 v[84:87], v[172:175], v[188:191], v[84:87]\n"
    "ds_write_b128 v136, v[232:235] offset:0\n"
    "v_mul_f32_e32 v43, s62, v43\n"
    "v_cvt_pk_bf16_f32 v28, v28, v29\n"
    "v_cvt_pk_bf16_f32 v29, v30, v31\n"
    "v_mfma_f32_16x16x32_bf16 v[88:91], v[172:175], v[192:195], v[88:91]\n"
    "ds_write_b128 v136, v[236:239] offset:1024\n"
    "v_cvt_pk_bf16_f32 v40, v40, v41\n"
    "v_cvt_pk_bf16_f32 v41, v42, v43\n"
    "v_cndmask_b32_e64 v112, v28, v40, s[74:75]\n"
    "v_mfma_f32_16x16x32_bf16 v[92:95], v[172:175], v[196:199], v[92:95]\n"
    "ds_read_b128 v[172:175], v243 offset:20480\n"
    "v_cndmask_b32_e64 v114, v40, v28, s[74:75]\n"
    "v_cndmask_b32_e64 v113, v29, v41, s[74:75]\n"
    "v_cndmask_b32_e64 v115, v41, v29, s[74:75]\n"
    "v_mul_f32_e32 v32, s62, v32\n"
    "v_mul_f32_e32 v33, s62, v33\n"
    "v_mul_f32_e32 v34, s62, v34\n"
    "v_mul_f32_e32 v35, s62, v35\n"
    "v_mul_f32_e32 v44, s62, v44\n"
    "v_mul_f32_e32 v45, s62, v45\n"
    "v_mul_f32_e32 v46, s62, v46\n"
    "v_mul_f32_e32 v47, s62, v47\n"
    "v_cvt_pk_bf16_f32 v32, v32, v33\n"
    "v_cvt_pk_bf16_f32 v33, v34, v35\n"
    "v_cvt_pk_bf16_f32 v44, v44, v45\n"
    "v_cvt_pk_bf16_f32 v45, v46, v47\n"
    "v_cndmask_b32_e64 v116, v32, v44, s[74:75]\n"
    "v_cndmask_b32_e64 v118, v44, v32, s[74:75]\n"
    "v_cndmask_b32_e64 v117, v33, v45, s[74:75]\n"
    "v_cndmask_b32_e64 v119, v45, v33, s[74:75]\n"
    "v_mfma_f32_16x16x32_bf16 v[176:179], v[144:147], v[96:99], 0\n"
    "v_mul_f32_e32 v48, s62, v48\n"
    "v_mul_f32_e32 v49, s62, v49\n"
    "v_mul_f32_e32 v50, s62, v50\n"
    "v_mul_f32_e32 v51, s62, v51\n"
    "v_mul_f32_e32 v60, s62, v60\n"
    "v_mul_f32_e32 v61, s62, v61\n"
    "v_mul_f32_e32 v62, s62, v62\n"
    "v_mul_f32_e32 v63, s62, v63\n"
    "v_mfma_f32_16x16x32_bf16 v[180:183], v[144:147], v[100:103], 0\n"
    "v_cvt_pk_bf16_f32 v48, v48, v49\n"
    "v_cvt_pk_bf16_f32 v49, v50, v51\n"
    "v_cvt_pk_bf16_f32 v60, v60, v61\n"
    "v_cvt_pk_bf16_f32 v61, v62, v63\n"
    "v_cndmask_b32_e64 v120, v48, v60, s[74:75]\n"
    "v_cndmask_b32_e64 v122, v60, v48, s[74:75]\n"
    "v_cndmask_b32_e64 v121, v49, v61, s[74:75]\n"
    "v_cndmask_b32_e64 v123, v61, v49, s[74:75]\n"
    "v_mfma_f32_16x16x32_bf16 v[184:187], v[144:147], v[104:107], 0\n"
    "ds_read_b64_tr_b16 v[144:145], v244 offset:0\n"
    "ds_read_b64_tr_b16 v[146:147], v244 offset:16384\n"
    "v_mul_f32_e32 v52, s62, v52\n"
    "v_mul_f32_e32 v53, s62, v53\n"
    "v_mul_f32_e32 v54, s62, v54\n"
    "v_mul_f32_e32 v55, s62, v55\n"
    "v_mul_f32_e32 v64, s62, v64\n"
    "v_mul_f32_e32 v65, s62, v65\n"
    "v_mul_f32_e32 v66, s62, v66\n"
    "v_mul_f32_e32 v67, s62, v67\n"
    "v_mfma_f32_16x16x32_bf16 v[176:179], v[148:151], v[108:111], v[176:179]\n"
    "v_cvt_pk_bf16_f32 v52, v52, v53\n"
    "v_cvt_pk_bf16_f32 v53, v54, v55\n"
    "v_cvt_pk_bf16_f32 v64, v64, v65\n"
    "v_cvt_pk_bf16_f32 v65, v66, v67\n"
    "v_cndmask_b32_e64 v124, v52, v64, s[74:75]\n"
    "v_cndmask_b32_e64 v126, v64, v52, s[74:75]\n"
    "v_cndmask_b32_e64 v125, v53, v65, s[74:75]\n"
    "v_cndmask_b32_e64 v127, v65, v53, s[74:75]\n"
    "v_mfma_f32_16x16x32_bf16 v[180:183], v[148:151], v[112:115], v[180:183]\n"
    "v_mul_f32_e32 v56, s62, v56\n"
    "v_mul_f32_e32 v57, s62, v57\n"
    "v_mul_f32_e32 v58, s62, v58\n"
    "v_mul_f32_e32 v59, s62, v59\n"
    "v_mul_f32_e32 v68, s62, v68\n"
    "v_mul_f32_e32 v69, s62, v69\n"
    "v_mul_f32_e32 v70, s62, v70\n"
    "v_mul_f32_e32 v71, s62, v71\n"
    "v_mfma_f32_16x16x32_bf16 v[184:187], v[148:151], v[116:119], v[184:187]\n"
    "ds_read_b64_tr_b16 v[148:149], v245 offset:0\n"
    "ds_read_b64_tr_b16 v[150:151], v245 offset:16384\n"
    "v_cvt_pk_bf16_f32 v56, v56, v57\n"
    "v_cvt_pk_bf16_f32 v57, v58, v59\n"
    "v_cvt_pk_bf16_f32 v68, v68, v69\n"
    "v_cvt_pk_bf16_f32 v69, v70, v71\n"
    "v_cndmask_b32_e64 v128, v56, v68, s[74:75]\n"
    "v_cndmask_b32_e64 v130, v68, v56, s[74:75]\n"
    "v_cndmask_b32_e64 v129, v57, v69, s[74:75]\n"
    "v_cndmask_b32_e64 v131, v69, v57, s[74:75]\n"
    "v_mfma_f32_16x16x32_bf16 v[176:179], v[152:155], v[120:123], v[176:179]\n"
    "v_mul_f32_e32 v72, s62, v72\n"
    "v_mul_f32_e32 v73, s62, v73\n"
    "v_mul_f32_e32 v74, s62, v74\n"
    "v_mul_f32_e32 v75, s62, v75\n"
    "v_mul_f32_e32 v84, s62, v84\n"
    "v_mul_f32_e32 v85, s62, v85\n"
    "v_mul_f32_e32 v86, s62, v86\n"
    "v_mul_f32_e32 v87, s62, v87\n"
    "v_cvt_pk_bf16_f32 v72, v72, v73\n"
    "v_cvt_pk_bf16_f32 v73, v74, v75\n"
    "v_cvt_pk_bf16_f32 v84, v84, v85\n"
    "v_cvt_pk_bf16_f32 v85, v86, v87\n"
    "v_cndmask_b32_e64 v132, v72, v84, s[74:75]\n"
    "v_cndmask_b32_e64 v134, v84, v72, s[74:75]\n"
    "v_cndmask_b32_e64 v133, v73, v85, s[74:75]\n"
    "v_cndmask_b32_e64 v135, v85, v73, s[74:75]\n"
    "v_mfma_f32_16x16x32_bf16 v[180:183], v[152:155], v[124:127], v[180:183]\n"
    "v_mul_f32_e32 v76, s62, v76\n"
    "v_mul_f32_e32 v77, s62, v77\n"
    "v_mul_f32_e32 v78, s62, v78\n"
    "v_mul_f32_e32 v79, s62, v79\n"
    "v_mul_f32_e32 v88, s62, v88\n"
    "v_mul_f32_e32 v89, s62, v89\n"
    "v_mul_f32_e32 v90, s62, v90\n"
    "v_mul_f32_e32 v91, s62, v91\n"
    "v_cvt_pk_bf16_f32 v76, v76, v77\n"
    "v_cvt_pk_bf16_f32 v77, v78, v79\n"
    "v_cvt_pk_bf16_f32 v88, v88, v89\n"
    "v_cvt_pk_bf16_f32 v89, v90, v91\n"
    "s_waitcnt lgkmcnt(5)\n"
    "v_cndmask_b32_e64 v136, v76, v88, s[74:75]\n"
    "v_cndmask_b32_e64 v138, v88, v76, s[74:75]\n"
    "v_cndmask_b32_e64 v137, v77, v89, s[74:75]\n"
    "v_cndmask_b32_e64 v139, v89, v77, s[74:75]\n"
    "v_mfma_f32_16x16x32_bf16 v[184:187], v[152:155], v[128:131], v[184:187]\n"
    "ds_read_b64_tr_b16 v[152:153], v244 offset:512\n"
    "ds_read_b64_tr_b16 v[154:155], v244 offset:16896\n"
    "v_mul_f32_e32 v80, s62, v80\n"
    "v_mul_f32_e32 v81, s62, v81\n"
    "v_mul_f32_e32 v82, s62, v82\n"
    "v_mul_f32_e32 v83, s62, v83\n"
    "v_mul_f32_e32 v92, s62, v92\n"
    "v_mul_f32_e32 v93, s62, v93\n"
    "v_mul_f32_e32 v94, s62, v94\n"
    "v_mul_f32_e32 v95, s62, v95\n"
    "v_cvt_pk_bf16_f32 v80, v80, v81\n"
    "v_cvt_pk_bf16_f32 v81, v82, v83\n"
    "v_cvt_pk_bf16_f32 v92, v92, v93\n"
    "v_cvt_pk_bf16_f32 v93, v94, v95\n"
    "v_cndmask_b32_e64 v140, v80, v92, s[74:75]\n"
    "v_cndmask_b32_e64 v142, v92, v80, s[74:75]\n"
    "v_cndmask_b32_e64 v141, v81, v93, s[74:75]\n"
    "v_cndmask_b32_e64 v143, v93, v81, s[74:75]\n"
    "v_mfma_f32_16x16x32_bf16 v[176:179], v[156:159], v[132:135], v[176:179]\n"
    "v_add_u32_e32 v242, 4294836224, v242\n"
    "v_mfma_f32_16x16x32_bf16 v[180:183], v[156:159], v[136:139], v[180:183]\n"
    "v_xor_b32_e32 v243, 64, v242\n"
    "v_mfma_f32_16x16x32_bf16 v[184:187], v[156:159], v[140:143], v[184:187]\n"
    "ds_read_b64_tr_b16 v[156:157], v245 offset:512\n"
    "ds_read_b64_tr_b16 v[158:159], v245 offset:16896\n"
    "v_mfma_f32_16x16x32_bf16 v[188:191], v[160:163], v[96:99], 0\n"
    "v_mov_b32_e32 v236, 0x3f803f80\n"
    "v_mfma_f32_16x16x32_bf16 v[188:191], v[164:167], v[108:111], v[188:191]\n"
    "v_mov_b32_e32 v237, 0x3f803f80\n"
    "v_mfma_f32_16x16x32_bf16 v[188:191], v[168:171], v[120:123], v[188:191]\n"
    "v_mov_b32_e32 v238, 0x3f803f80\n"
    "s_waitcnt lgkmcnt(8)\n"
    "v_mfma_f32_16x16x32_bf16 v[188:191], v[172:175], v[132:135], v[188:191]\n"
    "v_mov_b32_e32 v239, 0x3f803f80\n"
    "v_mfma_f32_16x16x32_bf16 v[192:195], v[160:163], v[100:103], 0\n"
    "v_mfma_f32_16x16x32_bf16 v[192:195], v[164:167], v[112:115], v[192:195]\n"
    "s_add_u32 m0, s56, 98304\n"
    "s_add_u32 s61, s65, 32768\n"
    "s_and_b32 s61, s61, 0x1ffff\n"
    "buffer_load_dwordx4 v247, s[48:51], s61 offen offset:2048 nt lds\n"
    "s_add_u32 m0, s56, 98304\n"
    "s_add_u32 s61, s65, 32768\n"
    "s_and_b32 s61, s61, 0x1ffff\n"
    "s_add_u32 s61, s61, 130048\n"
    "buffer_load_dwordx4 v247, s[48:51], s61 offen offset:3072 nt lds\n"
    "v_mfma_f32_16x16x32_bf16 v[192:195], v[168:171], v[124:127], v[192:195]\n"
    "v_max3_f32 v252, v176, v177, v178\n"
    "v_max3_f32 v253, v179, v188, v189\n"
    "v_max3_f32 v252, v252, v190, v191\n"
    "v_max_f32_e32 v252, v252, v253\n"
    "v_mov_b32_e32 v253, v252\n"
    "s_nop 1\n"
    "v_permlane16_swap_b32 v252, v253\n"
    "s_nop 1\n"
    "v_mfma_f32_16x16x32_bf16 v[192:195], v[172:175], v[136:139], v[192:195]\n"
    "v_max_f32_e32 v252, v252, v253\n"
    "v_mov_b32_e32 v253, v252\n"
    "s_nop 1\n"
    "v_permlane32_swap_b32 v252, v253\n"
    "s_nop 1\n"
    "v_max_f32_e32 v252, v252, v253\n"
    "v_add_f32_e32 v252, 0x40e00000, v252\n"
    "v_sub_f32_e32 v176, v176, v252\n"
    "v_mfma_f32_16x16x32_bf16 v[196:199], v[160:163], v[104:107], 0\n"
    "ds_read_b64_tr_b16 v[160:161], v244 offset:4096\n"
    "ds_read_b64_tr_b16 v[162:163], v244 offset:20480\n"
    "v_sub_f32_e32 v177, v177, v252\n"
    "v_sub_f32_e32 v178, v178, v252\n"
    "v_sub_f32_e32 v179, v179, v252\n"
    "v_sub_f32_e32 v188, v188, v252\n"
    "v_sub_f32_e32 v189, v189, v252\n"
    "v_sub_f32_e32 v190, v190, v252\n"
    "v_mfma_f32_16x16x32_bf16 v[196:199], v[164:167], v[116:119], v[196:199]\n"
    "ds_read_b64_tr_b16 v[164:165], v245 offset:4096\n"
    "ds_read_b64_tr_b16 v[166:167], v245 offset:20480\n"
    "v_sub_f32_e32 v191, v191, v252\n"
    "v_sub_f32_e32 v224, 0, v252\n"
    "v_sub_f32_e32 v225, 0, v252\n"
    "v_sub_f32_e32 v226, 0, v252\n"
    "v_sub_f32_e32 v227, 0, v252\n"
    "v_exp_f32_e32 v248, v176\n"
    "s_add_u32 s61, s65, 40960\n"
    "s_and_b32 s61, s61, 0x1ffff\n"
    "buffer_load_dword v241, v240, s[48:51], s61 offen offset:2048\n"
    "v_mfma_f32_16x16x32_bf16 v[196:199], v[168:171], v[128:131], v[196:199]\n"
    "ds_read_b64_tr_b16 v[168:169], v244 offset:4608\n"
    "ds_read_b64_tr_b16 v[170:171], v244 offset:20992\n"
    "v_exp_f32_e32 v249, v177\n"
    "v_exp_f32_e32 v250, v178\n"
    "v_cvt_pk_bf16_f32 v200, v248, v249\n"
    "v_exp_f32_e32 v251, v179\n"
    "v_exp_f32_e32 v248, v188\n"
    "v_cvt_pk_bf16_f32 v201, v250, v251\n"
    "v_mfma_f32_16x16x32_bf16 v[196:199], v[172:175], v[140:143], v[196:199]\n"
    "ds_read_b64_tr_b16 v[172:173], v245 offset:4608\n"
    "ds_read_b64_tr_b16 v[174:175], v245 offset:20992\n"
    "v_exp_f32_e32 v249, v189\n"
    "v_exp_f32_e32 v250, v190\n"
    "v_cvt_pk_bf16_f32 v202, v248, v249\n"
    "v_exp_f32_e32 v251, v191\n"
    "s_nop 0\n"
    "v_cvt_pk_bf16_f32 v203, v250, v251\n"
    "s_waitcnt lgkmcnt(14)\n"
    "s_nop 0\n"
    "v_mfma_f32_16x16x32_bf16 v[0:3], v[144:147], v[200:203], 0\n"
    "v_max3_f32 v252, v180, v181, v182\n"
    "v_max3_f32 v253, v183, v192, v193\n"
    "v_max3_f32 v252, v252, v194, v195\n"
    "v_max_f32_e32 v252, v252, v253\n"
    "v_mov_b32_e32 v253, v252\n"
    "s_nop 1\n"
    "v_permlane16_swap_b32 v252, v253\n"
    "s_nop 1\n"
    "v_max_f32_e32 v252, v252, v253\n"
    "s_waitcnt lgkmcnt(12)\n"
    "v_mfma_f32_16x16x32_bf16 v[12:15], v[148:151], v[200:203], 0\n"
    "v_mov_b32_e32 v253, v252\n"
    "s_nop 1\n"
    "v_permlane32_swap_b32 v252, v253\n"
    "s_nop 1\n"
    "v_max_f32_e32 v252, v252, v253\n"
    "v_add_f32_e32 v252, 0x40e00000, v252\n"
    "v_sub_f32_e32 v180, v180, v252\n"
    "v_sub_f32_e32 v181, v181, v252\n"
    "v_sub_f32_e32 v182, v182, v252\n"
    "s_waitcnt lgkmcnt(10)\n"
    "v_mfma_f32_16x16x32_bf16 v[24:27], v[152:155], v[200:203], 0\n"
    "v_sub_f32_e32 v183, v183, v252\n"
    "v_sub_f32_e32 v192, v192, v252\n"
    "v_sub_f32_e32 v193, v193, v252\n"
    "v_sub_f32_e32 v194, v194, v252\n"
    "v_sub_f32_e32 v195, v195, v252\n"
    "v_sub_f32_e32 v228, 0, v252\n"
    "v_sub_f32_e32 v229, 0, v252\n"
    "s_waitcnt lgkmcnt(8)\n"
    "v_mfma_f32_16x16x32_bf16 v[36:39], v[156:159], v[200:203], 0\n"
    "v_sub_f32_e32 v230, 0, v252\n"
    "v_sub_f32_e32 v231, 0, v252\n"
    "v_exp_f32_e32 v248, v180\n"
    "v_exp_f32_e32 v249, v181\n"
    "v_exp_f32_e32 v250, v182\n"
    "v_cvt_pk_bf16_f32 v204, v248, v249\n"
    "v_exp_f32_e32 v251, v183\n"
    "v_mfma_f32_16x16x32_bf16 v[212:215], v[236:239], v[200:203], 0\n"
    "v_exp_f32_e32 v248, v192\n"
    "v_cvt_pk_bf16_f32 v205, v250, v251\n"
    "v_exp_f32_e32 v249, v193\n"
    "v_exp_f32_e32 v250, v194\n"
    "v_cvt_pk_bf16_f32 v206, v248, v249\n"
    "v_exp_f32_e32 v251, v195\n"
    "s_nop 0\n"
    "v_cvt_pk_bf16_f32 v207, v250, v251\n"
    "s_nop 1\n"
    "v_mfma_f32_16x16x32_bf16 v[4:7], v[144:147], v[204:207], 0\n"
    "v_max3_f32 v252, v184, v185, v186\n"
    "v_max3_f32 v253, v187, v196, v197\n"
    "v_max3_f32 v252, v252, v198, v199\n"
    "v_max_f32_e32 v252, v252, v253\n"
    "v_mfma_f32_16x16x32_bf16 v[16:19], v[148:151], v[204:207], 0\n"
    "v_mov_b32_e32 v253, v252\n"
    "s_nop 1\n"
    "v_permlane16_swap_b32 v252, v253\n"
    "s_nop 1\n"
    "v_max_f32_e32 v252, v252, v253\n"
    "v_mov_b32_e32 v253, v252\n"
    "v_mfma_f32_16x16x32_bf16 v[28:31], v[152:155], v[204:207], 0\n"
    "s_nop 0\n"
    "v_permlane32_swap_b32 v252, v253\n"
    "s_nop 1\n"
    "v_max_f32_e32 v252, v252, v253\n"
    "v_add_f32_e32 v252, 0x40e00000, v252\n"
    "v_sub_f32_e32 v184, v184, v252\n"
    "v_mfma_f32_16x16x32_bf16 v[40:43], v[156:159], v[204:207], 0\n"
    "v_sub_f32_e32 v185, v185, v252\n"
    "v_sub_f32_e32 v186, v186, v252\n"
    "v_sub_f32_e32 v187, v187, v252\n"
    "v_sub_f32_e32 v196, v196, v252\n"
    "v_mfma_f32_16x16x32_bf16 v[216:219], v[236:239], v[204:207], 0\n"
    "v_sub_f32_e32 v197, v197, v252\n"
    "v_sub_f32_e32 v198, v198, v252\n"
    "v_sub_f32_e32 v199, v199, v252\n"
    "v_sub_f32_e32 v232, 0, v252\n"
    "s_waitcnt lgkmcnt(0)\n"
    "v_mfma_f32_16x16x32_bf16 v[48:51], v[160:163], v[200:203], 0\n"
    "v_sub_f32_e32 v233, 0, v252\n"
    "v_sub_f32_e32 v234, 0, v252\n"
    "v_sub_f32_e32 v235, 0, v252\n"
    "v_exp_f32_e32 v248, v184\n"
    "v_mfma_f32_16x16x32_bf16 v[60:63], v[164:167], v[200:203], 0\n"
    "v_exp_f32_e32 v249, v185\n"
    "v_exp_f32_e32 v250, v186\n"
    "v_cvt_pk_bf16_f32 v208, v248, v249\n"
    "v_exp_f32_e32 v251, v187\n"
    "v_mfma_f32_16x16x32_bf16 v[72:75], v[168:171], v[200:203], 0\n"
    "v_exp_f32_e32 v248, v196\n"
    "v_cvt_pk_bf16_f32 v209, v250, v251\n"
    "v_exp_f32_e32 v249, v197\n"
    "v_exp_f32_e32 v250, v198\n"
    "v_mfma_f32_16x16x32_bf16 v[84:87], v[172:175], v[200:203], 0\n"
    "v_cvt_pk_bf16_f32 v210, v248, v249\n"
    "v_exp_f32_e32 v251, v199\n"
    "s_nop 0\n"
    "v_cvt_pk_bf16_f32 v211, v250, v251\n"
    "s_waitcnt vmcnt(45)\n"
    "s_barrier\n"
    "v_mfma_f32_16x16x32_bf16 v[8:11], v[144:147], v[208:211], 0\n"
    "ds_read_b128 v[144:147], v242 offset:0\n"
    "v_mfma_f32_16x16x32_bf16 v[20:23], v[148:151], v[208:211], 0\n"
    "ds_read_b128 v[148:151], v243 offset:0\n"
    "v_add_u32_e32 v244, 4294836224, v244\n"
    "v_mfma_f32_16x16x32_bf16 v[32:35], v[152:155], v[208:211], 0\n"
    "ds_read_b128 v[152:155], v242 offset:4096\n"
    "v_xor_b32_e32 v245, 32, v244\n"
    "v_mfma_f32_16x16x32_bf16 v[44:47], v[156:159], v[208:211], 0\n"
    "ds_read_b128 v[156:159], v243 offset:4096\n"
    "v_mfma_f32_16x16x32_bf16 v[52:55], v[160:163], v[204:207], 0\n"
    "v_mfma_f32_16x16x32_bf16 v[64:67], v[164:167], v[204:207], 0\n"
    "v_mfma_f32_16x16x32_bf16 v[76:79], v[168:171], v[204:207], 0\n"
    "v_mfma_f32_16x16x32_bf16 v[88:91], v[172:175], v[204:207], 0\n"
    "v_mfma_f32_16x16x32_bf16 v[56:59], v[160:163], v[208:211], 0\n"
    "ds_read_b128 v[160:163], v242 offset:16384\n"
    "v_mfma_f32_16x16x32_bf16 v[68:71], v[164:167], v[208:211], 0\n"
    "ds_read_b128 v[164:167], v243 offset:16384\n"
    "v_mfma_f32_16x16x32_bf16 v[80:83], v[168:171], v[208:211], 0\n"
    "ds_read_b128 v[168:171], v242 offset:20480\n"
    "v_mfma_f32_16x16x32_bf16 v[92:95], v[172:175], v[208:211], 0\n"
    "ds_read_b128 v[172:175], v243 offset:20480\n"
    "s_add_u32 m0, s56, 131072\n"
    "s_add_u32 s61, s65, 40960\n"
    "s_and_b32 s61, s61, 0x1ffff\n"
    "buffer_load_dwordx4 v246, s[44:47], s61 offen offset:0 nt lds\n"
    "s_add_u32 m0, s56, 131072\n"
    "s_add_u32 s61, s65, 40960\n"
    "s_and_b32 s61, s61, 0x1ffff\n"
    "s_add_u32 s61, s61, 130048\n"
    "buffer_load_dwordx4 v246, s[44:47], s61 offen offset:1024 nt lds\n"
    "v_mfma_f32_16x16x32_bf16 v[220:223], v[236:239], v[208:211], 0\n"
    "s_waitcnt lgkmcnt(4)\n"
    "v_mfma_f32_16x16x32_bf16 v[176:179], v[144:147], v[96:99], v[224:227]\n"
    "v_mfma_f32_16x16x32_bf16 v[180:183], v[144:147], v[100:103], v[228:231]\n"
    "v_mfma_f32_16x16x32_bf16 v[184:187], v[144:147], v[104:107], v[232:235]\n"
    "ds_read_b64_tr_b16 v[144:145], v244 offset:0\n"
    "ds_read_b64_tr_b16 v[146:147], v244 offset:16384\n"
    "s_add_u32 s61, s65, 49152\n"
    "s_and_b32 s61, s61, 0x1ffff\n"
    "buffer_load_dword v241, v240, s[44:47], s61 offen offset:0\n"
    "v_mfma_f32_16x16x32_bf16 v[176:179], v[148:151], v[108:111], v[176:179]\n"
    "v_mfma_f32_16x16x32_bf16 v[180:183], v[148:151], v[112:115], v[180:183]\n"
    "v_mfma_f32_16x16x32_bf16 v[184:187], v[148:151], v[116:119], v[184:187]\n"
    "ds_read_b64_tr_b16 v[148:149], v245 offset:0\n"
    "ds_read_b64_tr_b16 v[150:151], v245 offset:16384\n"
    "v_mfma_f32_16x16x32_bf16 v[176:179], v[152:155], v[120:123], v[176:179]\n"
    "v_mfma_f32_16x16x32_bf16 v[180:183], v[152:155], v[124:127], v[180:183]\n"
    "v_mfma_f32_16x16x32_bf16 v[184:187], v[152:155], v[128:131], v[184:187]\n"
    "ds_read_b64_tr_b16 v[152:153], v244 offset:512\n"
    "ds_read_b64_tr_b16 v[154:155], v244 offset:16896\n"
    "v_mfma_f32_16x16x32_bf16 v[176:179], v[156:159], v[132:135], v[176:179]\n"
    "v_add_u32_e32 v242, 32768, v242\n"
    "v_mfma_f32_16x16x32_bf16 v[180:183], v[156:159], v[136:139], v[180:183]\n"
    "v_xor_b32_e32 v243, 64, v242\n"
    "v_mfma_f32_16x16x32_bf16 v[184:187], v[156:159], v[140:143], v[184:187]\n"
    "ds_read_b64_tr_b16 v[156:157], v245 offset:512\n"
    "ds_read_b64_tr_b16 v[158:159], v245 offset:16896\n"
    "s_waitcnt lgkmcnt(8)\n"
    "v_mfma_f32_16x16x32_bf16 v[188:191], v[160:163], v[96:99], v[224:227]\n"
    "v_mfma_f32_16x16x32_bf16 v[188:191], v[164:167], v[108:111], v[188:191]\n"
    "v_exp_f32_e32 v248, v176\n"
    "v_exp_f32_e32 v249, v177\n"
    "v_exp_f32_e32 v250, v178\n"
    "v_mfma_f32_16x16x32_bf16 v[188:191], v[168:171], v[120:123], v[188:191]\n"
    "v_cvt_pk_bf16_f32 v200, v248, v249\n"
    "v_exp_f32_e32 v251, v179\n"
    "v_mfma_f32_16x16x32_bf16 v[188:191], v[172:175], v[132:135], v[188:191]\n"
    "v_exp_f32_e32 v248, v180\n"
    "v_cvt_pk_bf16_f32 v201, v250, v251\n"
    "v_mfma_f32_16x16x32_bf16 v[192:195], v[160:163], v[100:103], v[228:231]\n"
    "v_exp_f32_e32 v249, v181\n"
    "v_exp_f32_e32 v250, v182\n"
    "v_mfma_f32_16x16x32_bf16 v[192:195], v[164:167], v[112:115], v[192:195]\n"
    "v_cvt_pk_bf16_f32 v204, v248, v249\n"
    "v_exp_f32_e32 v251, v183\n"
    "v_mfma_f32_16x16x32_bf16 v[192:195], v[168:171], v[124:127], v[192:195]\n"
    "v_exp_f32_e32 v248, v188\n"
    "v_cvt_pk_bf16_f32 v205, v250, v251\n"
    "v_mfma_f32_16x16x32_bf16 v[192:195], v[172:175], v[136:139], v[192:195]\n"
    "v_exp_f32_e32 v249, v189\n"
    "v_exp_f32_e32 v250, v190\n"
    "v_mfma_f32_16x16x32_bf16 v[196:199], v[160:163], v[104:107], v[232:235]\n"
    "ds_read_b64_tr_b16 v[160:161], v244 offset:4096\n"
    "ds_read_b64_tr_b16 v[162:163], v244 offset:20480\n"
    "v_cvt_pk_bf16_f32 v202, v248, v249\n"
    "v_exp_f32_e32 v251, v191\n"
    "v_mfma_f32_16x16x32_bf16 v[196:199], v[164:167], v[116:119], v[196:199]\n"
    "ds_read_b64_tr_b16 v[164:165], v245 offset:4096\n"
    "ds_read_b64_tr_b16 v[166:167], v245 offset:20480\n"
    "v_or3_b32 v255, v200, v201, v202\n"
    "v_cvt_pk_bf16_f32 v203, v250, v251\n"
    "v_mfma_f32_16x16x32_bf16 v[196:199], v[168:171], v[128:131], v[196:199]\n"
    "ds_read_b64_tr_b16 v[168:169], v244 offset:4608\n"
    "ds_read_b64_tr_b16 v[170:171], v244 offset:20992\n"
    "v_bitop3_b32 v255, v255, s63, v203 bitop3:0xc8\n"
    "v_cmp_ne_u32_e32 vcc, 0, v255\n"
    "v_mfma_f32_16x16x32_bf16 v[196:199], v[172:175], v[140:143], v[196:199]\n"
    "s_cbranch_vccnz .Lst1_0_%=\n"
    ".Lbk1_0_%=:\n"
    "ds_read_b64_tr_b16 v[172:173], v245 offset:4608\n"
    "ds_read_b64_tr_b16 v[174:175], v245 offset:20992\n"
    "v_exp_f32_e32 v248, v192\n"
    "v_exp_f32_e32 v249, v193\n"
    "s_waitcnt lgkmcnt(8)\n"
    "v_mfma_f32_16x16x32_bf16 v[0:3], v[144:147], v[200:203], v[0:3]\n"
    "v_exp_f32_e32 v250, v194\n"
    "v_cvt_pk_bf16_f32 v206, v248, v249\n"
    "v_mfma_f32_16x16x32_bf16 v[12:15], v[148:151], v[200:203], v[12:15]\n"
    "v_exp_f32_e32 v251, v195\n"
    "v_or3_b32 v255, v204, v205, v206\n"
    "v_mfma_f32_16x16x32_bf16 v[24:27], v[152:155], v[200:203], v[24:27]\n"
    "v_cvt_pk_bf16_f32 v207, v250, v251\n"
    "v_bitop3_b32 v255, v255, s63, v207 bitop3:0xc8\n"
    "v_mfma_f32_16x16x32_bf16 v[36:39], v[156:159], v[200:203], v[36:39]\n"
    "v_cmp_ne_u32_e32 vcc, 0, v255\n"
    "v_mfma_f32_16x16x32_bf16 v[212:215], v[236:239], v[200:203], v[212:215]\n"
    "s_cbranch_vccnz .Lst1_1_%=\n"
    ".Lbk1_1_%=:\n"
    "v_exp_f32_e32 v248, v184\n"
    "v_exp_f32_e32 v249, v185\n"
    "v_mfma_f32_16x16x32_bf16 v[4:7], v[144:147], v[204:207], v[4:7]\n"
    "v_exp_f32_e32 v250, v186\n"
    "v_cvt_pk_bf16_f32 v208, v248, v249\n"
    "v_mfma_f32_16x16x32_bf16 v[16:19], v[148:151], v[204:207], v[16:19]\n"
    "v_exp_f32_e32 v251, v187\n"
    "s_add_u32 m0, s56, 131072\n"
    "s_add_u32 s61, s65, 40960\n"
    "s_and_b32 s61, s61, 0x1ffff\n"
    "buffer_load_dwordx4 v247, s[48:51], s61 offen offset:2048 nt lds\n"
    "s_add_u32 m0, s56, 131072\n"
    "s_add_u32 s61, s65, 40960\n"
    "s_and_b32 s61, s61, 0x1ffff\n"
    "s_add_u32 s61, s61, 130048\n"
    "buffer_load_dwordx4 v247, s[48:51], s61 offen offset:3072 nt lds\n"
    "v_mfma_f32_16x16x32_bf16 v[28:31], v[152:155], v[204:207], v[28:31]\n"
    "v_exp_f32_e32 v248, v196\n"
    "v_cvt_pk_bf16_f32 v209, v250, v251\n"
    "v_mfma_f32_16x16x32_bf16 v[40:43], v[156:159], v[204:207], v[40:43]\n"
    "v_exp_f32_e32 v249, v197\n"
    "v_exp_f32_e32 v250, v198\n"
    "v_mfma_f32_16x16x32_bf16 v[216:219], v[236:239], v[204:207], v[216:219]\n"
    "v_cvt_pk_bf16_f32 v210, v248, v249\n"
    "s_waitcnt lgkmcnt(0)\n"
    "v_mfma_f32_16x16x32_bf16 v[48:51], v[160:163], v[200:203], v[48:51]\n"
    "v_exp_f32_e32 v251, v199\n"
    "v_or3_b32 v255, v208, v209, v210\n"
    "s_add_u32 s61, s65, 49152\n"
    "s_and_b32 s61, s61, 0x1ffff\n"
    "buffer_load_dword v241, v240, s[48:51], s61 offen offset:2048\n"
    "v_mfma_f32_16x16x32_bf16 v[60:63], v[164:167], v[200:203], v[60:63]\n"
    "v_cvt_pk_bf16_f32 v211, v250, v251\n"
    "v_bitop3_b32 v255, v255, s63, v211 bitop3:0xc8\n"
    "v_mfma_f32_16x16x32_bf16 v[72:75], v[168:171], v[200:203], v[72:75]\n"
    "v_cmp_ne_u32_e32 vcc, 0, v255\n"
    "v_mfma_f32_16x16x32_bf16 v[84:87], v[172:175], v[200:203], v[84:87]\n"
    "s_cbranch_vccnz .Lst1_2_%=\n"
    ".Lbk1_2_%=:\n"
    "s_barrier\n"
    "v_mfma_f32_16x16x32_bf16 v[8:11], v[144:147], v[208:211], v[8:11]\n"
    "ds_read_b128 v[144:147], v242 offset:0\n"
    "v_mfma_f32_16x16x32_bf16 v[20:23], v[148:151], v[208:211], v[20:23]\n"
    "ds_read_b128 v[148:151], v243 offset:0\n"
    "v_add_u32_e32 v244, 32768, v244\n"
    "v_mfma_f32_16x16x32_bf16 v[32:35], v[152:155], v[208:211], v[32:35]\n"
    "ds_read_b128 v[152:155], v242 offset:4096\n"
    "v_xor_b32_e32 v245, 32, v244\n"
    "v_mfma_f32_16x16x32_bf16 v[44:47], v[156:159], v[208:211], v[44:47]\n"
    "ds_read_b128 v[156:159], v243 offset:4096\n"
    "v_mfma_f32_16x16x32_bf16 v[52:55], v[160:163], v[204:207], v[52:55]\n"
    "v_mfma_f32_16x16x32_bf16 v[64:67], v[164:167], v[204:207], v[64:67]\n"
    "v_mfma_f32_16x16x32_bf16 v[76:79], v[168:171], v[204:207], v[76:79]\n"
    "v_mfma_f32_16x16x32_bf16 v[88:91], v[172:175], v[204:207], v[88:91]\n"
    "v_mfma_f32_16x16x32_bf16 v[56:59], v[160:163], v[208:211], v[56:59]\n"
    "ds_read_b128 v[160:163], v242 offset:16384\n"
    "v_mfma_f32_16x16x32_bf16 v[68:71], v[164:167], v[208:211], v[68:71]\n"
    "ds_read_b128 v[164:167], v243 offset:16384\n"
    "v_mfma_f32_16x16x32_bf16 v[80:83], v[168:171], v[208:211], v[80:83]\n"
    "ds_read_b128 v[168:171], v242 offset:20480\n"
    "v_mfma_f32_16x16x32_bf16 v[92:95], v[172:175], v[208:211], v[92:95]\n"
    "ds_read_b128 v[172:175], v243 offset:20480\n"
    "v_mfma_f32_16x16x32_bf16 v[220:223], v[236:239], v[208:211], v[220:223]\n"
    "s_waitcnt lgkmcnt(4)\n"
    "v_mfma_f32_16x16x32_bf16 v[176:179], v[144:147], v[96:99], v[224:227]\n"
    "v_mfma_f32_16x16x32_bf16 v[180:183], v[144:147], v[100:103], v[228:231]\n"
    "v_mfma_f32_16x16x32_bf16 v[184:187], v[144:147], v[104:107], v[232:235]\n"
    "ds_read_b64_tr_b16 v[144:145], v244 offset:0\n"
    "ds_read_b64_tr_b16 v[146:147], v244 offset:16384\n"
    "v_mfma_f32_16x16x32_bf16 v[176:179], v[148:151], v[108:111], v[176:179]\n"
    "v_mfma_f32_16x16x32_bf16 v[180:183], v[148:151], v[112:115], v[180:183]\n"
    "v_mfma_f32_16x16x32_bf16 v[184:187], v[148:151], v[116:119], v[184:187]\n"
    "ds_read_b64_tr_b16 v[148:149], v245 offset:0\n"
    "ds_read_b64_tr_b16 v[150:151], v245 offset:16384\n"
    "v_mfma_f32_16x16x32_bf16 v[176:179], v[152:155], v[120:123], v[176:179]\n"
    "v_mfma_f32_16x16x32_bf16 v[180:183], v[152:155], v[124:127], v[180:183]\n"
    "v_mfma_f32_16x16x32_bf16 v[184:187], v[152:155], v[128:131], v[184:187]\n"
    "ds_read_b64_tr_b16 v[152:153], v244 offset:512\n"
    "ds_read_b64_tr_b16 v[154:155], v244 offset:16896\n"
    "v_mfma_f32_16x16x32_bf16 v[176:179], v[156:159], v[132:135], v[176:179]\n"
    "v_add_u32_e32 v242, 32768, v242\n"
    "v_mfma_f32_16x16x32_bf16 v[180:183], v[156:159], v[136:139], v[180:183]\n"
    "v_xor_b32_e32 v243, 64, v242\n"
    "v_mfma_f32_16x16x32_bf16 v[184:187], v[156:159], v[140:143], v[184:187]\n"
    "ds_read_b64_tr_b16 v[156:157], v245 offset:512\n"
    "ds_read_b64_tr_b16 v[158:159], v245 offset:16896\n"
    "s_waitcnt lgkmcnt(8)\n"
    "s_add_u32 m0, s56, 0\n"
    "s_add_u32 s61, s65, 49152\n"
    "s_and_b32 s61, s61, 0x1ffff\n"
    "buffer_load_dwordx4 v246, s[44:47], s61 offen offset:0 nt lds\n"
    "s_add_u32 m0, s56, 0\n"
    "s_add_u32 s61, s65, 49152\n"
    "s_and_b32 s61, s61, 0x1ffff\n"
    "s_add_u32 s61, s61, 130048\n"
    "buffer_load_dwordx4 v246, s[44:47], s61 offen offset:1024 nt lds\n"
    "v_mfma_f32_16x16x32_bf16 v[188:191], v[160:163], v[96:99], v[224:227]\n"
    "v_mfma_f32_16x16x32_bf16 v[188:191], v[164:167], v[108:111], v[188:191]\n"
    "v_exp_f32_e32 v248, v176\n"
    "v_exp_f32_e32 v249, v177\n"
    "v_exp_f32_e32 v250, v178\n"
    "v_mfma_f32_16x16x32_bf16 v[188:191], v[168:171], v[120:123], v[188:191]\n"
    "v_cvt_pk_bf16_f32 v200, v248, v249\n"
    "v_exp_f32_e32 v251, v179\n"
    "v_mfma_f32_16x16x32_bf16 v[188:191], v[172:175], v[132:135], v[188:191]\n"
    "v_exp_f32_e32 v248, v180\n"
    "v_cvt_pk_bf16_f32 v201, v250, v251\n"
    "v_mfma_f32_16x16x32_bf16 v[192:195], v[160:163], v[100:103], v[228:231]\n"
    "v_exp_f32_e32 v249, v181\n"
    "v_exp_f32_e32 v250, v182\n"
    "s_add_u32 s61, s65, 57344\n"
    "s_and_b32 s61, s61, 0x1ffff\n"
    "buffer_load_dword v241, v240, s[44:47], s61 offen offset:0\n"
    "v_mfma_f32_16x16x32_bf16 v[192:195], v[164:167], v[112:115], v[192:195]\n"
    "v_cvt_pk_bf16_f32 v204, v248, v249\n"
    "v_exp_f32_e32 v251, v183\n"
    "v_mfma_f32_16x16x32_bf16 v[192:195], v[168:171], v[124:127], v[192:195]\n"
    "v_exp_f32_e32 v248, v188\n"
    "v_cvt_pk_bf16_f32 v205, v250, v251\n"
    "v_mfma_f32_16x16x32_bf16 v[192:195], v[172:175], v[136:139], v[192:195]\n"
    "v_exp_f32_e32 v249, v189\n"
    "v_exp_f32_e32 v250, v190\n"
    "v_mfma_f32_16x16x32_bf16 v[196:199], v[160:163], v[104:107], v[232:235]\n"
    "ds_read_b64_tr_b16 v[160:161], v244 offset:4096\n"
    "ds_read_b64_tr_b16 v[162:163], v244 offset:20480\n"
    "v_cvt_pk_bf16_f32 v202, v248, v249\n"
    "v_exp_f32_e32 v251, v191\n"
    "v_mfma_f32_16x16x32_bf16 v[196:199], v[164:167], v[116:119], v[196:199]\n"
    "ds_read_b64_tr_b16 v[164:165], v245 offset:4096\n"
    "ds_read_b64_tr_b16 v[166:167], v245 offset:20480\n"
    "v_or3_b32 v255, v200, v201, v202\n"
    "v_cvt_pk_bf16_f32 v203, v250, v251\n"
    "v_mfma_f32_16x16x32_bf16 v[196:199], v[168:171], v[128:131], v[196:199]\n"
    "ds_read_b64_tr_b16 v[168:169], v244 offset:4608\n"
    "ds_read_b64_tr_b16 v[170:171], v244 offset:20992\n"
    "v_bitop3_b32 v255, v255, s63, v203 bitop3:0xc8\n"
    "v_cmp_ne_u32_e32 vcc, 0, v255\n"
    "v_mfma_f32_16x16x32_bf16 v[196:199], v[172:175], v[140:143], v[196:199]\n"
    "s_cbranch_vccnz .Lst2_0_%=\n"
    ".Lbk2_0_%=:\n"
    "ds_read_b64_tr_b16 v[172:173], v245 offset:4608\n"
    "ds_read_b64_tr_b16 v[174:175], v245 offset:20992\n"
    "v_exp_f32_e32 v248, v192\n"
    "v_exp_f32_e32 v249, v193\n"
    "s_waitcnt lgkmcnt(8)\n"
    "v_mfma_f32_16x16x32_bf16 v[0:3], v[144:147], v[200:203], v[0:3]\n"
    "v_exp_f32_e32 v250, v194\n"
    "v_cvt_pk_bf16_f32 v206, v248, v249\n"
    "v_mfma_f32_16x16x32_bf16 v[12:15], v[148:151], v[200:203], v[12:15]\n"
    "v_exp_f32_e32 v251, v195\n"
    "v_or3_b32 v255, v204, v205, v206\n"
    "v_mfma_f32_16x16x32_bf16 v[24:27], v[152:155], v[200:203], v[24:27]\n"
    "v_cvt_pk_bf16_f32 v207, v250, v251\n"
    "v_bitop3_b32 v255, v255, s63, v207 bitop3:0xc8\n"
    "v_mfma_f32_16x16x32_bf16 v[36:39], v[156:159], v[200:203], v[36:39]\n"
    "v_cmp_ne_u32_e32 vcc, 0, v255\n"
    "v_mfma_f32_16x16x32_bf16 v[212:215], v[236:239], v[200:203], v[212:215]\n"
    "s_cbranch_vccnz .Lst2_1_%=\n"
    ".Lbk2_1_%=:\n"
    "v_exp_f32_e32 v248, v184\n"
    "v_exp_f32_e32 v249, v185\n"
    "v_mfma_f32_16x16x32_bf16 v[4:7], v[144:147], v[204:207], v[4:7]\n"
    "v_exp_f32_e32 v250, v186\n"
    "v_cvt_pk_bf16_f32 v208, v248, v249\n"
    "v_mfma_f32_16x16x32_bf16 v[16:19], v[148:151], v[204:207], v[16:19]\n"
    "v_exp_f32_e32 v251, v187\n"
    "v_mfma_f32_16x16x32_bf16 v[28:31], v[152:155], v[204:207], v[28:31]\n"
    "v_exp_f32_e32 v248, v196\n"
    "v_cvt_pk_bf16_f32 v209, v250, v251\n"
    "v_mfma_f32_16x16x32_bf16 v[40:43], v[156:159], v[204:207], v[40:43]\n"
    "v_exp_f32_e32 v249, v197\n"
    "v_exp_f32_e32 v250, v198\n"
    "v_mfma_f32_16x16x32_bf16 v[216:219], v[236:239], v[204:207], v[216:219]\n"
    "v_cvt_pk_bf16_f32 v210, v248, v249\n"
    "s_waitcnt lgkmcnt(0)\n"
    "v_mfma_f32_16x16x32_bf16 v[48:51], v[160:163], v[200:203], v[48:51]\n"
    "v_exp_f32_e32 v251, v199\n"
    "v_or3_b32 v255, v208, v209, v210\n"
    "v_mfma_f32_16x16x32_bf16 v[60:63], v[164:167], v[200:203], v[60:63]\n"
    "v_cvt_pk_bf16_f32 v211, v250, v251\n"
    "v_bitop3_b32 v255, v255, s63, v211 bitop3:0xc8\n"
    "v_mfma_f32_16x16x32_bf16 v[72:75], v[168:171], v[200:203], v[72:75]\n"
    "v_cmp_ne_u32_e32 vcc, 0, v255\n"
    "v_mfma_f32_16x16x32_bf16 v[84:87], v[172:175], v[200:203], v[84:87]\n"
    "s_cbranch_vccnz .Lst2_2_%=\n"
    ".Lbk2_2_%=:\n"
    "s_barrier\n"
    "v_mfma_f32_16x16x32_bf16 v[8:11], v[144:147], v[208:211], v[8:11]\n"
    "ds_read_b128 v[144:147], v242 offset:0\n"
    "v_mfma_f32_16x16x32_bf16 v[20:23], v[148:151], v[208:211], v[20:23]\n"
    "ds_read_b128 v[148:151], v243 offset:0\n"
    "v_add_u32_e32 v244, 32768, v244\n"
    "v_mfma_f32_16x16x32_bf16 v[32:35], v[152:155], v[208:211], v[32:35]\n"
    "ds_read_b128 v[152:155], v242 offset:4096\n"
    "v_xor_b32_e32 v245, 32, v244\n"
    "v_mfma_f32_16x16x32_bf16 v[44:47], v[156:159], v[208:211], v[44:47]\n"
    "ds_read_b128 v[156:159], v243 offset:4096\n"
    "v_mfma_f32_16x16x32_bf16 v[52:55], v[160:163], v[204:207], v[52:55]\n"
    "v_mfma_f32_16x16x32_bf16 v[64:67], v[164:167], v[204:207], v[64:67]\n"
    "s_add_u32 m0, s56, 0\n"
    "s_add_u32 s61, s65, 49152\n"
    "s_and_b32 s61, s61, 0x1ffff\n"
    "buffer_load_dwordx4 v247, s[48:51], s61 offen offset:2048 nt lds\n"
    "s_add_u32 m0, s56, 0\n"
    "s_add_u32 s61, s65, 49152\n"
    "s_and_b32 s61, s61, 0x1ffff\n"
    "s_add_u32 s61, s61, 130048\n"
    "buffer_load_dwordx4 v247, s[48:51], s61 offen offset:3072 nt lds\n"
    "v_mfma_f32_16x16x32_bf16 v[76:79], v[168:171], v[204:207], v[76:79]\n"
    "v_mfma_f32_16x16x32_bf16 v[88:91], v[172:175], v[204:207], v[88:91]\n"
    "v_mfma_f32_16x16x32_bf16 v[56:59], v[160:163], v[208:211], v[56:59]\n"
    "ds_read_b128 v[160:163], v242 offset:16384\n"
    "v_mfma_f32_16x16x32_bf16 v[68:71], v[164:167], v[208:211], v[68:71]\n"
    "ds_read_b128 v[164:167], v243 offset:16384\n"
    "v_mfma_f32_16x16x32_bf16 v[80:83], v[168:171], v[208:211], v[80:83]\n"
    "ds_read_b128 v[168:171], v242 offset:20480\n"
    "s_add_u32 m0, s56, 32768\n"
    "s_add_u32 s61, s65, 57344\n"
    "s_and_b32 s61, s61, 0x1ffff\n"
    "buffer_load_dwordx4 v247, s[48:51], s61 offen offset:2048 nt lds\n"
    "v_mfma_f32_16x16x32_bf16 v[92:95], v[172:175], v[208:211], v[92:95]\n"
    "ds_read_b128 v[172:175], v243 offset:20480\n"
    "v_mfma_f32_16x16x32_bf16 v[220:223], v[236:239], v[208:211], v[220:223]\n"
    "s_waitcnt lgkmcnt(4)\n"
    "v_mfma_f32_16x16x32_bf16 v[176:179], v[144:147], v[96:99], v[224:227]\n"
    "v_mfma_f32_16x16x32_bf16 v[180:183], v[144:147], v[100:103], v[228:231]\n"
    "v_mfma_f32_16x16x32_bf16 v[184:187], v[144:147], v[104:107], v[232:235]\n"
    "ds_read_b64_tr_b16 v[144:145], v244 offset:0\n"
    "ds_read_b64_tr_b16 v[146:147], v244 offset:16384\n"
    "v_mfma_f32_16x16x32_bf16 v[176:179], v[148:151], v[108:111], v[176:179]\n"
    "v_mfma_f32_16x16x32_bf16 v[180:183], v[148:151], v[112:115], v[180:183]\n"
    "v_mfma_f32_16x16x32_bf16 v[184:187], v[148:151], v[116:119], v[184:187]\n"
    "ds_read_b64_tr_b16 v[148:149], v245 offset:0\n"
    "ds_read_b64_tr_b16 v[150:151], v245 offset:16384\n"
    "v_mfma_f32_16x16x32_bf16 v[176:179], v[152:155], v[120:123], v[176:179]\n"
    "v_mfma_f32_16x16x32_bf16 v[180:183], v[152:155], v[124:127], v[180:183]\n"
    "v_mfma_f32_16x16x32_bf16 v[184:187], v[152:155], v[128:131], v[184:187]\n"
    "ds_read_b64_tr_b16 v[152:153], v244 offset:512\n"
    "ds_read_b64_tr_b16 v[154:155], v244 offset:16896\n"
    "v_mfma_f32_16x16x32_bf16 v[176:179], v[156:159], v[132:135], v[176:179]\n"
    "v_add_u32_e32 v242, 32768, v242\n"
    "v_mfma_f32_16x16x32_bf16 v[180:183], v[156:159], v[136:139], v[180:183]\n"
    "v_xor_b32_e32 v243, 64, v242\n"
    "v_mfma_f32_16x16x32_bf16 v[184:187], v[156:159], v[140:143], v[184:187]\n"
    "ds_read_b64_tr_b16 v[156:157], v245 offset:512\n"
    "ds_read_b64_tr_b16 v[158:159], v245 offset:16896\n"
    "s_waitcnt lgkmcnt(8)\n"
    "v_mfma_f32_16x16x32_bf16 v[188:191], v[160:163], v[96:99], v[224:227]\n"
    "v_mfma_f32_16x16x32_bf16 v[188:191], v[164:167], v[108:111], v[188:191]\n"
    "v_exp_f32_e32 v248, v176\n"
    "v_exp_f32_e32 v249, v177\n"
    "v_exp_f32_e32 v250, v178\n"
    "s_add_u32 m0, s56, 32768\n"
    "s_add_u32 s61, s65, 57344\n"
    "s_and_b32 s61, s61, 0x1ffff\n"
    "s_add_u32 s61, s61, 130048\n"
    "buffer_load_dwordx4 v247, s[48:51], s61 offen offset:3072 nt lds\n"
    "v_mfma_f32_16x16x32_bf16 v[188:191], v[168:171], v[120:123], v[188:191]\n"
    "v_cvt_pk_bf16_f32 v200, v248, v249\n"
    "v_exp_f32_e32 v251, v179\n"
    "v_mfma_f32_16x16x32_bf16 v[188:191], v[172:175], v[132:135], v[188:191]\n"
    "v_exp_f32_e32 v248, v180\n"
    "v_cvt_pk_bf16_f32 v201, v250, v251\n"
    "v_mfma_f32_16x16x32_bf16 v[192:195], v[160:163], v[100:103], v[228:231]\n"
    "v_exp_f32_e32 v249, v181\n"
    "v_exp_f32_e32 v250, v182\n"
    "v_mfma_f32_16x16x32_bf16 v[192:195], v[164:167], v[112:115], v[192:195]\n"
    "v_cvt_pk_bf16_f32 v204, v248, v249\n"
    "v_exp_f32_e32 v251, v183\n"
    "v_mfma_f32_16x16x32_bf16 v[192:195], v[168:171], v[124:127], v[192:195]\n"
    "v_exp_f32_e32 v248, v188\n"
    "v_cvt_pk_bf16_f32 v205, v250, v251\n"
    "v_mfma_f32_16x16x32_bf16 v[192:195], v[172:175], v[136:139], v[192:195]\n"
    "v_exp_f32_e32 v249, v189\n"
    "v_exp_f32_e32 v250, v190\n"
    "v_mfma_f32_16x16x32_bf16 v[196:199], v[160:163], v[104:107], v[232:235]\n"
    "ds_read_b64_tr_b16 v[160:161], v244 offset:4096\n"
    "ds_read_b64_tr_b16 v[162:163], v244 offset:20480\n"
    "v_cvt_pk_bf16_f32 v202, v248, v249\n"
    "v_exp_f32_e32 v251, v191\n"
    "v_mfma_f32_16x16x32_bf16 v[196:199], v[164:167], v[116:119], v[196:199]\n"
    "ds_read_b64_tr_b16 v[164:165], v245 offset:4096\n"
    "ds_read_b64_tr_b16 v[166:167], v245 offset:20480\n"
    "v_or3_b32 v255, v200, v201, v202\n"
    "v_cvt_pk_bf16_f32 v203, v250, v251\n"
    "v_mfma_f32_16x16x32_bf16 v[196:199], v[168:171], v[128:131], v[196:199]\n"
    "ds_read_b64_tr_b16 v[168:169], v244 offset:4608\n"
    "ds_read_b64_tr_b16 v[170:171], v244 offset:20992\n"
    "v_bitop3_b32 v255, v255, s63, v203 bitop3:0xc8\n"
    "v_cmp_ne_u32_e32 vcc, 0, v255\n"
    "v_mfma_f32_16x16x32_bf16 v[196:199], v[172:175], v[140:143], v[196:199]\n"
    "s_cbranch_vccnz .Lst3_0_%=\n"
    ".Lbk3_0_%=:\n"
    "ds_read_b64_tr_b16 v[172:173], v245 offset:4608\n"
    "ds_read_b64_tr_b16 v[174:175], v245 offset:20992\n"
    "v_exp_f32_e32 v248, v192\n"
    "v_exp_f32_e32 v249, v193\n"
    "s_waitcnt lgkmcnt(8)\n"
    "v_mfma_f32_16x16x32_bf16 v[0:3], v[144:147], v[200:203], v[0:3]\n"
    "v_exp_f32_e32 v250, v194\n"
    "v_cvt_pk_bf16_f32 v206, v248, v249\n"
    "v_mfma_f32_16x16x32_bf16 v[12:15], v[148:151], v[200:203], v[12:15]\n"
    "v_exp_f32_e32 v251, v195\n"
    "v_or3_b32 v255, v204, v205, v206\n"
    "s_add_u32 m0, s56, 32768\n"
    "s_add_u32 s61, s65, 57344\n"
    "s_and_b32 s61, s61, 0x1ffff\n"
    "buffer_load_dwordx4 v246, s[44:47], s61 offen offset:0 nt lds\n"
    "s_add_u32 m0, s56, 32768\n"
    "s_add_u32 s61, s65, 57344\n"
    "s_and_b32 s61, s61, 0x1ffff\n"
    "s_add_u32 s61, s61, 130048\n"
    "buffer_load_dwordx4 v246, s[44:47], s61 offen offset:1024 nt lds\n"
    "v_mfma_f32_16x16x32_bf16 v[24:27], v[152:155], v[200:203], v[24:27]\n"
    "v_cvt_pk_bf16_f32 v207, v250, v251\n"
    "v_bitop3_b32 v255, v255, s63, v207 bitop3:0xc8\n"
    "v_mfma_f32_16x16x32_bf16 v[36:39], v[156:159], v[200:203], v[36:39]\n"
    "v_cmp_ne_u32_e32 vcc, 0, v255\n"
    "v_mfma_f32_16x16x32_bf16 v[212:215], v[236:239], v[200:203], v[212:215]\n"
    "s_cbranch_vccnz .Lst3_1_%=\n"
    ".Lbk3_1_%=:\n"
    "v_exp_f32_e32 v248, v184\n"
    "v_exp_f32_e32 v249, v185\n"
    "v_mfma_f32_16x16x32_bf16 v[4:7], v[144:147], v[204:207], v[4:7]\n"
    "v_exp_f32_e32 v250, v186\n"
    "v_cvt_pk_bf16_f32 v208, v248, v249\n"
    "v_mfma_f32_16x16x32_bf16 v[16:19], v[148:151], v[204:207], v[16:19]\n"
    "v_exp_f32_e32 v251, v187\n"
    "s_add_u32 s61, s65, 65536\n"
    "s_and_b32 s61, s61, 0x1ffff\n"
    "buffer_load_dword v241, v240, s[44:47], s61 offen offset:0\n"
    "v_mfma_f32_16x16x32_bf16 v[28:31], v[152:155], v[204:207], v[28:31]\n"
    "v_exp_f32_e32 v248, v196\n"
    "v_cvt_pk_bf16_f32 v209, v250, v251\n"
    "v_mfma_f32_16x16x32_bf16 v[40:43], v[156:159], v[204:207], v[40:43]\n"
    "v_exp_f32_e32 v249, v197\n"
    "v_exp_f32_e32 v250, v198\n"
    "v_mfma_f32_16x16x32_bf16 v[216:219], v[236:239], v[204:207], v[216:219]\n"
    "v_cvt_pk_bf16_f32 v210, v248, v249\n"
    "s_waitcnt lgkmcnt(0)\n"
    "v_mfma_f32_16x16x32_bf16 v[48:51], v[160:163], v[200:203], v[48:51]\n"
    "v_exp_f32_e32 v251, v199\n"
    "v_or3_b32 v255, v208, v209, v210\n"
    "v_mfma_f32_16x16x32_bf16 v[60:63], v[164:167], v[200:203], v[60:63]\n"
    "v_cvt_pk_bf16_f32 v211, v250, v251\n"
    "v_bitop3_b32 v255, v255, s63, v211 bitop3:0xc8\n"
    "v_mfma_f32_16x16x32_bf16 v[72:75], v[168:171], v[200:203], v[72:75]\n"
    "v_cmp_ne_u32_e32 vcc, 0, v255\n"
    "v_mfma_f32_16x16x32_bf16 v[84:87], v[172:175], v[200:203], v[84:87]\n"
    "s_cbranch_vccnz .Lst3_2_%=\n"
    ".Lbk3_2_%=:\n"
    "s_waitcnt vmcnt(17)\n"
    "s_barrier\n"
    "v_mfma_f32_16x16x32_bf16 v[8:11], v[144:147], v[208:211], v[8:11]\n"
    "ds_read_b128 v[144:147], v242 offset:0\n"
    "v_mfma_f32_16x16x32_bf16 v[20:23], v[148:151], v[208:211], v[20:23]\n"
    "ds_read_b128 v[148:151], v243 offset:0\n"
    "v_add_u32_e32 v244, 32768, v244\n"
    "v_mfma_f32_16x16x32_bf16 v[32:35], v[152:155], v[208:211], v[32:35]\n"
    "ds_read_b128 v[152:155], v242 offset:4096\n"
    "v_xor_b32_e32 v245, 32, v244\n"
    "v_mfma_f32_16x16x32_bf16 v[44:47], v[156:159], v[208:211], v[44:47]\n"
    "ds_read_b128 v[156:159], v243 offset:4096\n"
    "v_mfma_f32_16x16x32_bf16 v[52:55], v[160:163], v[204:207], v[52:55]\n"
    "v_mfma_f32_16x16x32_bf16 v[64:67], v[164:167], v[204:207], v[64:67]\n"
    "v_mfma_f32_16x16x32_bf16 v[76:79], v[168:171], v[204:207], v[76:79]\n"
    "v_mfma_f32_16x16x32_bf16 v[88:91], v[172:175], v[204:207], v[88:91]\n"
    "v_mfma_f32_16x16x32_bf16 v[56:59], v[160:163], v[208:211], v[56:59]\n"
    "ds_read_b128 v[160:163], v242 offset:16384\n"
    "v_mfma_f32_16x16x32_bf16 v[68:71], v[164:167], v[208:211], v[68:71]\n"
    "ds_read_b128 v[164:167], v243 offset:16384\n"
    "v_mfma_f32_16x16x32_bf16 v[80:83], v[168:171], v[208:211], v[80:83]\n"
    "ds_read_b128 v[168:171], v242 offset:20480\n"
    "v_mfma_f32_16x16x32_bf16 v[92:95], v[172:175], v[208:211], v[92:95]\n"
    "ds_read_b128 v[172:175], v243 offset:20480\n"
    "v_mfma_f32_16x16x32_bf16 v[220:223], v[236:239], v[208:211], v[220:223]\n"
    "s_waitcnt lgkmcnt(4)\n"
    "v_mfma_f32_16x16x32_bf16 v[176:179], v[144:147], v[96:99], v[224:227]\n"
    "v_mfma_f32_16x16x32_bf16 v[180:183], v[144:147], v[100:103], v[228:231]\n"
    "v_mfma_f32_16x16x32_bf16 v[184:187], v[144:147], v[104:107], v[232:235]\n"
    "ds_read_b64_tr_b16 v[144:145], v244 offset:0\n"
    "ds_read_b64_tr_b16 v[146:147], v244 offset:16384\n"
    "v_mfma_f32_16x16x32_bf16 v[176:179], v[148:151], v[108:111], v[176:179]\n"
    "v_mfma_f32_16x16x32_bf16 v[180:183], v[148:151], v[112:115], v[180:183]\n"
    "v_mfma_f32_16x16x32_bf16 v[184:187], v[148:151], v[116:119], v[184:187]\n"
    "ds_read_b64_tr_b16 v[148:149], v245 offset:0\n"
    "ds_read_b64_tr_b16 v[150:151], v245 offset:16384\n"
    "v_mfma_f32_16x16x32_bf16 v[176:179], v[152:155], v[120:123], v[176:179]\n"
    "v_mfma_f32_16x16x32_bf16 v[180:183], v[152:155], v[124:127], v[180:183]\n"
    "v_mfma_f32_16x16x32_bf16 v[184:187], v[152:155], v[128:131], v[184:187]\n"
    "ds_read_b64_tr_b16 v[152:153], v244 offset:512\n"
    "ds_read_b64_tr_b16 v[154:155], v244 offset:16896\n"
    "v_mfma_f32_16x16x32_bf16 v[176:179], v[156:159], v[132:135], v[176:179]\n"
    "v_add_u32_e32 v242, 32768, v242\n"
    "v_mfma_f32_16x16x32_bf16 v[180:183], v[156:159], v[136:139], v[180:183]\n"
    "v_xor_b32_e32 v243, 64, v242\n"
    "v_mfma_f32_16x16x32_bf16 v[184:187], v[156:159], v[140:143], v[184:187]\n"
    "ds_read_b64_tr_b16 v[156:157], v245 offset:512\n"
    "ds_read_b64_tr_b16 v[158:159], v245 offset:16896\n"
    "s_waitcnt lgkmcnt(8)\n"
    "s_add_u32 m0, s56, 65536\n"
    "s_add_u32 s61, s65, 65536\n"
    "s_and_b32 s61, s61, 0x1ffff\n"
    "buffer_load_dwordx4 v247, s[48:51], s61 offen offset:2048 nt lds\n"
    "v_mfma_f32_16x16x32_bf16 v[188:191], v[160:163], v[96:99], v[224:227]\n"
    "v_mfma_f32_16x16x32_bf16 v[188:191], v[164:167], v[108:111], v[188:191]\n"
    "v_exp_f32_e32 v248, v176\n"
    "v_exp_f32_e32 v249, v177\n"
    "v_exp_f32_e32 v250, v178\n"
    "v_mfma_f32_16x16x32_bf16 v[188:191], v[168:171], v[120:123], v[188:191]\n"
    "v_cvt_pk_bf16_f32 v200, v248, v249\n"
    "v_exp_f32_e32 v251, v179\n"
    "v_mfma_f32_16x16x32_bf16 v[188:191], v[172:175], v[132:135], v[188:191]\n"
    "v_exp_f32_e32 v248, v180\n"
    "v_cvt_pk_bf16_f32 v201, v250, v251\n"
    "v_mfma_f32_16x16x32_bf16 v[192:195], v[160:163], v[100:103], v[228:231]\n"
    "v_exp_f32_e32 v249, v181\n"
    "v_exp_f32_e32 v250, v182\n"
    "v_mfma_f32_16x16x32_bf16 v[192:195], v[164:167], v[112:115], v[192:195]\n"
    "v_cvt_pk_bf16_f32 v204, v248, v249\n"
    "v_exp_f32_e32 v251, v183\n"
    "v_mfma_f32_16x16x32_bf16 v[192:195], v[168:171], v[124:127], v[192:195]\n"
    "v_exp_f32_e32 v248, v188\n"
    "v_cvt_pk_bf16_f32 v205, v250, v251\n"
    "v_mfma_f32_16x16x32_bf16 v[192:195], v[172:175], v[136:139], v[192:195]\n"
    "v_exp_f32_e32 v249, v189\n"
    "v_exp_f32_e32 v250, v190\n"
    "v_mfma_f32_16x16x32_bf16 v[196:199], v[160:163], v[104:107], v[232:235]\n"
    "ds_read_b64_tr_b16 v[160:161], v244 offset:4096\n"
    "ds_read_b64_tr_b16 v[162:163], v244 offset:20480\n"
    "v_cvt_pk_bf16_f32 v202, v248, v249\n"
    "v_exp_f32_e32 v251, v191\n"
    "v_mfma_f32_16x16x32_bf16 v[196:199], v[164:167], v[116:119], v[196:199]\n"
    "ds_read_b64_tr_b16 v[164:165], v245 offset:4096\n"
    "ds_read_b64_tr_b16 v[166:167], v245 offset:20480\n"
    "v_or3_b32 v255, v200, v201, v202\n"
    "v_cvt_pk_bf16_f32 v203, v250, v251\n"
    "v_mfma_f32_16x16x32_bf16 v[196:199], v[168:171], v[128:131], v[196:199]\n"
    "ds_read_b64_tr_b16 v[168:169], v244 offset:4608\n"
    "ds_read_b64_tr_b16 v[170:171], v244 offset:20992\n"
    "v_bitop3_b32 v255, v255, s63, v203 bitop3:0xc8\n"
    "v_cmp_ne_u32_e32 vcc, 0, v255\n"
    "v_mfma_f32_16x16x32_bf16 v[196:199], v[172:175], v[140:143], v[196:199]\n"
    "s_cbranch_vccnz .Lst4_0_%=\n"
    ".Lbk4_0_%=:\n"
    "ds_read_b64_tr_b16 v[172:173], v245 offset:4608\n"
    "ds_read_b64_tr_b16 v[174:175], v245 offset:20992\n"
    "v_exp_f32_e32 v248, v192\n"
    "v_exp_f32_e32 v249, v193\n"
    "s_waitcnt lgkmcnt(8)\n"
    "v_mfma_f32_16x16x32_bf16 v[0:3], v[144:147], v[200:203], v[0:3]\n"
    "v_exp_f32_e32 v250, v194\n"
    "v_cvt_pk_bf16_f32 v206, v248, v249\n"
    "v_mfma_f32_16x16x32_bf16 v[12:15], v[148:151], v[200:203], v[12:15]\n"
    "v_exp_f32_e32 v251, v195\n"
    "v_or3_b32 v255, v204, v205, v206\n"
    "v_mfma_f32_16x16x32_bf16 v[24:27], v[152:155], v[200:203], v[24:27]\n"
    "v_cvt_pk_bf16_f32 v207, v250, v251\n"
    "v_bitop3_b32 v255, v255, s63, v207 bitop3:0xc8\n"
    "v_mfma_f32_16x16x32_bf16 v[36:39], v[156:159], v[200:203], v[36:39]\n"
    "v_cmp_ne_u32_e32 vcc, 0, v255\n"
    "s_add_u32 m0, s56, 65536\n"
    "s_add_u32 s61, s65, 65536\n"
    "s_and_b32 s61, s61, 0x1ffff\n"
    "s_add_u32 s61, s61, 130048\n"
    "buffer_load_dwordx4 v247, s[48:51], s61 offen offset:3072 nt lds\n"
    "v_mfma_f32_16x16x32_bf16 v[212:215], v[236:239], v[200:203], v[212:215]\n"
    "s_cbranch_vccnz .Lst4_1_%=\n"
    ".Lbk4_1_%=:\n"
    "v_exp_f32_e32 v248, v184\n"
    "v_exp_f32_e32 v249, v185\n"
    "v_mfma_f32_16x16x32_bf16 v[4:7], v[144:147], v[204:207], v[4:7]\n"
    "v_exp_f32_e32 v250, v186\n"
    "v_cvt_pk_bf16_f32 v208, v248, v249\n"
    "v_mfma_f32_16x16x32_bf16 v[16:19], v[148:151], v[204:207], v[16:19]\n"
    "v_exp_f32_e32 v251, v187\n"
    "v_mfma_f32_16x16x32_bf16 v[28:31], v[152:155], v[204:207], v[28:31]\n"
    "v_exp_f32_e32 v248, v196\n"
    "v_cvt_pk_bf16_f32 v209, v250, v251\n"
    "v_mfma_f32_16x16x32_bf16 v[40:43], v[156:159], v[204:207], v[40:43]\n"
    "v_exp_f32_e32 v249, v197\n"
    "v_exp_f32_e32 v250, v198\n"
    "v_mfma_f32_16x16x32_bf16 v[216:219], v[236:239], v[204:207], v[216:219]\n"
    "v_cvt_pk_bf16_f32 v210, v248, v249\n"
    "s_waitcnt lgkmcnt(0)\n"
    "v_mfma_f32_16x16x32_bf16 v[48:51], v[160:163], v[200:203], v[48:51]\n"
    "v_exp_f32_e32 v251, v199\n"
    "v_or3_b32 v255, v208, v209, v210\n"
    "v_mfma_f32_16x16x32_bf16 v[60:63], v[164:167], v[200:203], v[60:63]\n"
    "v_cvt_pk_bf16_f32 v211, v250, v251\n"
    "v_bitop3_b32 v255, v255, s63, v211 bitop3:0xc8\n"
    "v_mfma_f32_16x16x32_bf16 v[72:75], v[168:171], v[200:203], v[72:75]\n"
    "v_cmp_ne_u32_e32 vcc, 0, v255\n"
    "v_mfma_f32_16x16x32_bf16 v[84:87], v[172:175], v[200:203], v[84:87]\n"
    "s_cbranch_vccnz .Lst4_2_%=\n"
    ".Lbk4_2_%=:\n"
    "s_waitcnt vmcnt(13)\n"
    "s_barrier\n"
    "v_mfma_f32_16x16x32_bf16 v[8:11], v[144:147], v[208:211], v[8:11]\n"
    "ds_read_b128 v[144:147], v242 offset:0\n"
    "v_mfma_f32_16x16x32_bf16 v[20:23], v[148:151], v[208:211], v[20:23]\n"
    "ds_read_b128 v[148:151], v243 offset:0\n"
    "v_add_u32_e32 v244, 32768, v244\n"
    "s_add_u32 m0, s56, 65536\n"
    "s_add_u32 s61, s65, 65536\n"
    "s_and_b32 s61, s61, 0x1ffff\n"
    "buffer_load_dwordx4 v246, s[44:47], s61 offen offset:0 nt lds\n"
    "s_add_u32 m0, s56, 65536\n"
    "s_add_u32 s61, s65, 65536\n"
    "s_and_b32 s61, s61, 0x1ffff\n"
    "s_add_u32 s61, s61, 130048\n"
    "buffer_load_dwordx4 v246, s[44:47], s61 offen offset:1024 nt lds\n"
    "v_mfma_f32_16x16x32_bf16 v[32:35], v[152:155], v[208:211], v[32:35]\n"
    "ds_read_b128 v[152:155], v242 offset:4096\n"
    "v_xor_b32_e32 v245, 32, v244\n"
    "v_mfma_f32_16x16x32_bf16 v[44:47], v[156:159], v[208:211], v[44:47]\n"
    "ds_read_b128 v[156:159], v243 offset:4096\n"
    "v_mfma_f32_16x16x32_bf16 v[52:55], v[160:163], v[204:207], v[52:55]\n"
    "v_mfma_f32_16x16x32_bf16 v[64:67], v[164:167], v[204:207], v[64:67]\n"
    "s_add_u32 m0, s56, 98304\n"
    "s_add_u32 s61, s65, 73728\n"
    "s_and_b32 s61, s61, 0x1ffff\n"
    "buffer_load_dwordx4 v246, s[44:47], s61 offen offset:0 nt lds\n"
    "v_mfma_f32_16x16x32_bf16 v[76:79], v[168:171], v[204:207], v[76:79]\n"
    "v_mfma_f32_16x16x32_bf16 v[88:91], v[172:175], v[204:207], v[88:91]\n"
    "v_mfma_f32_16x16x32_bf16 v[56:59], v[160:163], v[208:211], v[56:59]\n"
    "ds_read_b128 v[160:163], v242 offset:16384\n"
    "v_mfma_f32_16x16x32_bf16 v[68:71], v[164:167], v[208:211], v[68:71]\n"
    "ds_read_b128 v[164:167], v243 offset:16384\n"
    "v_mfma_f32_16x16x32_bf16 v[80:83], v[168:171], v[208:211], v[80:83]\n"
    "ds_read_b128 v[168:171], v242 offset:20480\n"
    "v_mfma_f32_16x16x32_bf16 v[92:95], v[172:175], v[208:211], v[92:95]\n"
    "ds_read_b128 v[172:175], v243 offset:20480\n"
    "v_mfma_f32_16x16x32_bf16 v[220:223], v[236:239], v[208:211], v[220:223]\n"
    "s_waitcnt lgkmcnt(4)\n"
    "v_mfma_f32_16x16x32_bf16 v[176:179], v[144:147], v[96:99], v[224:227]\n"
    "v_mfma_f32_16x16x32_bf16 v[180:183], v[144:147], v[100:103], v[228:231]\n"
    "v_mfma_f32_16x16x32_bf16 v[184:187], v[144:147], v[104:107], v[232:235]\n"
    "ds_read_b64_tr_b16 v[144:145], v244 offset:0\n"
    "ds_read_b64_tr_b16 v[146:147], v244 offset:16384\n"
    "v_mfma_f32_16x16x32_bf16 v[176:179], v[148:151], v[108:111], v[176:179]\n"
    "v_mfma_f32_16x16x32_bf16 v[180:183], v[148:151], v[112:115], v[180:183]\n"
    "v_mfma_f32_16x16x32_bf16 v[184:187], v[148:151], v[116:119], v[184:187]\n"
    "ds_read_b64_tr_b16 v[148:149], v245 offset:0\n"
    "ds_read_b64_tr_b16 v[150:151], v245 offset:16384\n"
    "v_mfma_f32_16x16x32_bf16 v[176:179], v[152:155], v[120:123], v[176:179]\n"
    "v_mfma_f32_16x16x32_bf16 v[180:183], v[152:155], v[124:127], v[180:183]\n"
    "v_mfma_f32_16x16x32_bf16 v[184:187], v[152:155], v[128:131], v[184:187]\n"
    "ds_read_b64_tr_b16 v[152:153], v244 offset:512\n"
    "ds_read_b64_tr_b16 v[154:155], v244 offset:16896\n"
    "s_add_u32 m0, s56, 98304\n"
    "s_add_u32 s61, s65, 73728\n"
    "s_and_b32 s61, s61, 0x1ffff\n"
    "s_add_u32 s61, s61, 130048\n"
    "buffer_load_dwordx4 v246, s[44:47], s61 offen offset:1024 nt lds\n"
    "v_mfma_f32_16x16x32_bf16 v[176:179], v[156:159], v[132:135], v[176:179]\n"
    "v_add_u32_e32 v242, 4294836224, v242\n"
    "v_mfma_f32_16x16x32_bf16 v[180:183], v[156:159], v[136:139], v[180:183]\n"
    "v_xor_b32_e32 v243, 64, v242\n"
    "v_mfma_f32_16x16x32_bf16 v[184:187], v[156:159], v[140:143], v[184:187]\n"
    "ds_read_b64_tr_b16 v[156:157], v245 offset:512\n"
    "ds_read_b64_tr_b16 v[158:159], v245 offset:16896\n"
    "s_waitcnt lgkmcnt(8)\n"
    "v_mfma_f32_16x16x32_bf16 v[188:191], v[160:163], v[96:99], v[224:227]\n"
    "v_mfma_f32_16x16x32_bf16 v[188:191], v[164:167], v[108:111], v[188:191]\n"
    "v_exp_f32_e32 v248, v176\n"
    "v_exp_f32_e32 v249, v177\n"
    "v_exp_f32_e32 v250, v178\n"
    "v_mfma_f32_16x16x32_bf16 v[188:191], v[168:171], v[120:123], v[188:191]\n"
    "v_cvt_pk_bf16_f32 v200, v248, v249\n"
    "v_exp_f32_e32 v251, v179\n"
    "v_mfma_f32_16x16x32_bf16 v[188:191], v[172:175], v[132:135], v[188:191]\n"
    "v_exp_f32_e32 v248, v180\n"
    "v_cvt_pk_bf16_f32 v201, v250, v251\n"
    "v_mfma_f32_16x16x32_bf16 v[192:195], v[160:163], v[100:103], v[228:231]\n"
    "v_exp_f32_e32 v249, v181\n"
    "v_exp_f32_e32 v250, v182\n"
    "v_mfma_f32_16x16x32_bf16 v[192:195], v[164:167], v[112:115], v[192:195]\n"
    "v_cvt_pk_bf16_f32 v204, v248, v249\n"
    "v_exp_f32_e32 v251, v183\n"
    "v_mfma_f32_16x16x32_bf16 v[192:195], v[168:171], v[124:127], v[192:195]\n"
    "v_exp_f32_e32 v248, v188\n"
    "v_cvt_pk_bf16_f32 v205, v250, v251\n"
    "v_mfma_f32_16x16x32_bf16 v[192:195], v[172:175], v[136:139], v[192:195]\n"
    "v_exp_f32_e32 v249, v189\n"
    "v_exp_f32_e32 v250, v190\n"
    "v_mfma_f32_16x16x32_bf16 v[196:199], v[160:163], v[104:107], v[232:235]\n"
    "ds_read_b64_tr_b16 v[160:161], v244 offset:4096\n"
    "ds_read_b64_tr_b16 v[162:163], v244 offset:20480\n"
    "v_cvt_pk_bf16_f32 v202, v248, v249\n"
    "v_exp_f32_e32 v251, v191\n"
    "v_mfma_f32_16x16x32_bf16 v[196:199], v[164:167], v[116:119], v[196:199]\n"
    "ds_read_b64_tr_b16 v[164:165], v245 offset:4096\n"
    "ds_read_b64_tr_b16 v[166:167], v245 offset:20480\n"
    "v_or3_b32 v255, v200, v201, v202\n"
    "v_cvt_pk_bf16_f32 v203, v250, v251\n"
    "v_mfma_f32_16x16x32_bf16 v[196:199], v[168:171], v[128:131], v[196:199]\n"
    "ds_read_b64_tr_b16 v[168:169], v244 offset:4608\n"
    "ds_read_b64_tr_b16 v[170:171], v244 offset:20992\n"
    "v_bitop3_b32 v255, v255, s63, v203 bitop3:0xc8\n"
    "v_cmp_ne_u32_e32 vcc, 0, v255\n"
    "v_mfma_f32_16x16x32_bf16 v[196:199], v[172:175], v[140:143], v[196:199]\n"
    "s_cbranch_vccnz .Lst5_0_%=\n"
    ".Lbk5_0_%=:\n"
    "ds_read_b64_tr_b16 v[172:173], v245 offset:4608\n"
    "ds_read_b64_tr_b16 v[174:175], v245 offset:20992\n"
    "v_exp_f32_e32 v248, v192\n"
    "v_exp_f32_e32 v249, v193\n"
    "s_waitcnt lgkmcnt(8)\n"
    "v_mfma_f32_16x16x32_bf16 v[0:3], v[144:147], v[200:203], v[0:3]\n"
    "v_exp_f32_e32 v250, v194\n"
    "v_cvt_pk_bf16_f32 v206, v248, v249\n"
    "v_mfma_f32_16x16x32_bf16 v[12:15], v[148:151], v[200:203], v[12:15]\n"
    "v_exp_f32_e32 v251, v195\n"
    "v_or3_b32 v255, v204, v205, v206\n"
    "s_add_u32 m0, s56, 98304\n"
    "s_add_u32 s61, s65, 73728\n"
    "s_and_b32 s61, s61, 0x1ffff\n"
    "buffer_load_dwordx4 v247, s[48:51], s61 offen offset:2048 nt lds\n"
    "v_mfma_f32_16x16x32_bf16 v[24:27], v[152:155], v[200:203], v[24:27]\n"
    "v_cvt_pk_bf16_f32 v207, v250, v251\n"
    "v_bitop3_b32 v255, v255, s63, v207 bitop3:0xc8\n"
    "v_mfma_f32_16x16x32_bf16 v[36:39], v[156:159], v[200:203], v[36:39]\n"
    "v_cmp_ne_u32_e32 vcc, 0, v255\n"
    "v_mfma_f32_16x16x32_bf16 v[212:215], v[236:239], v[200:203], v[212:215]\n"
    "s_cbranch_vccnz .Lst5_1_%=\n"
    ".Lbk5_1_%=:\n"
    "v_exp_f32_e32 v248, v184\n"
    "v_exp_f32_e32 v249, v185\n"
    "v_mfma_f32_16x16x32_bf16 v[4:7], v[144:147], v[204:207], v[4:7]\n"
    "v_exp_f32_e32 v250, v186\n"
    "v_cvt_pk_bf16_f32 v208, v248, v249\n"
    "v_mfma_f32_16x16x32_bf16 v[16:19], v[148:151], v[204:207], v[16:19]\n"
    "v_exp_f32_e32 v251, v187\n"
    "v_mfma_f32_16x16x32_bf16 v[28:31], v[152:155], v[204:207], v[28:31]\n"
    "v_exp_f32_e32 v248, v196\n"
    "v_cvt_pk_bf16_f32 v209, v250, v251\n"
    "v_mfma_f32_16x16x32_bf16 v[40:43], v[156:159], v[204:207], v[40:43]\n"
    "v_exp_f32_e32 v249, v197\n"
    "v_exp_f32_e32 v250, v198\n"
    "v_mfma_f32_16x16x32_bf16 v[216:219], v[236:239], v[204:207], v[216:219]\n"
    "v_cvt_pk_bf16_f32 v210, v248, v249\n"
    "s_waitcnt lgkmcnt(0)\n"
    "v_mfma_f32_16x16x32_bf16 v[48:51], v[160:163], v[200:203], v[48:51]\n"
    "v_exp_f32_e32 v251, v199\n"
    "v_or3_b32 v255, v208, v209, v210\n"
    "v_mfma_f32_16x16x32_bf16 v[60:63], v[164:167], v[200:203], v[60:63]\n"
    "v_cvt_pk_bf16_f32 v211, v250, v251\n"
    "v_bitop3_b32 v255, v255, s63, v211 bitop3:0xc8\n"
    "v_mfma_f32_16x16x32_bf16 v[72:75], v[168:171], v[200:203], v[72:75]\n"
    "v_cmp_ne_u32_e32 vcc, 0, v255\n"
    "v_mfma_f32_16x16x32_bf16 v[84:87], v[172:175], v[200:203], v[84:87]\n"
    "s_cbranch_vccnz .Lst5_2_%=\n"
    ".Lbk5_2_%=:\n"
    "s_waitcnt vmcnt(12)\n"
    "s_barrier\n"
    "v_mfma_f32_16x16x32_bf16 v[8:11], v[144:147], v[208:211], v[8:11]\n"
    "ds_read_b128 v[144:147], v242 offset:0\n"
    "v_mfma_f32_16x16x32_bf16 v[20:23], v[148:151], v[208:211], v[20:23]\n"
    "ds_read_b128 v[148:151], v243 offset:0\n"
    "v_add_u32_e32 v244, 4294836224, v244\n"
    "v_mfma_f32_16x16x32_bf16 v[32:35], v[152:155], v[208:211], v[32:35]\n"
    "ds_read_b128 v[152:155], v242 offset:4096\n"
    "v_xor_b32_e32 v245, 32, v244\n"
    "v_mfma_f32_16x16x32_bf16 v[44:47], v[156:159], v[208:211], v[44:47]\n"
    "ds_read_b128 v[156:159], v243 offset:4096\n"
    "s_add_u32 m0, s56, 98304\n"
    "s_add_u32 s61, s65, 73728\n"
    "s_and_b32 s61, s61, 0x1ffff\n"
    "s_add_u32 s61, s61, 130048\n"
    "buffer_load_dwordx4 v247, s[48:51], s61 offen offset:3072 nt lds\n"
    "v_mfma_f32_16x16x32_bf16 v[52:55], v[160:163], v[204:207], v[52:55]\n"
    "v_mfma_f32_16x16x32_bf16 v[64:67], v[164:167], v[204:207], v[64:67]\n"
    "v_mfma_f32_16x16x32_bf16 v[76:79], v[168:171], v[204:207], v[76:79]\n"
    "v_mfma_f32_16x16x32_bf16 v[88:91], v[172:175], v[204:207], v[88:91]\n"
    "v_mfma_f32_16x16x32_bf16 v[56:59], v[160:163], v[208:211], v[56:59]\n"
    "ds_read_b128 v[160:163], v242 offset:16384\n"
    "v_mfma_f32_16x16x32_bf16 v[68:71], v[164:167], v[208:211], v[68:71]\n"
    "ds_read_b128 v[164:167], v243 offset:16384\n"
    "v_mfma_f32_16x16x32_bf16 v[80:83], v[168:171], v[208:211], v[80:83]\n"
    "ds_read_b128 v[168:171], v242 offset:20480\n"
    "v_mfma_f32_16x16x32_bf16 v[92:95], v[172:175], v[208:211], v[92:95]\n"
    "ds_read_b128 v[172:175], v243 offset:20480\n"
    "v_mfma_f32_16x16x32_bf16 v[220:223], v[236:239], v[208:211], v[220:223]\n"
    "s_waitcnt lgkmcnt(4)\n"
    "v_mfma_f32_16x16x32_bf16 v[176:179], v[144:147], v[96:99], v[224:227]\n"
    "v_mfma_f32_16x16x32_bf16 v[180:183], v[144:147], v[100:103], v[228:231]\n"
    "v_mfma_f32_16x16x32_bf16 v[184:187], v[144:147], v[104:107], v[232:235]\n"
    "ds_read_b64_tr_b16 v[144:145], v244 offset:0\n"
    "ds_read_b64_tr_b16 v[146:147], v244 offset:16384\n"
    "v_mfma_f32_16x16x32_bf16 v[176:179], v[148:151], v[108:111], v[176:179]\n"
    "v_mfma_f32_16x16x32_bf16 v[180:183], v[148:151], v[112:115], v[180:183]\n"
    "v_mfma_f32_16x16x32_bf16 v[184:187], v[148:151], v[116:119], v[184:187]\n"
    "ds_read_b64_tr_b16 v[148:149], v245 offset:0\n"
    "ds_read_b64_tr_b16 v[150:151], v245 offset:16384\n"
    "v_mfma_f32_16x16x32_bf16 v[176:179], v[152:155], v[120:123], v[176:179]\n"
    "s_add_u32 m0, s56, 131072\n"
    "s_add_u32 s61, s65, 81920\n"
    "s_and_b32 s61, s61, 0x1ffff\n"
    "buffer_load_dwordx4 v246, s[44:47], s61 offen offset:0 nt lds\n"
    "v_mfma_f32_16x16x32_bf16 v[180:183], v[152:155], v[124:127], v[180:183]\n"
    "v_mfma_f32_16x16x32_bf16 v[184:187], v[152:155], v[128:131], v[184:187]\n"
    "ds_read_b64_tr_b16 v[152:153], v244 offset:512\n"
    "ds_read_b64_tr_b16 v[154:155], v244 offset:16896\n"
    "v_mfma_f32_16x16x32_bf16 v[176:179], v[156:159], v[132:135], v[176:179]\n"
    "v_add_u32_e32 v242, 32768, v242\n"
    "v_mfma_f32_16x16x32_bf16 v[180:183], v[156:159], v[136:139], v[180:183]\n"
    "v_xor_b32_e32 v243, 64, v242\n"
    "v_mfma_f32_16x16x32_bf16 v[184:187], v[156:159], v[140:143], v[184:187]\n"
    "ds_read_b64_tr_b16 v[156:157], v245 offset:512\n"
    "ds_read_b64_tr_b16 v[158:159], v245 offset:16896\n"
    "s_waitcnt lgkmcnt(8)\n"
    "v_mfma_f32_16x16x32_bf16 v[188:191], v[160:163], v[96:99], v[224:227]\n"
    "v_mfma_f32_16x16x32_bf16 v[188:191], v[164:167], v[108:111], v[188:191]\n"
    "v_exp_f32_e32 v248, v176\n"
    "v_exp_f32_e32 v249, v177\n"
    "v_exp_f32_e32 v250, v178\n"
    "v_mfma_f32_16x16x32_bf16 v[188:191], v[168:171], v[120:123], v[188:191]\n"
    "v_cvt_pk_bf16_f32 v200, v248, v249\n"
    "v_exp_f32_e32 v251, v179\n"
    "v_mfma_f32_16x16x32_bf16 v[188:191], v[172:175], v[132:135], v[188:191]\n"
    "v_exp_f32_e32 v248, v180\n"
    "v_cvt_pk_bf16_f32 v201, v250, v251\n"
    "v_mfma_f32_16x16x32_bf16 v[192:195], v[160:163], v[100:103], v[228:231]\n"
    "v_exp_f32_e32 v249, v181\n"
    "v_exp_f32_e32 v250, v182\n"
    "v_mfma_f32_16x16x32_bf16 v[192:195], v[164:167], v[112:115], v[192:195]\n"
    "v_cvt_pk_bf16_f32 v204, v248, v249\n"
    "v_exp_f32_e32 v251, v183\n"
    "v_mfma_f32_16x16x32_bf16 v[192:195], v[168:171], v[124:127], v[192:195]\n"
    "v_exp_f32_e32 v248, v188\n"
    "v_cvt_pk_bf16_f32 v205, v250, v251\n"
    "v_mfma_f32_16x16x32_bf16 v[192:195], v[172:175], v[136:139], v[192:195]\n"
    "v_exp_f32_e32 v249, v189\n"
    "v_exp_f32_e32 v250, v190\n"
    "v_mfma_f32_16x16x32_bf16 v[196:199], v[160:163], v[104:107], v[232:235]\n"
    "ds_read_b64_tr_b16 v[160:161], v244 offset:4096\n"
    "ds_read_b64_tr_b16 v[162:163], v244 offset:20480\n"
    "v_cvt_pk_bf16_f32 v202, v248, v249\n"
    "v_exp_f32_e32 v251, v191\n"
    "v_mfma_f32_16x16x32_bf16 v[196:199], v[164:167], v[116:119], v[196:199]\n"
    "ds_read_b64_tr_b16 v[164:165], v245 offset:4096\n"
    "ds_read_b64_tr_b16 v[166:167], v245 offset:20480\n"
    "v_or3_b32 v255, v200, v201, v202\n"
    "v_cvt_pk_bf16_f32 v203, v250, v251\n"
    "v_mfma_f32_16x16x32_bf16 v[196:199], v[168:171], v[128:131], v[196:199]\n"
    "ds_read_b64_tr_b16 v[168:169], v244 offset:4608\n"
    "ds_read_b64_tr_b16 v[170:171], v244 offset:20992\n"
    "v_bitop3_b32 v255, v255, s63, v203 bitop3:0xc8\n"
    "v_cmp_ne_u32_e32 vcc, 0, v255\n"
    "s_add_u32 m0, s56, 131072\n"
    "s_add_u32 s61, s65, 81920\n"
    "s_and_b32 s61, s61, 0x1ffff\n"
    "s_add_u32 s61, s61, 130048\n"
    "buffer_load_dwordx4 v246, s[44:47], s61 offen offset:1024 nt lds\n"
    "v_mfma_f32_16x16x32_bf16 v[196:199], v[172:175], v[140:143], v[196:199]\n"
    "s_cbranch_vccnz .Lst6_0_%=\n"
    ".Lbk6_0_%=:\n"
    "ds_read_b64_tr_b16 v[172:173], v245 offset:4608\n"
    "ds_read_b64_tr_b16 v[174:175], v245 offset:20992\n"
    "v_exp_f32_e32 v248, v192\n"
    "v_exp_f32_e32 v249, v193\n"
    "s_waitcnt lgkmcnt(8)\n"
    "v_mfma_f32_16x16x32_bf16 v[0:3], v[144:147], v[200:203], v[0:3]\n"
    "v_exp_f32_e32 v250, v194\n"
    "v_cvt_pk_bf16_f32 v206, v248, v249\n"
    "v_mfma_f32_16x16x32_bf16 v[12:15], v[148:151], v[200:203], v[12:15]\n"
    "v_exp_f32_e32 v251, v195\n"
    "v_or3_b32 v255, v204, v205, v206\n"
    "v_mfma_f32_16x16x32_bf16 v[24:27], v[152:155], v[200:203], v[24:27]\n"
    "v_cvt_pk_bf16_f32 v207, v250, v251\n"
    "v_bitop3_b32 v255, v255, s63, v207 bitop3:0xc8\n"
    "v_mfma_f32_16x16x32_bf16 v[36:39], v[156:159], v[200:203], v[36:39]\n"
    "v_cmp_ne_u32_e32 vcc, 0, v255\n"
    "v_mfma_f32_16x16x32_bf16 v[212:215], v[236:239], v[200:203], v[212:215]\n"
    "s_cbranch_vccnz .Lst6_1_%=\n"
    ".Lbk6_1_%=:\n"
    "v_exp_f32_e32 v248, v184\n"
    "v_exp_f32_e32 v249, v185\n"
    "v_mfma_f32_16x16x32_bf16 v[4:7], v[144:147], v[204:207], v[4:7]\n"
    "v_exp_f32_e32 v250, v186\n"
    "v_cvt_pk_bf16_f32 v208, v248, v249\n"
    "v_mfma_f32_16x16x32_bf16 v[16:19], v[148:151], v[204:207], v[16:19]\n"
    "v_exp_f32_e32 v251, v187\n"
    "v_mfma_f32_16x16x32_bf16 v[28:31], v[152:155], v[204:207], v[28:31]\n"
    "v_exp_f32_e32 v248, v196\n"
    "v_cvt_pk_bf16_f32 v209, v250, v251\n"
    "v_mfma_f32_16x16x32_bf16 v[40:43], v[156:159], v[204:207], v[40:43]\n"
    "v_exp_f32_e32 v249, v197\n"
    "v_exp_f32_e32 v250, v198\n"
    "v_mfma_f32_16x16x32_bf16 v[216:219], v[236:239], v[204:207], v[216:219]\n"
    "v_cvt_pk_bf16_f32 v210, v248, v249\n"
    "s_waitcnt lgkmcnt(0)\n"
    "v_mfma_f32_16x16x32_bf16 v[48:51], v[160:163], v[200:203], v[48:51]\n"
    "v_exp_f32_e32 v251, v199\n"
    "v_or3_b32 v255, v208, v209, v210\n"
    "v_mfma_f32_16x16x32_bf16 v[60:63], v[164:167], v[200:203], v[60:63]\n"
    "v_cvt_pk_bf16_f32 v211, v250, v251\n"
    "v_bitop3_b32 v255, v255, s63, v211 bitop3:0xc8\n"
    "v_mfma_f32_16x16x32_bf16 v[72:75], v[168:171], v[200:203], v[72:75]\n"
    "v_cmp_ne_u32_e32 vcc, 0, v255\n"
    "v_mfma_f32_16x16x32_bf16 v[84:87], v[172:175], v[200:203], v[84:87]\n"
    "s_cbranch_vccnz .Lst6_2_%=\n"
    ".Lbk6_2_%=:\n"
    "s_waitcnt vmcnt(11)\n"
    "s_barrier\n"
    "v_mfma_f32_16x16x32_bf16 v[8:11], v[144:147], v[208:211], v[8:11]\n"
    "ds_read_b128 v[144:147], v242 offset:0\n"
    "s_add_u32 m0, s56, 131072\n"
    "s_add_u32 s61, s65, 81920\n"
    "s_and_b32 s61, s61, 0x1ffff\n"
    "buffer_load_dwordx4 v247, s[48:51], s61 offen offset:2048 nt lds\n"
    "v_mfma_f32_16x16x32_bf16 v[20:23], v[148:151], v[208:211], v[20:23]\n"
    "ds_read_b128 v[148:151], v243 offset:0\n"
    "v_add_u32_e32 v244, 32768, v244\n"
    "v_mfma_f32_16x16x32_bf16 v[32:35], v[152:155], v[208:211], v[32:35]\n"
    "ds_read_b128 v[152:155], v242 offset:4096\n"
    "v_xor_b32_e32 v245, 32, v244\n"
    "v_mfma_f32_16x16x32_bf16 v[44:47], v[156:159], v[208:211], v[44:47]\n"
    "ds_read_b128 v[156:159], v243 offset:4096\n"
    "v_mfma_f32_16x16x32_bf16 v[52:55], v[160:163], v[204:207], v[52:55]\n"
    "v_mfma_f32_16x16x32_bf16 v[64:67], v[164:167], v[204:207], v[64:67]\n"
    "v_mfma_f32_16x16x32_bf16 v[76:79], v[168:171], v[204:207], v[76:79]\n"
    "v_mfma_f32_16x16x32_bf16 v[88:91], v[172:175], v[204:207], v[88:91]\n"
    "v_mfma_f32_16x16x32_bf16 v[56:59], v[160:163], v[208:211], v[56:59]\n"
    "ds_read_b128 v[160:163], v242 offset:16384\n"
    "v_mfma_f32_16x16x32_bf16 v[68:71], v[164:167], v[208:211], v[68:71]\n"
    "ds_read_b128 v[164:167], v243 offset:16384\n"
    "v_mfma_f32_16x16x32_bf16 v[80:83], v[168:171], v[208:211], v[80:83]\n"
    "ds_read_b128 v[168:171], v242 offset:20480\n"
    "v_mfma_f32_16x16x32_bf16 v[92:95], v[172:175], v[208:211], v[92:95]\n"
    "ds_read_b128 v[172:175], v243 offset:20480\n"
    "v_mfma_f32_16x16x32_bf16 v[220:223], v[236:239], v[208:211], v[220:223]\n"
    "s_waitcnt lgkmcnt(4)\n"
    "v_mfma_f32_16x16x32_bf16 v[176:179], v[144:147], v[96:99], v[224:227]\n"
    "v_mfma_f32_16x16x32_bf16 v[180:183], v[144:147], v[100:103], v[228:231]\n"
    "v_mfma_f32_16x16x32_bf16 v[184:187], v[144:147], v[104:107], v[232:235]\n"
    "ds_read_b64_tr_b16 v[144:145], v244 offset:0\n"
    "ds_read_b64_tr_b16 v[146:147], v244 offset:16384\n"
    "v_mfma_f32_16x16x32_bf16 v[176:179], v[148:151], v[108:111], v[176:179]\n"
    "v_mfma_f32_16x16x32_bf16 v[180:183], v[148:151], v[112:115], v[180:183]\n"
    "s_add_u32 m0, s56, 131072\n"
    "s_add_u32 s61, s65, 81920\n"
    "s_and_b32 s61, s61, 0x1ffff\n"
    "s_add_u32 s61, s61, 130048\n"
    "buffer_load_dwordx4 v247, s[48:51], s61 offen offset:3072 nt lds\n"
    "v_mfma_f32_16x16x32_bf16 v[184:187], v[148:151], v[116:119], v[184:187]\n"
    "ds_read_b64_tr_b16 v[148:149], v245 offset:0\n"
    "ds_read_b64_tr_b16 v[150:151], v245 offset:16384\n"
    "v_mfma_f32_16x16x32_bf16 v[176:179], v[152:155], v[120:123], v[176:179]\n"
    "v_mfma_f32_16x16x32_bf16 v[180:183], v[152:155], v[124:127], v[180:183]\n"
    "v_mfma_f32_16x16x32_bf16 v[184:187], v[152:155], v[128:131], v[184:187]\n"
    "ds_read_b64_tr_b16 v[152:153], v244 offset:512\n"
    "ds_read_b64_tr_b16 v[154:155], v244 offset:16896\n"
    "v_mfma_f32_16x16x32_bf16 v[176:179], v[156:159], v[132:135], v[176:179]\n"
    "v_add_u32_e32 v242, 32768, v242\n"
    "v_mfma_f32_16x16x32_bf16 v[180:183], v[156:159], v[136:139], v[180:183]\n"
    "v_xor_b32_e32 v243, 64, v242\n"
    "v_mfma_f32_16x16x32_bf16 v[184:187], v[156:159], v[140:143], v[184:187]\n"
    "ds_read_b64_tr_b16 v[156:157], v245 offset:512\n"
    "ds_read_b64_tr_b16 v[158:159], v245 offset:16896\n"
    "s_waitcnt lgkmcnt(8)\n"
    "v_mfma_f32_16x16x32_bf16 v[188:191], v[160:163], v[96:99], v[224:227]\n"
    "v_mfma_f32_16x16x32_bf16 v[188:191], v[164:167], v[108:111], v[188:191]\n"
    "v_exp_f32_e32 v248, v176\n"
    "v_exp_f32_e32 v249, v177\n"
    "v_exp_f32_e32 v250, v178\n"
    "v_mfma_f32_16x16x32_bf16 v[188:191], v[168:171], v[120:123], v[188:191]\n"
    "v_cvt_pk_bf16_f32 v200, v248, v249\n"
    "v_exp_f32_e32 v251, v179\n"
    "v_mfma_f32_16x16x32_bf16 v[188:191], v[172:175], v[132:135], v[188:191]\n"
    "v_exp_f32_e32 v248, v180\n"
    "v_cvt_pk_bf16_f32 v201, v250, v251\n"
    "v_mfma_f32_16x16x32_bf16 v[192:195], v[160:163], v[100:103], v[228:231]\n"
    "v_exp_f32_e32 v249, v181\n"
    "v_exp_f32_e32 v250, v182\n"
    "v_mfma_f32_16x16x32_bf16 v[192:195], v[164:167], v[112:115], v[192:195]\n"
    "v_cvt_pk_bf16_f32 v204, v248, v249\n"
    "v_exp_f32_e32 v251, v183\n"
    "v_mfma_f32_16x16x32_bf16 v[192:195], v[168:171], v[124:127], v[192:195]\n"
    "v_exp_f32_e32 v248, v188\n"
    "v_cvt_pk_bf16_f32 v205, v250, v251\n"
    "v_mfma_f32_16x16x32_bf16 v[192:195], v[172:175], v[136:139], v[192:195]\n"
    "v_exp_f32_e32 v249, v189\n"
    "v_exp_f32_e32 v250, v190\n"
    "v_mfma_f32_16x16x32_bf16 v[196:199], v[160:163], v[104:107], v[232:235]\n"
    "ds_read_b64_tr_b16 v[160:161], v244 offset:4096\n"
    "ds_read_b64_tr_b16 v[162:163], v244 offset:20480\n"
    "v_cvt_pk_bf16_f32 v202, v248, v249\n"
    "v_exp_f32_e32 v251, v191\n"
    "s_add_u32 m0, s56, 0\n"
    "s_add_u32 s61, s65, 90112\n"
    "s_and_b32 s61, s61, 0x1ffff\n"
    "buffer_load_dwordx4 v246, s[44:47], s61 offen offset:0 nt lds\n"
    "v_mfma_f32_16x16x32_bf16 v[196:199], v[164:167], v[116:119], v[196:199]\n"
    "ds_read_b64_tr_b16 v[164:165], v245 offset:4096\n"
    "ds_read_b64_tr_b16 v[166:167], v245 offset:20480\n"
    "v_or3_b32 v255, v200, v201, v202\n"
    "v_cvt_pk_bf16_f32 v203, v250, v251\n"
    "v_mfma_f32_16x16x32_bf16 v[196:199], v[168:171], v[128:131], v[196:199]\n"
    "ds_read_b64_tr_b16 v[168:169], v244 offset:4608\n"
    "ds_read_b64_tr_b16 v[170:171], v244 offset:20992\n"
    "v_bitop3_b32 v255, v255, s63, v203 bitop3:0xc8\n"
    "v_cmp_ne_u32_e32 vcc, 0, v255\n"
    "v_mfma_f32_16x16x32_bf16 v[196:199], v[172:175], v[140:143], v[196:199]\n"
    "s_cbranch_vccnz .Lst7_0_%=\n"
    ".Lbk7_0_%=:\n"
    "ds_read_b64_tr_b16 v[172:173], v245 offset:4608\n"
    "ds_read_b64_tr_b16 v[174:175], v245 offset:20992\n"
    "v_exp_f32_e32 v248, v192\n"
    "v_exp_f32_e32 v249, v193\n"
    "s_waitcnt lgkmcnt(8)\n"
    "v_mfma_f32_16x16x32_bf16 v[0:3], v[144:147], v[200:203], v[0:3]\n"
    "v_exp_f32_e32 v250, v194\n"
    "v_cvt_pk_bf16_f32 v206, v248, v249\n"
    "v_mfma_f32_16x16x32_bf16 v[12:15], v[148:151], v[200:203], v[12:15]\n"
    "v_exp_f32_e32 v251, v195\n"
    "v_or3_b32 v255, v204, v205, v206\n"
    "v_mfma_f32_16x16x32_bf16 v[24:27], v[152:155], v[200:203], v[24:27]\n"
    "v_cvt_pk_bf16_f32 v207, v250, v251\n"
    "v_bitop3_b32 v255, v255, s63, v207 bitop3:0xc8\n"
    "v_mfma_f32_16x16x32_bf16 v[36:39], v[156:159], v[200:203], v[36:39]\n"
    "v_cmp_ne_u32_e32 vcc, 0, v255\n"
    "v_mfma_f32_16x16x32_bf16 v[212:215], v[236:239], v[200:203], v[212:215]\n"
    "s_cbranch_vccnz .Lst7_1_%=\n"
    ".Lbk7_1_%=:\n"
    "v_exp_f32_e32 v248, v184\n"
    "v_exp_f32_e32 v249, v185\n"
    "v_mfma_f32_16x16x32_bf16 v[4:7], v[144:147], v[204:207], v[4:7]\n"
    "v_exp_f32_e32 v250, v186\n"
    "v_cvt_pk_bf16_f32 v208, v248, v249\n"
    "v_mfma_f32_16x16x32_bf16 v[16:19], v[148:151], v[204:207], v[16:19]\n"
    "v_exp_f32_e32 v251, v187\n"
    "v_mfma_f32_16x16x32_bf16 v[28:31], v[152:155], v[204:207], v[28:31]\n"
    "v_exp_f32_e32 v248, v196\n"
    "v_cvt_pk_bf16_f32 v209, v250, v251\n"
    "v_mfma_f32_16x16x32_bf16 v[40:43], v[156:159], v[204:207], v[40:43]\n"
    "v_exp_f32_e32 v249, v197\n"
    "v_exp_f32_e32 v250, v198\n"
    "v_mfma_f32_16x16x32_bf16 v[216:219], v[236:239], v[204:207], v[216:219]\n"
    "v_cvt_pk_bf16_f32 v210, v248, v249\n"
    "s_waitcnt lgkmcnt(0)\n"
    "v_mfma_f32_16x16x32_bf16 v[48:51], v[160:163], v[200:203], v[48:51]\n"
    "v_exp_f32_e32 v251, v199\n"
    "v_or3_b32 v255, v208, v209, v210\n"
    "v_mfma_f32_16x16x32_bf16 v[60:63], v[164:167], v[200:203], v[60:63]\n"
    "v_cvt_pk_bf16_f32 v211, v250, v251\n"
    "v_bitop3_b32 v255, v255, s63, v211 bitop3:0xc8\n"
    "v_mfma_f32_16x16x32_bf16 v[72:75], v[168:171], v[200:203], v[72:75]\n"
    "v_cmp_ne_u32_e32 vcc, 0, v255\n"
    "s_add_u32 m0, s56, 0\n"
    "s_add_u32 s61, s65, 90112\n"
    "s_and_b32 s61, s61, 0x1ffff\n"
    "s_add_u32 s61, s61, 130048\n"
    "buffer_load_dwordx4 v246, s[44:47], s61 offen offset:1024 nt lds\n"
    "v_mfma_f32_16x16x32_bf16 v[84:87], v[172:175], v[200:203], v[84:87]\n"
    "s_cbranch_vccnz .Lst7_2_%=\n"
    ".Lbk7_2_%=:\n"
    "s_waitcnt vmcnt(10)\n"
    "s_barrier\n"
    "v_mfma_f32_16x16x32_bf16 v[8:11], v[144:147], v[208:211], v[8:11]\n"
    "ds_read_b128 v[144:147], v242 offset:0\n"
    "v_mfma_f32_16x16x32_bf16 v[20:23], v[148:151], v[208:211], v[20:23]\n"
    "ds_read_b128 v[148:151], v243 offset:0\n"
    "v_add_u32_e32 v244, 32768, v244\n"
    "v_mfma_f32_16x16x32_bf16 v[32:35], v[152:155], v[208:211], v[32:35]\n"
    "ds_read_b128 v[152:155], v242 offset:4096\n"
    "v_xor_b32_e32 v245, 32, v244\n"
    "v_mfma_f32_16x16x32_bf16 v[44:47], v[156:159], v[208:211], v[44:47]\n"
    "ds_read_b128 v[156:159], v243 offset:4096\n"
    "v_mfma_f32_16x16x32_bf16 v[52:55], v[160:163], v[204:207], v[52:55]\n"
    "v_mfma_f32_16x16x32_bf16 v[64:67], v[164:167], v[204:207], v[64:67]\n"
    "v_mfma_f32_16x16x32_bf16 v[76:79], v[168:171], v[204:207], v[76:79]\n"
    "v_mfma_f32_16x16x32_bf16 v[88:91], v[172:175], v[204:207], v[88:91]\n"
    "v_mfma_f32_16x16x32_bf16 v[56:59], v[160:163], v[208:211], v[56:59]\n"
    "ds_read_b128 v[160:163], v242 offset:16384\n"
    "v_mfma_f32_16x16x32_bf16 v[68:71], v[164:167], v[208:211], v[68:71]\n"
    "ds_read_b128 v[164:167], v243 offset:16384\n"
    "v_mfma_f32_16x16x32_bf16 v[80:83], v[168:171], v[208:211], v[80:83]\n"
    "ds_read_b128 v[168:171], v242 offset:20480\n"
    "v_mfma_f32_16x16x32_bf16 v[92:95], v[172:175], v[208:211], v[92:95]\n"
    "ds_read_b128 v[172:175], v243 offset:20480\n"
    "v_mfma_f32_16x16x32_bf16 v[220:223], v[236:239], v[208:211], v[220:223]\n"
    "s_waitcnt lgkmcnt(4)\n"
    "v_mfma_f32_16x16x32_bf16 v[176:179], v[144:147], v[96:99], v[224:227]\n"
    "v_mfma_f32_16x16x32_bf16 v[180:183], v[144:147], v[100:103], v[228:231]\n"
    "s_add_u32 m0, s56, 0\n"
    "s_add_u32 s61, s65, 90112\n"
    "s_and_b32 s61, s61, 0x1ffff\n"
    "buffer_load_dwordx4 v247, s[48:51], s61 offen offset:2048 nt lds\n"
    "v_mfma_f32_16x16x32_bf16 v[184:187], v[144:147], v[104:107], v[232:235]\n"
    "ds_read_b64_tr_b16 v[144:145], v244 offset:0\n"
    "ds_read_b64_tr_b16 v[146:147], v244 offset:16384\n"
    "v_mfma_f32_16x16x32_bf16 v[176:179], v[148:151], v[108:111], v[176:179]\n"
    "v_mfma_f32_16x16x32_bf16 v[180:183], v[148:151], v[112:115], v[180:183]\n"
    "v_mfma_f32_16x16x32_bf16 v[184:187], v[148:151], v[116:119], v[184:187]\n"
    "ds_read_b64_tr_b16 v[148:149], v245 offset:0\n"
    "ds_read_b64_tr_b16 v[150:151], v245 offset:16384\n"
    "v_mfma_f32_16x16x32_bf16 v[176:179], v[152:155], v[120:123], v[176:179]\n"
    "v_mfma_f32_16x16x32_bf16 v[180:183], v[152:155], v[124:127], v[180:183]\n"
    "v_mfma_f32_16x16x32_bf16 v[184:187], v[152:155], v[128:131], v[184:187]\n"
    "ds_read_b64_tr_b16 v[152:153], v244 offset:512\n"
    "ds_read_b64_tr_b16 v[154:155], v244 offset:16896\n"
    "v_mfma_f32_16x16x32_bf16 v[176:179], v[156:159], v[132:135], v[176:179]\n"
    "v_add_u32_e32 v242, 32768, v242\n"
    "v_mfma_f32_16x16x32_bf16 v[180:183], v[156:159], v[136:139], v[180:183]\n"
    "v_xor_b32_e32 v243, 64, v242\n"
    "v_mfma_f32_16x16x32_bf16 v[184:187], v[156:159], v[140:143], v[184:187]\n"
    "ds_read_b64_tr_b16 v[156:157], v245 offset:512\n"
    "ds_read_b64_tr_b16 v[158:159], v245 offset:16896\n"
    "s_waitcnt lgkmcnt(8)\n"
    "v_mfma_f32_16x16x32_bf16 v[188:191], v[160:163], v[96:99], v[224:227]\n"
    "v_mfma_f32_16x16x32_bf16 v[188:191], v[164:167], v[108:111], v[188:191]\n"
    "v_exp_f32_e32 v248, v176\n"
    "v_exp_f32_e32 v249, v177\n"
    "v_exp_f32_e32 v250, v178\n"
    "v_mfma_f32_16x16x32_bf16 v[188:191], v[168:171], v[120:123], v[188:191]\n"
    "v_cvt_pk_bf16_f32 v200, v248, v249\n"
    "v_exp_f32_e32 v251, v179\n"
    "v_mfma_f32_16x16x32_bf16 v[188:191], v[172:175], v[132:135], v[188:191]\n"
    "v_exp_f32_e32 v248, v180\n"
    "v_cvt_pk_bf16_f32 v201, v250, v251\n"
    "v_mfma_f32_16x16x32_bf16 v[192:195], v[160:163], v[100:103], v[228:231]\n"
    "v_exp_f32_e32 v249, v181\n"
    "v_exp_f32_e32 v250, v182\n"
    "v_mfma_f32_16x16x32_bf16 v[192:195], v[164:167], v[112:115], v[192:195]\n"
    "v_cvt_pk_bf16_f32 v204, v248, v249\n"
    "v_exp_f32_e32 v251, v183\n"
    "s_add_u32 m0, s56, 0\n"
    "s_add_u32 s61, s65, 90112\n"
    "s_and_b32 s61, s61, 0x1ffff\n"
    "s_add_u32 s61, s61, 130048\n"
    "buffer_load_dwordx4 v247, s[48:51], s61 offen offset:3072 nt lds\n"
    "v_mfma_f32_16x16x32_bf16 v[192:195], v[168:171], v[124:127], v[192:195]\n"
    "v_exp_f32_e32 v248, v188\n"
    "v_cvt_pk_bf16_f32 v205, v250, v251\n"
    "v_mfma_f32_16x16x32_bf16 v[192:195], v[172:175], v[136:139], v[192:195]\n"
    "v_exp_f32_e32 v249, v189\n"
    "v_exp_f32_e32 v250, v190\n"
    "v_mfma_f32_16x16x32_bf16 v[196:199], v[160:163], v[104:107], v[232:235]\n"
    "ds_read_b64_tr_b16 v[160:161], v244 offset:4096\n"
    "ds_read_b64_tr_b16 v[162:163], v244 offset:20480\n"
    "v_cvt_pk_bf16_f32 v202, v248, v249\n"
    "v_exp_f32_e32 v251, v191\n"
    "v_mfma_f32_16x16x32_bf16 v[196:199], v[164:167], v[116:119], v[196:199]\n"
    "ds_read_b64_tr_b16 v[164:165], v245 offset:4096\n"
    "ds_read_b64_tr_b16 v[166:167], v245 offset:20480\n"
    "v_or3_b32 v255, v200, v201, v202\n"
    "v_cvt_pk_bf16_f32 v203, v250, v251\n"
    "v_mfma_f32_16x16x32_bf16 v[196:199], v[168:171], v[128:131], v[196:199]\n"
    "ds_read_b64_tr_b16 v[168:169], v244 offset:4608\n"
    "ds_read_b64_tr_b16 v[170:171], v244 offset:20992\n"
    "v_bitop3_b32 v255, v255, s63, v203 bitop3:0xc8\n"
    "v_cmp_ne_u32_e32 vcc, 0, v255\n"
    "v_mfma_f32_16x16x32_bf16 v[196:199], v[172:175], v[140:143], v[196:199]\n"
    "s_cbranch_vccnz .Lst8_0_%=\n"
    ".Lbk8_0_%=:\n"
    "ds_read_b64_tr_b16 v[172:173], v245 offset:4608\n"
    "ds_read_b64_tr_b16 v[174:175], v245 offset:20992\n"
    "v_exp_f32_e32 v248, v192\n"
    "v_exp_f32_e32 v249, v193\n"
    "s_waitcnt lgkmcnt(8)\n"
    "v_mfma_f32_16x16x32_bf16 v[0:3], v[144:147], v[200:203], v[0:3]\n"
    "v_exp_f32_e32 v250, v194\n"
    "v_cvt_pk_bf16_f32 v206, v248, v249\n"
    "v_mfma_f32_16x16x32_bf16 v[12:15], v[148:151], v[200:203], v[12:15]\n"
    "v_exp_f32_e32 v251, v195\n"
    "v_or3_b32 v255, v204, v205, v206\n"
    "v_mfma_f32_16x16x32_bf16 v[24:27], v[152:155], v[200:203], v[24:27]\n"
    "v_cvt_pk_bf16_f32 v207, v250, v251\n"
    "v_bitop3_b32 v255, v255, s63, v207 bitop3:0xc8\n"
    "v_mfma_f32_16x16x32_bf16 v[36:39], v[156:159], v[200:203], v[36:39]\n"
    "v_cmp_ne_u32_e32 vcc, 0, v255\n"
    "v_mfma_f32_16x16x32_bf16 v[212:215], v[236:239], v[200:203], v[212:215]\n"
    "s_cbranch_vccnz .Lst8_1_%=\n"
    ".Lbk8_1_%=:\n"
    "v_exp_f32_e32 v248, v184\n"
    "v_exp_f32_e32 v249, v185\n"
    "v_mfma_f32_16x16x32_bf16 v[4:7], v[144:147], v[204:207], v[4:7]\n"
    "v_exp_f32_e32 v250, v186\n"
    "v_cvt_pk_bf16_f32 v208, v248, v249\n"
    "v_mfma_f32_16x16x32_bf16 v[16:19], v[148:151], v[204:207], v[16:19]\n"
    "v_exp_f32_e32 v251, v187\n"
    "v_mfma_f32_16x16x32_bf16 v[28:31], v[152:155], v[204:207], v[28:31]\n"
    "v_exp_f32_e32 v248, v196\n"
    "v_cvt_pk_bf16_f32 v209, v250, v251\n"
    "v_mfma_f32_16x16x32_bf16 v[40:43], v[156:159], v[204:207], v[40:43]\n"
    "v_exp_f32_e32 v249, v197\n"
    "v_exp_f32_e32 v250, v198\n"
    "v_mfma_f32_16x16x32_bf16 v[216:219], v[236:239], v[204:207], v[216:219]\n"
    "v_cvt_pk_bf16_f32 v210, v248, v249\n"
    "s_waitcnt lgkmcnt(0)\n"
    "v_mfma_f32_16x16x32_bf16 v[48:51], v[160:163], v[200:203], v[48:51]\n"
    "v_exp_f32_e32 v251, v199\n"
    "v_or3_b32 v255, v208, v209, v210\n"
    "s_add_u32 m0, s56, 32768\n"
    "s_add_u32 s61, s65, 98304\n"
    "s_and_b32 s61, s61, 0x1ffff\n"
    "buffer_load_dwordx4 v246, s[44:47], s61 offen offset:0 nt lds\n"
    "v_mfma_f32_16x16x32_bf16 v[60:63], v[164:167], v[200:203], v[60:63]\n"
    "v_cvt_pk_bf16_f32 v211, v250, v251\n"
    "v_bitop3_b32 v255, v255, s63, v211 bitop3:0xc8\n"
    "v_mfma_f32_16x16x32_bf16 v[72:75], v[168:171], v[200:203], v[72:75]\n"
    "v_cmp_ne_u32_e32 vcc, 0, v255\n"
    "v_mfma_f32_16x16x32_bf16 v[84:87], v[172:175], v[200:203], v[84:87]\n"
    "s_cbranch_vccnz .Lst8_2_%=\n"
    ".Lbk8_2_%=:\n"
    "s_waitcnt vmcnt(9)\n"
    "s_barrier\n"
    "v_mfma_f32_16x16x32_bf16 v[8:11], v[144:147], v[208:211], v[8:11]\n"
    "ds_read_b128 v[144:147], v242 offset:0\n"
    "v_mfma_f32_16x16x32_bf16 v[20:23], v[148:151], v[208:211], v[20:23]\n"
    "ds_read_b128 v[148:151], v243 offset:0\n"
    "v_add_u32_e32 v244, 32768, v244\n"
    "v_mfma_f32_16x16x32_bf16 v[32:35], v[152:155], v[208:211], v[32:35]\n"
    "ds_read_b128 v[152:155], v242 offset:4096\n"
    "v_xor_b32_e32 v245, 32, v244\n"
    "v_mfma_f32_16x16x32_bf16 v[44:47], v[156:159], v[208:211], v[44:47]\n"
    "ds_read_b128 v[156:159], v243 offset:4096\n"
    "v_mfma_f32_16x16x32_bf16 v[52:55], v[160:163], v[204:207], v[52:55]\n"
    "v_mfma_f32_16x16x32_bf16 v[64:67], v[164:167], v[204:207], v[64:67]\n"
    "v_mfma_f32_16x16x32_bf16 v[76:79], v[168:171], v[204:207], v[76:79]\n"
    "v_mfma_f32_16x16x32_bf16 v[88:91], v[172:175], v[204:207], v[88:91]\n"
    "v_mfma_f32_16x16x32_bf16 v[56:59], v[160:163], v[208:211], v[56:59]\n"
    "ds_read_b128 v[160:163], v242 offset:16384\n"
    "v_mfma_f32_16x16x32_bf16 v[68:71], v[164:167], v[208:211], v[68:71]\n"
    "ds_read_b128 v[164:167], v243 offset:16384\n"
    "v_mfma_f32_16x16x32_bf16 v[80:83], v[168:171], v[208:211], v[80:83]\n"
    "ds_read_b128 v[168:171], v242 offset:20480\n"
    "v_mfma_f32_16x16x32_bf16 v[92:95], v[172:175], v[208:211], v[92:95]\n"
    "ds_read_b128 v[172:175], v243 offset:20480\n"
    "v_mfma_f32_16x16x32_bf16 v[220:223], v[236:239], v[208:211], v[220:223]\n"
    "s_add_u32 m0, s56, 32768\n"
    "s_add_u32 s61, s65, 98304\n"
    "s_and_b32 s61, s61, 0x1ffff\n"
    "s_add_u32 s61, s61, 130048\n"
    "buffer_load_dwordx4 v246, s[44:47], s61 offen offset:1024 nt lds\n"
    "s_waitcnt lgkmcnt(4)\n"
    "v_mfma_f32_16x16x32_bf16 v[176:179], v[144:147], v[96:99], v[224:227]\n"
    "v_mfma_f32_16x16x32_bf16 v[180:183], v[144:147], v[100:103], v[228:231]\n"
    "v_mfma_f32_16x16x32_bf16 v[184:187], v[144:147], v[104:107], v[232:235]\n"
    "ds_read_b64_tr_b16 v[144:145], v244 offset:0\n"
    "ds_read_b64_tr_b16 v[146:147], v244 offset:16384\n"
    "v_mfma_f32_16x16x32_bf16 v[176:179], v[148:151], v[108:111], v[176:179]\n"
    "v_mfma_f32_16x16x32_bf16 v[180:183], v[148:151], v[112:115], v[180:183]\n"
    "v_mfma_f32_16x16x32_bf16 v[184:187], v[148:151], v[116:119], v[184:187]\n"
    "ds_read_b64_tr_b16 v[148:149], v245 offset:0\n"
    "ds_read_b64_tr_b16 v[150:151], v245 offset:16384\n"
    "v_mfma_f32_16x16x32_bf16 v[176:179], v[152:155], v[120:123], v[176:179]\n"
    "v_mfma_f32_16x16x32_bf16 v[180:183], v[152:155], v[124:127], v[180:183]\n"
    "v_mfma_f32_16x16x32_bf16 v[184:187], v[152:155], v[128:131], v[184:187]\n"
    "ds_read_b64_tr_b16 v[152:153], v244 offset:512\n"
    "ds_read_b64_tr_b16 v[154:155], v244 offset:16896\n"
    "v_mfma_f32_16x16x32_bf16 v[176:179], v[156:159], v[132:135], v[176:179]\n"
    "v_add_u32_e32 v242, 32768, v242\n"
    "v_mfma_f32_16x16x32_bf16 v[180:183], v[156:159], v[136:139], v[180:183]\n"
    "v_xor_b32_e32 v243, 64, v242\n"
    "v_mfma_f32_16x16x32_bf16 v[184:187], v[156:159], v[140:143], v[184:187]\n"
    "ds_read_b64_tr_b16 v[156:157], v245 offset:512\n"
    "ds_read_b64_tr_b16 v[158:159], v245 offset:16896\n"
    "s_waitcnt lgkmcnt(8)\n"
    "v_mfma_f32_16x16x32_bf16 v[188:191], v[160:163], v[96:99], v[224:227]\n"
    "v_mfma_f32_16x16x32_bf16 v[188:191], v[164:167], v[108:111], v[188:191]\n"
    "v_exp_f32_e32 v248, v176\n"
    "v_exp_f32_e32 v249, v177\n"
    "v_exp_f32_e32 v250, v178\n"
    "v_mfma_f32_16x16x32_bf16 v[188:191], v[168:171], v[120:123], v[188:191]\n"
    "v_cvt_pk_bf16_f32 v200, v248, v249\n"
    "v_exp_f32_e32 v251, v179\n"
    "v_mfma_f32_16x16x32_bf16 v[188:191], v[172:175], v[132:135], v[188:191]\n"
    "v_exp_f32_e32 v248, v180\n"
    "v_cvt_pk_bf16_f32 v201, v250, v251\n"
    "s_add_u32 m0, s56, 32768\n"
    "s_add_u32 s61, s65, 98304\n"
    "s_and_b32 s61, s61, 0x1ffff\n"
    "buffer_load_dwordx4 v247, s[48:51], s61 offen offset:2048 nt lds\n"
    "v_mfma_f32_16x16x32_bf16 v[192:195], v[160:163], v[100:103], v[228:231]\n"
    "v_exp_f32_e32 v249, v181\n"
    "v_exp_f32_e32 v250, v182\n"
    "v_mfma_f32_16x16x32_bf16 v[192:195], v[164:167], v[112:115], v[192:195]\n"
    "v_cvt_pk_bf16_f32 v204, v248, v249\n"
    "v_exp_f32_e32 v251, v183\n"
    "v_mfma_f32_16x16x32_bf16 v[192:195], v[168:171], v[124:127], v[192:195]\n"
    "v_exp_f32_e32 v248, v188\n"
    "v_cvt_pk_bf16_f32 v205, v250, v251\n"
    "v_mfma_f32_16x16x32_bf16 v[192:195], v[172:175], v[136:139], v[192:195]\n"
    "v_exp_f32_e32 v249, v189\n"
    "v_exp_f32_e32 v250, v190\n"
    "v_mfma_f32_16x16x32_bf16 v[196:199], v[160:163], v[104:107], v[232:235]\n"
    "ds_read_b64_tr_b16 v[160:161], v244 offset:4096\n"
    "ds_read_b64_tr_b16 v[162:163], v244 offset:20480\n"
    "v_cvt_pk_bf16_f32 v202, v248, v249\n"
    "v_exp_f32_e32 v251, v191\n"
    "v_mfma_f32_16x16x32_bf16 v[196:199], v[164:167], v[116:119], v[196:199]\n"
    "ds_read_b64_tr_b16 v[164:165], v245 offset:4096\n"
    "ds_read_b64_tr_b16 v[166:167], v245 offset:20480\n"
    "v_or3_b32 v255, v200, v201, v202\n"
    "v_cvt_pk_bf16_f32 v203, v250, v251\n"
    "v_mfma_f32_16x16x32_bf16 v[196:199], v[168:171], v[128:131], v[196:199]\n"
    "ds_read_b64_tr_b16 v[168:169], v244 offset:4608\n"
    "ds_read_b64_tr_b16 v[170:171], v244 offset:20992\n"
    "v_bitop3_b32 v255, v255, s63, v203 bitop3:0xc8\n"
    "v_cmp_ne_u32_e32 vcc, 0, v255\n"
    "v_mfma_f32_16x16x32_bf16 v[196:199], v[172:175], v[140:143], v[196:199]\n"
    "s_cbranch_vccnz .Lst9_0_%=\n"
    ".Lbk9_0_%=:\n"
    "ds_read_b64_tr_b16 v[172:173], v245 offset:4608\n"
    "ds_read_b64_tr_b16 v[174:175], v245 offset:20992\n"
    "v_exp_f32_e32 v248, v192\n"
    "v_exp_f32_e32 v249, v193\n"
    "s_waitcnt lgkmcnt(8)\n"
    "v_mfma_f32_16x16x32_bf16 v[0:3], v[144:147], v[200:203], v[0:3]\n"
    "v_exp_f32_e32 v250, v194\n"
    "v_cvt_pk_bf16_f32 v206, v248, v249\n"
    "v_mfma_f32_16x16x32_bf16 v[12:15], v[148:151], v[200:203], v[12:15]\n"
    "v_exp_f32_e32 v251, v195\n"
    "v_or3_b32 v255, v204, v205, v206\n"
    "v_mfma_f32_16x16x32_bf16 v[24:27], v[152:155], v[200:203], v[24:27]\n"
    "v_cvt_pk_bf16_f32 v207, v250, v251\n"
    "v_bitop3_b32 v255, v255, s63, v207 bitop3:0xc8\n"
    "v_mfma_f32_16x16x32_bf16 v[36:39], v[156:159], v[200:203], v[36:39]\n"
    "v_cmp_ne_u32_e32 vcc, 0, v255\n"
    "v_mfma_f32_16x16x32_bf16 v[212:215], v[236:239], v[200:203], v[212:215]\n"
    "s_cbranch_vccnz .Lst9_1_%=\n"
    ".Lbk9_1_%=:\n"
    "v_exp_f32_e32 v248, v184\n"
    "v_exp_f32_e32 v249, v185\n"
    "v_mfma_f32_16x16x32_bf16 v[4:7], v[144:147], v[204:207], v[4:7]\n"
    "v_exp_f32_e32 v250, v186\n"
    "v_cvt_pk_bf16_f32 v208, v248, v249\n"
    "v_mfma_f32_16x16x32_bf16 v[16:19], v[148:151], v[204:207], v[16:19]\n"
    "v_exp_f32_e32 v251, v187\n"
    "v_mfma_f32_16x16x32_bf16 v[28:31], v[152:155], v[204:207], v[28:31]\n"
    "v_exp_f32_e32 v248, v196\n"
    "v_cvt_pk_bf16_f32 v209, v250, v251\n"
    "s_add_u32 m0, s56, 32768\n"
    "s_add_u32 s61, s65, 98304\n"
    "s_and_b32 s61, s61, 0x1ffff\n"
    "s_add_u32 s61, s61, 130048\n"
    "buffer_load_dwordx4 v247, s[48:51], s61 offen offset:3072 nt lds\n"
    "v_mfma_f32_16x16x32_bf16 v[40:43], v[156:159], v[204:207], v[40:43]\n"
    "v_exp_f32_e32 v249, v197\n"
    "v_exp_f32_e32 v250, v198\n"
    "v_mfma_f32_16x16x32_bf16 v[216:219], v[236:239], v[204:207], v[216:219]\n"
    "v_cvt_pk_bf16_f32 v210, v248, v249\n"
    "s_waitcnt lgkmcnt(0)\n"
    "v_mfma_f32_16x16x32_bf16 v[48:51], v[160:163], v[200:203], v[48:51]\n"
    "v_exp_f32_e32 v251, v199\n"
    "v_or3_b32 v255, v208, v209, v210\n"
    "v_mfma_f32_16x16x32_bf16 v[60:63], v[164:167], v[200:203], v[60:63]\n"
    "v_cvt_pk_bf16_f32 v211, v250, v251\n"
    "v_bitop3_b32 v255, v255, s63, v211 bitop3:0xc8\n"
    "v_mfma_f32_16x16x32_bf16 v[72:75], v[168:171], v[200:203], v[72:75]\n"
    "v_cmp_ne_u32_e32 vcc, 0, v255\n"
    "v_mfma_f32_16x16x32_bf16 v[84:87], v[172:175], v[200:203], v[84:87]\n"
    "s_cbranch_vccnz .Lst9_2_%=\n"
    ".Lbk9_2_%=:\n"
    "s_waitcnt vmcnt(8)\n"
    "s_barrier\n"
    "v_mfma_f32_16x16x32_bf16 v[8:11], v[144:147], v[208:211], v[8:11]\n"
    "ds_read_b128 v[144:147], v242 offset:0\n"
    "v_mfma_f32_16x16x32_bf16 v[20:23], v[148:151], v[208:211], v[20:23]\n"
    "ds_read_b128 v[148:151], v243 offset:0\n"
    "v_add_u32_e32 v244, 32768, v244\n"
    "v_mfma_f32_16x16x32_bf16 v[32:35], v[152:155], v[208:211], v[32:35]\n"
    "ds_read_b128 v[152:155], v242 offset:4096\n"
    "v_xor_b32_e32 v245, 32, v244\n"
    "v_mfma_f32_16x16x32_bf16 v[44:47], v[156:159], v[208:211], v[44:47]\n"
    "ds_read_b128 v[156:159], v243 offset:4096\n"
    "v_mfma_f32_16x16x32_bf16 v[52:55], v[160:163], v[204:207], v[52:55]\n"
    "v_mfma_f32_16x16x32_bf16 v[64:67], v[164:167], v[204:207], v[64:67]\n"
    "v_mfma_f32_16x16x32_bf16 v[76:79], v[168:171], v[204:207], v[76:79]\n"
    "v_mfma_f32_16x16x32_bf16 v[88:91], v[172:175], v[204:207], v[88:91]\n"
    "v_mfma_f32_16x16x32_bf16 v[56:59], v[160:163], v[208:211], v[56:59]\n"
    "ds_read_b128 v[160:163], v242 offset:16384\n"
    "v_mfma_f32_16x16x32_bf16 v[68:71], v[164:167], v[208:211], v[68:71]\n"
    "ds_read_b128 v[164:167], v243 offset:16384\n"
    "s_add_u32 m0, s56, 65536\n"
    "s_add_u32 s61, s65, 106496\n"
    "s_and_b32 s61, s61, 0x1ffff\n"
    "buffer_load_dwordx4 v246, s[44:47], s61 offen offset:0 nt lds\n"
    "v_mfma_f32_16x16x32_bf16 v[80:83], v[168:171], v[208:211], v[80:83]\n"
    "ds_read_b128 v[168:171], v242 offset:20480\n"
    "v_mfma_f32_16x16x32_bf16 v[92:95], v[172:175], v[208:211], v[92:95]\n"
    "ds_read_b128 v[172:175], v243 offset:20480\n"
    "v_mfma_f32_16x16x32_bf16 v[220:223], v[236:239], v[208:211], v[220:223]\n"
    "s_waitcnt lgkmcnt(4)\n"
    "v_mfma_f32_16x16x32_bf16 v[176:179], v[144:147], v[96:99], v[224:227]\n"
    "v_mfma_f32_16x16x32_bf16 v[180:183], v[144:147], v[100:103], v[228:231]\n"
    "v_mfma_f32_16x16x32_bf16 v[184:187], v[144:147], v[104:107], v[232:235]\n"
    "ds_read_b64_tr_b16 v[144:145], v244 offset:0\n"
    "ds_read_b64_tr_b16 v[146:147], v244 offset:16384\n"
    "v_mfma_f32_16x16x32_bf16 v[176:179], v[148:151], v[108:111], v[176:179]\n"
    "v_mfma_f32_16x16x32_bf16 v[180:183], v[148:151], v[112:115], v[180:183]\n"
    "v_mfma_f32_16x16x32_bf16 v[184:187], v[148:151], v[116:119], v[184:187]\n"
    "ds_read_b64_tr_b16 v[148:149], v245 offset:0\n"
    "ds_read_b64_tr_b16 v[150:151], v245 offset:16384\n"
    "v_mfma_f32_16x16x32_bf16 v[176:179], v[152:155], v[120:123], v[176:179]\n"
    "v_mfma_f32_16x16x32_bf16 v[180:183], v[152:155], v[124:127], v[180:183]\n"
    "v_mfma_f32_16x16x32_bf16 v[184:187], v[152:155], v[128:131], v[184:187]\n"
    "ds_read_b64_tr_b16 v[152:153], v244 offset:512\n"
    "ds_read_b64_tr_b16 v[154:155], v244 offset:16896\n"
    "v_mfma_f32_16x16x32_bf16 v[176:179], v[156:159], v[132:135], v[176:179]\n"
    "v_add_u32_e32 v242, 4294836224, v242\n"
    "v_mfma_f32_16x16x32_bf16 v[180:183], v[156:159], v[136:139], v[180:183]\n"
    "v_xor_b32_e32 v243, 64, v242\n"
    "v_mfma_f32_16x16x32_bf16 v[184:187], v[156:159], v[140:143], v[184:187]\n"
    "ds_read_b64_tr_b16 v[156:157], v245 offset:512\n"
    "ds_read_b64_tr_b16 v[158:159], v245 offset:16896\n"
    "s_waitcnt lgkmcnt(8)\n"
    "v_mfma_f32_16x16x32_bf16 v[188:191], v[160:163], v[96:99], v[224:227]\n"
    "s_add_u32 m0, s56, 65536\n"
    "s_add_u32 s61, s65, 106496\n"
    "s_and_b32 s61, s61, 0x1ffff\n"
    "s_add_u32 s61, s61, 130048\n"
    "buffer_load_dwordx4 v246, s[44:47], s61 offen offset:1024 nt lds\n"
    "v_mfma_f32_16x16x32_bf16 v[188:191], v[164:167], v[108:111], v[188:191]\n"
    "v_exp_f32_e32 v248, v176\n"
    "v_exp_f32_e32 v249, v177\n"
    "v_exp_f32_e32 v250, v178\n"
    "v_mfma_f32_16x16x32_bf16 v[188:191], v[168:171], v[120:123], v[188:191]\n"
    "v_cvt_pk_bf16_f32 v200, v248, v249\n"
    "v_exp_f32_e32 v251, v179\n"
    "v_mfma_f32_16x16x32_bf16 v[188:191], v[172:175], v[132:135], v[188:191]\n"
    "v_exp_f32_e32 v248, v180\n"
    "v_cvt_pk_bf16_f32 v201, v250, v251\n"
    "v_mfma_f32_16x16x32_bf16 v[192:195], v[160:163], v[100:103], v[228:231]\n"
    "v_exp_f32_e32 v249, v181\n"
    "v_exp_f32_e32 v250, v182\n"
    "v_mfma_f32_16x16x32_bf16 v[192:195], v[164:167], v[112:115], v[192:195]\n"
    "v_cvt_pk_bf16_f32 v204, v248, v249\n"
    "v_exp_f32_e32 v251, v183\n"
    "v_mfma_f32_16x16x32_bf16 v[192:195], v[168:171], v[124:127], v[192:195]\n"
    "v_exp_f32_e32 v248, v188\n"
    "v_cvt_pk_bf16_f32 v205, v250, v251\n"
    "v_mfma_f32_16x16x32_bf16 v[192:195], v[172:175], v[136:139], v[192:195]\n"
    "v_exp_f32_e32 v249, v189\n"
    "v_exp_f32_e32 v250, v190\n"
    "v_mfma_f32_16x16x32_bf16 v[196:199], v[160:163], v[104:107], v[232:235]\n"
    "ds_read_b64_tr_b16 v[160:161], v244 offset:4096\n"
    "ds_read_b64_tr_b16 v[162:163], v244 offset:20480\n"
    "v_cvt_pk_bf16_f32 v202, v248, v249\n"
    "v_exp_f32_e32 v251, v191\n"
    "v_mfma_f32_16x16x32_bf16 v[196:199], v[164:167], v[116:119], v[196:199]\n"
    "ds_read_b64_tr_b16 v[164:165], v245 offset:4096\n"
    "ds_read_b64_tr_b16 v[166:167], v245 offset:20480\n"
    "v_or3_b32 v255, v200, v201, v202\n"
    "v_cvt_pk_bf16_f32 v203, v250, v251\n"
    "v_mfma_f32_16x16x32_bf16 v[196:199], v[168:171], v[128:131], v[196:199]\n"
    "ds_read_b64_tr_b16 v[168:169], v244 offset:4608\n"
    "ds_read_b64_tr_b16 v[170:171], v244 offset:20992\n"
    "v_bitop3_b32 v255, v255, s63, v203 bitop3:0xc8\n"
    "v_cmp_ne_u32_e32 vcc, 0, v255\n"
    "v_mfma_f32_16x16x32_bf16 v[196:199], v[172:175], v[140:143], v[196:199]\n"
    "s_cbranch_vccnz .Lst10_0_%=\n"
    ".Lbk10_0_%=:\n"
    "ds_read_b64_tr_b16 v[172:173], v245 offset:4608\n"
    "ds_read_b64_tr_b16 v[174:175], v245 offset:20992\n"
    "v_exp_f32_e32 v248, v192\n"
    "v_exp_f32_e32 v249, v193\n"
    "s_waitcnt lgkmcnt(8)\n"
    "v_mfma_f32_16x16x32_bf16 v[0:3], v[144:147], v[200:203], v[0:3]\n"
    "v_exp_f32_e32 v250, v194\n"
    "v_cvt_pk_bf16_f32 v206, v248, v249\n"
    "v_mfma_f32_16x16x32_bf16 v[12:15], v[148:151], v[200:203], v[12:15]\n"
    "v_exp_f32_e32 v251, v195\n"
    "v_or3_b32 v255, v204, v205, v206\n"
    "v_mfma_f32_16x16x32_bf16 v[24:27], v[152:155], v[200:203], v[24:27]\n"
    "v_cvt_pk_bf16_f32 v207, v250, v251\n"
    "v_bitop3_b32 v255, v255, s63, v207 bitop3:0xc8\n"
    "v_mfma_f32_16x16x32_bf16 v[36:39], v[156:159], v[200:203], v[36:39]\n"
    "v_cmp_ne_u32_e32 vcc, 0, v255\n"
    "v_mfma_f32_16x16x32_bf16 v[212:215], v[236:239], v[200:203], v[212:215]\n"
    "s_cbranch_vccnz .Lst10_1_%=\n"
    ".Lbk10_1_%=:\n"
    "v_exp_f32_e32 v248, v184\n"
    "v_exp_f32_e32 v249, v185\n"
    "v_mfma_f32_16x16x32_bf16 v[4:7], v[144:147], v[204:207], v[4:7]\n"
    "v_exp_f32_e32 v250, v186\n"
    "v_cvt_pk_bf16_f32 v208, v248, v249\n"
    "s_add_u32 m0, s56, 65536\n"
    "s_add_u32 s61, s65, 106496\n"
    "s_and_b32 s61, s61, 0x1ffff\n"
    "buffer_load_dwordx4 v247, s[48:51], s61 offen offset:2048 nt lds\n"
    "v_mfma_f32_16x16x32_bf16 v[16:19], v[148:151], v[204:207], v[16:19]\n"
    "v_exp_f32_e32 v251, v187\n"
    "v_mfma_f32_16x16x32_bf16 v[28:31], v[152:155], v[204:207], v[28:31]\n"
    "v_exp_f32_e32 v248, v196\n"
    "v_cvt_pk_bf16_f32 v209, v250, v251\n"
    "v_mfma_f32_16x16x32_bf16 v[40:43], v[156:159], v[204:207], v[40:43]\n"
    "v_exp_f32_e32 v249, v197\n"
    "v_exp_f32_e32 v250, v198\n"
    "v_mfma_f32_16x16x32_bf16 v[216:219], v[236:239], v[204:207], v[216:219]\n"
    "v_cvt_pk_bf16_f32 v210, v248, v249\n"
    "s_waitcnt lgkmcnt(0)\n"
    "v_mfma_f32_16x16x32_bf16 v[48:51], v[160:163], v[200:203], v[48:51]\n"
    "v_exp_f32_e32 v251, v199\n"
    "v_or3_b32 v255, v208, v209, v210\n"
    "v_mfma_f32_16x16x32_bf16 v[60:63], v[164:167], v[200:203], v[60:63]\n"
    "v_cvt_pk_bf16_f32 v211, v250, v251\n"
    "v_bitop3_b32 v255, v255, s63, v211 bitop3:0xc8\n"
    "v_mfma_f32_16x16x32_bf16 v[72:75], v[168:171], v[200:203], v[72:75]\n"
    "v_cmp_ne_u32_e32 vcc, 0, v255\n"
    "v_mfma_f32_16x16x32_bf16 v[84:87], v[172:175], v[200:203], v[84:87]\n"
    "s_cbranch_vccnz .Lst10_2_%=\n"
    ".Lbk10_2_%=:\n"
    "s_waitcnt vmcnt(7)\n"
    "s_barrier\n"
    "v_mfma_f32_16x16x32_bf16 v[8:11], v[144:147], v[208:211], v[8:11]\n"
    "ds_read_b128 v[144:147], v242 offset:0\n"
    "v_mfma_f32_16x16x32_bf16 v[20:23], v[148:151], v[208:211], v[20:23]\n"
    "ds_read_b128 v[148:151], v243 offset:0\n"
    "v_add_u32_e32 v244, 4294836224, v244\n"
    "v_mfma_f32_16x16x32_bf16 v[32:35], v[152:155], v[208:211], v[32:35]\n"
    "ds_read_b128 v[152:155], v242 offset:4096\n"
    "v_xor_b32_e32 v245, 32, v244\n"
    "v_mfma_f32_16x16x32_bf16 v[44:47], v[156:159], v[208:211], v[44:47]\n"
    "ds_read_b128 v[156:159], v243 offset:4096\n"
    "v_mfma_f32_16x16x32_bf16 v[52:55], v[160:163], v[204:207], v[52:55]\n"
    "v_mfma_f32_16x16x32_bf16 v[64:67], v[164:167], v[204:207], v[64:67]\n"
    "v_mfma_f32_16x16x32_bf16 v[76:79], v[168:171], v[204:207], v[76:79]\n"
    "v_mfma_f32_16x16x32_bf16 v[88:91], v[172:175], v[204:207], v[88:91]\n"
    "s_add_u32 m0, s56, 65536\n"
    "s_add_u32 s61, s65, 106496\n"
    "s_and_b32 s61, s61, 0x1ffff\n"
    "s_add_u32 s61, s61, 130048\n"
    "buffer_load_dwordx4 v247, s[48:51], s61 offen offset:3072 nt lds\n"
    "v_mfma_f32_16x16x32_bf16 v[56:59], v[160:163], v[208:211], v[56:59]\n"
    "ds_read_b128 v[160:163], v242 offset:16384\n"
    "v_mfma_f32_16x16x32_bf16 v[68:71], v[164:167], v[208:211], v[68:71]\n"
    "ds_read_b128 v[164:167], v243 offset:16384\n"
    "v_mfma_f32_16x16x32_bf16 v[80:83], v[168:171], v[208:211], v[80:83]\n"
    "ds_read_b128 v[168:171], v242 offset:20480\n"
    "v_mfma_f32_16x16x32_bf16 v[92:95], v[172:175], v[208:211], v[92:95]\n"
    "ds_read_b128 v[172:175], v243 offset:20480\n"
    "v_mfma_f32_16x16x32_bf16 v[220:223], v[236:239], v[208:211], v[220:223]\n"
    "s_waitcnt lgkmcnt(4)\n"
    "v_mfma_f32_16x16x32_bf16 v[176:179], v[144:147], v[96:99], v[224:227]\n"
    "v_mfma_f32_16x16x32_bf16 v[180:183], v[144:147], v[100:103], v[228:231]\n"
    "v_mfma_f32_16x16x32_bf16 v[184:187], v[144:147], v[104:107], v[232:235]\n"
    "ds_read_b64_tr_b16 v[144:145], v244 offset:0\n"
    "ds_read_b64_tr_b16 v[146:147], v244 offset:16384\n"
    "v_mfma_f32_16x16x32_bf16 v[176:179], v[148:151], v[108:111], v[176:179]\n"
    "v_mfma_f32_16x16x32_bf16 v[180:183], v[148:151], v[112:115], v[180:183]\n"
    "v_mfma_f32_16x16x32_bf16 v[184:187], v[148:151], v[116:119], v[184:187]\n"
    "ds_read_b64_tr_b16 v[148:149], v245 offset:0\n"
    "ds_read_b64_tr_b16 v[150:151], v245 offset:16384\n"
    "v_mfma_f32_16x16x32_bf16 v[176:179], v[152:155], v[120:123], v[176:179]\n"
    "v_mfma_f32_16x16x32_bf16 v[180:183], v[152:155], v[124:127], v[180:183]\n"
    "v_mfma_f32_16x16x32_bf16 v[184:187], v[152:155], v[128:131], v[184:187]\n"
    "ds_read_b64_tr_b16 v[152:153], v244 offset:512\n"
    "ds_read_b64_tr_b16 v[154:155], v244 offset:16896\n"
    "v_mfma_f32_16x16x32_bf16 v[176:179], v[156:159], v[132:135], v[176:179]\n"
    "v_add_u32_e32 v242, 32768, v242\n"
    "v_mfma_f32_16x16x32_bf16 v[180:183], v[156:159], v[136:139], v[180:183]\n"
    "v_xor_b32_e32 v243, 64, v242\n"
    "s_add_u32 m0, s56, 98304\n"
    "s_add_u32 s61, s65, 114688\n"
    "s_and_b32 s61, s61, 0x1ffff\n"
    "buffer_load_dwordx4 v246, s[44:47], s61 offen offset:0 nt lds\n"
    "v_mfma_f32_16x16x32_bf16 v[184:187], v[156:159], v[140:143], v[184:187]\n"
    "ds_read_b64_tr_b16 v[156:157], v245 offset:512\n"
    "ds_read_b64_tr_b16 v[158:159], v245 offset:16896\n"
    "s_waitcnt lgkmcnt(8)\n"
    "v_mfma_f32_16x16x32_bf16 v[188:191], v[160:163], v[96:99], v[224:227]\n"
    "v_mfma_f32_16x16x32_bf16 v[188:191], v[164:167], v[108:111], v[188:191]\n"
    "v_exp_f32_e32 v248, v176\n"
    "v_exp_f32_e32 v249, v177\n"
    "v_exp_f32_e32 v250, v178\n"
    "v_mfma_f32_16x16x32_bf16 v[188:191], v[168:171], v[120:123], v[188:191]\n"
    "v_cvt_pk_bf16_f32 v200, v248, v249\n"
    "v_exp_f32_e32 v251, v179\n"
    "v_mfma_f32_16x16x32_bf16 v[188:191], v[172:175], v[132:135], v[188:191]\n"
    "v_exp_f32_e32 v248, v180\n"
    "v_cvt_pk_bf16_f32 v201, v250, v251\n"
    "v_mfma_f32_16x16x32_bf16 v[192:195], v[160:163], v[100:103], v[228:231]\n"
    "v_exp_f32_e32 v249, v181\n"
    "v_exp_f32_e32 v250, v182\n"
    "v_mfma_f32_16x16x32_bf16 v[192:195], v[164:167], v[112:115], v[192:195]\n"
    "v_cvt_pk_bf16_f32 v204, v248, v249\n"
    "v_exp_f32_e32 v251, v183\n"
    "v_mfma_f32_16x16x32_bf16 v[192:195], v[168:171], v[124:127], v[192:195]\n"
    "v_exp_f32_e32 v248, v188\n"
    "v_cvt_pk_bf16_f32 v205, v250, v251\n"
    "v_mfma_f32_16x16x32_bf16 v[192:195], v[172:175], v[136:139], v[192:195]\n"
    "v_exp_f32_e32 v249, v189\n"
    "v_exp_f32_e32 v250, v190\n"
    "v_mfma_f32_16x16x32_bf16 v[196:199], v[160:163], v[104:107], v[232:235]\n"
    "ds_read_b64_tr_b16 v[160:161], v244 offset:4096\n"
    "ds_read_b64_tr_b16 v[162:163], v244 offset:20480\n"
    "v_cvt_pk_bf16_f32 v202, v248, v249\n"
    "v_exp_f32_e32 v251, v191\n"
    "v_mfma_f32_16x16x32_bf16 v[196:199], v[164:167], v[116:119], v[196:199]\n"
    "ds_read_b64_tr_b16 v[164:165], v245 offset:4096\n"
    "ds_read_b64_tr_b16 v[166:167], v245 offset:20480\n"
    "v_or3_b32 v255, v200, v201, v202\n"
    "v_cvt_pk_bf16_f32 v203, v250, v251\n"
    "v_mfma_f32_16x16x32_bf16 v[196:199], v[168:171], v[128:131], v[196:199]\n"
    "ds_read_b64_tr_b16 v[168:169], v244 offset:4608\n"
    "ds_read_b64_tr_b16 v[170:171], v244 offset:20992\n"
    "v_bitop3_b32 v255, v255, s63, v203 bitop3:0xc8\n"
    "v_cmp_ne_u32_e32 vcc, 0, v255\n"
    "v_mfma_f32_16x16x32_bf16 v[196:199], v[172:175], v[140:143], v[196:199]\n"
    "s_cbranch_vccnz .Lst11_0_%=\n"
    ".Lbk11_0_%=:\n"
    "ds_read_b64_tr_b16 v[172:173], v245 offset:4608\n"
    "ds_read_b64_tr_b16 v[174:175], v245 offset:20992\n"
    "v_exp_f32_e32 v248, v192\n"
    "v_exp_f32_e32 v249, v193\n"
    "s_waitcnt lgkmcnt(8)\n"
    "v_mfma_f32_16x16x32_bf16 v[0:3], v[144:147], v[200:203], v[0:3]\n"
    "v_exp_f32_e32 v250, v194\n"
    "v_cvt_pk_bf16_f32 v206, v248, v249\n"
    "v_mfma_f32_16x16x32_bf16 v[12:15], v[148:151], v[200:203], v[12:15]\n"
    "v_exp_f32_e32 v251, v195\n"
    "v_or3_b32 v255, v204, v205, v206\n"
    "v_mfma_f32_16x16x32_bf16 v[24:27], v[152:155], v[200:203], v[24:27]\n"
    "v_cvt_pk_bf16_f32 v207, v250, v251\n"
    "v_bitop3_b32 v255, v255, s63, v207 bitop3:0xc8\n"
    "s_add_u32 m0, s56, 98304\n"
    "s_add_u32 s61, s65, 114688\n"
    "s_and_b32 s61, s61, 0x1ffff\n"
    "s_add_u32 s61, s61, 130048\n"
    "buffer_load_dwordx4 v246, s[44:47], s61 offen offset:1024 nt lds\n"
    "v_mfma_f32_16x16x32_bf16 v[36:39], v[156:159], v[200:203], v[36:39]\n"
    "v_cmp_ne_u32_e32 vcc, 0, v255\n"
    "v_mfma_f32_16x16x32_bf16 v[212:215], v[236:239], v[200:203], v[212:215]\n"
    "s_cbranch_vccnz .Lst11_1_%=\n"
    ".Lbk11_1_%=:\n"
    "v_exp_f32_e32 v248, v184\n"
    "v_exp_f32_e32 v249, v185\n"
    "v_mfma_f32_16x16x32_bf16 v[4:7], v[144:147], v[204:207], v[4:7]\n"
    "v_exp_f32_e32 v250, v186\n"
    "v_cvt_pk_bf16_f32 v208, v248, v249\n"
    "v_mfma_f32_16x16x32_bf16 v[16:19], v[148:151], v[204:207], v[16:19]\n"
    "v_exp_f32_e32 v251, v187\n"
    "v_mfma_f32_16x16x32_bf16 v[28:31], v[152:155], v[204:207], v[28:31]\n"
    "v_exp_f32_e32 v248, v196\n"
    "v_cvt_pk_bf16_f32 v209, v250, v251\n"
    "v_mfma_f32_16x16x32_bf16 v[40:43], v[156:159], v[204:207], v[40:43]\n"
    "v_exp_f32_e32 v249, v197\n"
    "v_exp_f32_e32 v250, v198\n"
    "v_mfma_f32_16x16x32_bf16 v[216:219], v[236:239], v[204:207], v[216:219]\n"
    "v_cvt_pk_bf16_f32 v210, v248, v249\n"
    "s_waitcnt lgkmcnt(0)\n"
    "v_mfma_f32_16x16x32_bf16 v[48:51], v[160:163], v[200:203], v[48:51]\n"
    "v_exp_f32_e32 v251, v199\n"
    "v_or3_b32 v255, v208, v209, v210\n"
    "v_mfma_f32_16x16x32_bf16 v[60:63], v[164:167], v[200:203], v[60:63]\n"
    "v_cvt_pk_bf16_f32 v211, v250, v251\n"
    "v_bitop3_b32 v255, v255, s63, v211 bitop3:0xc8\n"
    "v_mfma_f32_16x16x32_bf16 v[72:75], v[168:171], v[200:203], v[72:75]\n"
    "v_cmp_ne_u32_e32 vcc, 0, v255\n"
    "v_mfma_f32_16x16x32_bf16 v[84:87], v[172:175], v[200:203], v[84:87]\n"
    "s_cbranch_vccnz .Lst11_2_%=\n"
    ".Lbk11_2_%=:\n"
    "s_waitcnt vmcnt(6)\n"
    "s_barrier\n"
    "v_mfma_f32_16x16x32_bf16 v[8:11], v[144:147], v[208:211], v[8:11]\n"
    "ds_read_b128 v[144:147], v242 offset:0\n"
    "v_mfma_f32_16x16x32_bf16 v[20:23], v[148:151], v[208:211], v[20:23]\n"
    "ds_read_b128 v[148:151], v243 offset:0\n"
    "v_add_u32_e32 v244, 32768, v244\n"
    "v_mfma_f32_16x16x32_bf16 v[32:35], v[152:155], v[208:211], v[32:35]\n"
    "ds_read_b128 v[152:155], v242 offset:4096\n"
    "v_xor_b32_e32 v245, 32, v244\n"
    "v_mfma_f32_16x16x32_bf16 v[44:47], v[156:159], v[208:211], v[44:47]\n"
    "ds_read_b128 v[156:159], v243 offset:4096\n"
    "v_mfma_f32_16x16x32_bf16 v[52:55], v[160:163], v[204:207], v[52:55]\n"
    "s_add_u32 m0, s56, 98304\n"
    "s_add_u32 s61, s65, 114688\n"
    "s_and_b32 s61, s61, 0x1ffff\n"
    "buffer_load_dwordx4 v247, s[48:51], s61 offen offset:2048 nt lds\n"
    "v_mfma_f32_16x16x32_bf16 v[64:67], v[164:167], v[204:207], v[64:67]\n"
    "v_mfma_f32_16x16x32_bf16 v[76:79], v[168:171], v[204:207], v[76:79]\n"
    "v_mfma_f32_16x16x32_bf16 v[88:91], v[172:175], v[204:207], v[88:91]\n"
    "v_mfma_f32_16x16x32_bf16 v[56:59], v[160:163], v[208:211], v[56:59]\n"
    "ds_read_b128 v[160:163], v242 offset:16384\n"
    "v_mfma_f32_16x16x32_bf16 v[68:71], v[164:167], v[208:211], v[68:71]\n"
    "ds_read_b128 v[164:167], v243 offset:16384\n"
    "v_mfma_f32_16x16x32_bf16 v[80:83], v[168:171], v[208:211], v[80:83]\n"
    "ds_read_b128 v[168:171], v242 offset:20480\n"
    "v_mfma_f32_16x16x32_bf16 v[92:95], v[172:175], v[208:211], v[92:95]\n"
    "ds_read_b128 v[172:175], v243 offset:20480\n"
    "v_mfma_f32_16x16x32_bf16 v[220:223], v[236:239], v[208:211], v[220:223]\n"
    "s_waitcnt lgkmcnt(4)\n"
    "v_mfma_f32_16x16x32_bf16 v[176:179], v[144:147], v[96:99], v[224:227]\n"
    "v_mbcnt_lo_u32_b32 v241, -1, 0\n"
    "v_mfma_f32_16x16x32_bf16 v[180:183], v[144:147], v[100:103], v[228:231]\n"
    "v_mbcnt_hi_u32_b32 v241, -1, v241\n"
    "v_mfma_f32_16x16x32_bf16 v[184:187], v[144:147], v[104:107], v[232:235]\n"
    "ds_read_b64_tr_b16 v[144:145], v244 offset:0\n"
    "ds_read_b64_tr_b16 v[146:147], v244 offset:16384\n"
    "v_mfma_f32_16x16x32_bf16 v[176:179], v[148:151], v[108:111], v[176:179]\n"
    "v_mfma_f32_16x16x32_bf16 v[180:183], v[148:151], v[112:115], v[180:183]\n"
    "v_mfma_f32_16x16x32_bf16 v[184:187], v[148:151], v[116:119], v[184:187]\n"
    "ds_read_b64_tr_b16 v[148:149], v245 offset:0\n"
    "ds_read_b64_tr_b16 v[150:151], v245 offset:16384\n"
    "v_mfma_f32_16x16x32_bf16 v[176:179], v[152:155], v[120:123], v[176:179]\n"
    "v_mfma_f32_16x16x32_bf16 v[180:183], v[152:155], v[124:127], v[180:183]\n"
    "v_mfma_f32_16x16x32_bf16 v[184:187], v[152:155], v[128:131], v[184:187]\n"
    "ds_read_b64_tr_b16 v[152:153], v244 offset:512\n"
    "ds_read_b64_tr_b16 v[154:155], v244 offset:16896\n"
    "s_add_u32 m0, s56, 98304\n"
    "s_add_u32 s61, s65, 114688\n"
    "s_and_b32 s61, s61, 0x1ffff\n"
    "s_add_u32 s61, s61, 130048\n"
    "buffer_load_dwordx4 v247, s[48:51], s61 offen offset:3072 nt lds\n"
    "v_mfma_f32_16x16x32_bf16 v[176:179], v[156:159], v[132:135], v[176:179]\n"
    "v_add_u32_e32 v242, 32768, v242\n"
    "v_mfma_f32_16x16x32_bf16 v[180:183], v[156:159], v[136:139], v[180:183]\n"
    "v_xor_b32_e32 v243, 64, v242\n"
    "v_mfma_f32_16x16x32_bf16 v[184:187], v[156:159], v[140:143], v[184:187]\n"
    "ds_read_b64_tr_b16 v[156:157], v245 offset:512\n"
    "ds_read_b64_tr_b16 v[158:159], v245 offset:16896\n"
    "s_waitcnt lgkmcnt(8)\n"
    "v_mfma_f32_16x16x32_bf16 v[188:191], v[160:163], v[96:99], v[224:227]\n"
    "v_mfma_f32_16x16x32_bf16 v[188:191], v[164:167], v[108:111], v[188:191]\n"
    "v_exp_f32_e32 v248, v176\n"
    "v_exp_f32_e32 v249, v177\n"
    "v_exp_f32_e32 v250, v178\n"
    "v_mfma_f32_16x16x32_bf16 v[188:191], v[168:171], v[120:123], v[188:191]\n"
    "v_cvt_pk_bf16_f32 v200, v248, v249\n"
    "v_exp_f32_e32 v251, v179\n"
    "v_mfma_f32_16x16x32_bf16 v[188:191], v[172:175], v[132:135], v[188:191]\n"
    "v_exp_f32_e32 v248, v180\n"
    "v_cvt_pk_bf16_f32 v201, v250, v251\n"
    "v_mfma_f32_16x16x32_bf16 v[192:195], v[160:163], v[100:103], v[228:231]\n"
    "v_exp_f32_e32 v249, v181\n"
    "v_exp_f32_e32 v250, v182\n"
    "v_mfma_f32_16x16x32_bf16 v[192:195], v[164:167], v[112:115], v[192:195]\n"
    "v_cvt_pk_bf16_f32 v204, v248, v249\n"
    "v_exp_f32_e32 v251, v183\n"
    "v_mfma_f32_16x16x32_bf16 v[192:195], v[168:171], v[124:127], v[192:195]\n"
    "v_exp_f32_e32 v248, v188\n"
    "v_cvt_pk_bf16_f32 v205, v250, v251\n"
    "v_mfma_f32_16x16x32_bf16 v[192:195], v[172:175], v[136:139], v[192:195]\n"
    "v_exp_f32_e32 v249, v189\n"
    "v_exp_f32_e32 v250, v190\n"
    "v_mfma_f32_16x16x32_bf16 v[196:199], v[160:163], v[104:107], v[232:235]\n"
    "ds_read_b64_tr_b16 v[160:161], v244 offset:4096\n"
    "ds_read_b64_tr_b16 v[162:163], v244 offset:20480\n"
    "v_cvt_pk_bf16_f32 v202, v248, v249\n"
    "v_exp_f32_e32 v251, v191\n"
    "v_mfma_f32_16x16x32_bf16 v[196:199], v[164:167], v[116:119], v[196:199]\n"
    "ds_read_b64_tr_b16 v[164:165], v245 offset:4096\n"
    "ds_read_b64_tr_b16 v[166:167], v245 offset:20480\n"
    "v_or3_b32 v255, v200, v201, v202\n"
    "v_cvt_pk_bf16_f32 v203, v250, v251\n"
    "v_mfma_f32_16x16x32_bf16 v[196:199], v[168:171], v[128:131], v[196:199]\n"
    "ds_read_b64_tr_b16 v[168:169], v244 offset:4608\n"
    "ds_read_b64_tr_b16 v[170:171], v244 offset:20992\n"
    "v_bitop3_b32 v255, v255, s63, v203 bitop3:0xc8\n"
    "v_cmp_ne_u32_e32 vcc, 0, v255\n"
    "v_mfma_f32_16x16x32_bf16 v[196:199], v[172:175], v[140:143], v[196:199]\n"
    "s_cbranch_vccnz .Lst12_0_%=\n"
    ".Lbk12_0_%=:\n"
    "ds_read_b64_tr_b16 v[172:173], v245 offset:4608\n"
    "ds_read_b64_tr_b16 v[174:175], v245 offset:20992\n"
    "v_exp_f32_e32 v248, v192\n"
    "v_exp_f32_e32 v249, v193\n"
    "s_waitcnt lgkmcnt(8)\n"
    "v_mfma_f32_16x16x32_bf16 v[0:3], v[144:147], v[200:203], v[0:3]\n"
    "v_exp_f32_e32 v250, v194\n"
    "v_cvt_pk_bf16_f32 v206, v248, v249\n"
    "s_add_u32 m0, s56, 131072\n"
    "s_add_u32 s61, s65, 122880\n"
    "s_and_b32 s61, s61, 0x1ffff\n"
    "buffer_load_dwordx4 v246, s[44:47], s61 offen offset:0 nt lds\n"
    "v_mfma_f32_16x16x32_bf16 v[12:15], v[148:151], v[200:203], v[12:15]\n"
    "v_exp_f32_e32 v251, v195\n"
    "v_or3_b32 v255, v204, v205, v206\n"
    "v_mfma_f32_16x16x32_bf16 v[24:27], v[152:155], v[200:203], v[24:27]\n"
    "v_cvt_pk_bf16_f32 v207, v250, v251\n"
    "v_bitop3_b32 v255, v255, s63, v207 bitop3:0xc8\n"
    "v_mfma_f32_16x16x32_bf16 v[36:39], v[156:159], v[200:203], v[36:39]\n"
    "v_cmp_ne_u32_e32 vcc, 0, v255\n"
    "v_mfma_f32_16x16x32_bf16 v[212:215], v[236:239], v[200:203], v[212:215]\n"
    "s_cbranch_vccnz .Lst12_1_%=\n"
    ".Lbk12_1_%=:\n"
    "v_exp_f32_e32 v248, v184\n"
    "v_exp_f32_e32 v249, v185\n"
    "v_mfma_f32_16x16x32_bf16 v[4:7], v[144:147], v[204:207], v[4:7]\n"
    "v_exp_f32_e32 v250, v186\n"
    "v_cvt_pk_bf16_f32 v208, v248, v249\n"
    "v_mfma_f32_16x16x32_bf16 v[16:19], v[148:151], v[204:207], v[16:19]\n"
    "v_exp_f32_e32 v251, v187\n"
    "v_mfma_f32_16x16x32_bf16 v[28:31], v[152:155], v[204:207], v[28:31]\n"
    "v_exp_f32_e32 v248, v196\n"
    "v_cvt_pk_bf16_f32 v209, v250, v251\n"
    "v_mfma_f32_16x16x32_bf16 v[40:43], v[156:159], v[204:207], v[40:43]\n"
    "v_exp_f32_e32 v249, v197\n"
    "v_exp_f32_e32 v250, v198\n"
    "v_mfma_f32_16x16x32_bf16 v[216:219], v[236:239], v[204:207], v[216:219]\n"
    "v_cvt_pk_bf16_f32 v210, v248, v249\n"
    "s_waitcnt lgkmcnt(0)\n"
    "v_mfma_f32_16x16x32_bf16 v[48:51], v[160:163], v[200:203], v[48:51]\n"
    "v_exp_f32_e32 v251, v199\n"
    "v_or3_b32 v255, v208, v209, v210\n"
    "v_mfma_f32_16x16x32_bf16 v[60:63], v[164:167], v[200:203], v[60:63]\n"
    "v_cvt_pk_bf16_f32 v211, v250, v251\n"
    "v_bitop3_b32 v255, v255, s63, v211 bitop3:0xc8\n"
    "v_mfma_f32_16x16x32_bf16 v[72:75], v[168:171], v[200:203], v[72:75]\n"
    "v_cmp_ne_u32_e32 vcc, 0, v255\n"
    "v_mfma_f32_16x16x32_bf16 v[84:87], v[172:175], v[200:203], v[84:87]\n"
    "s_cbranch_vccnz .Lst12_2_%=\n"
    ".Lbk12_2_%=:\n"
    "s_waitcnt vmcnt(5)\n"
    "s_barrier\n"
    "v_mfma_f32_16x16x32_bf16 v[8:11], v[144:147], v[208:211], v[8:11]\n"
    "ds_read_b128 v[144:147], v242 offset:0\n"
    "v_add_u32_e32 v248, 0, v241\n"
    "v_mul_u32_u24_e32 v240, 0xccd, v248\n"
    "v_mfma_f32_16x16x32_bf16 v[20:23], v[148:151], v[208:211], v[20:23]\n"
    "ds_read_b128 v[148:151], v243 offset:0\n"
    "v_add_u32_e32 v244, 32768, v244\n"
    "v_lshrrev_b32_e32 v240, 16, v240\n"
    "v_mul_u32_u24_e32 v249, 20, v240\n"
    "v_mfma_f32_16x16x32_bf16 v[32:35], v[152:155], v[208:211], v[32:35]\n"
    "ds_read_b128 v[152:155], v242 offset:4096\n"
    "v_xor_b32_e32 v245, 32, v244\n"
    "s_add_u32 m0, s56, 131072\n"
    "s_add_u32 s61, s65, 122880\n"
    "s_and_b32 s61, s61, 0x1ffff\n"
    "s_add_u32 s61, s61, 130048\n"
    "buffer_load_dwordx4 v246, s[44:47], s61 offen offset:1024 nt lds\n"
    "v_sub_u32_e32 v248, v248, v249\n"
    "v_mfma_f32_16x16x32_bf16 v[44:47], v[156:159], v[208:211], v[44:47]\n"
    "ds_read_b128 v[156:159], v243 offset:4096\n"
    "v_lshrrev_b32_e32 v249, 1, v240\n"
    "v_and_b32_e32 v249, 2, v249\n"
    "v_mfma_f32_16x16x32_bf16 v[52:55], v[160:163], v[204:207], v[52:55]\n"
    "v_xor_b32_e32 v248, v248, v249\n"
    "v_mul_u32_u24_e32 v240, 0x500, v240\n"
    "v_mfma_f32_16x16x32_bf16 v[64:67], v[164:167], v[204:207], v[64:67]\n"
    "v_lshl_add_u32 v240, v248, 4, v240\n"
    "v_mfma_f32_16x16x32_bf16 v[76:79], v[168:171], v[204:207], v[76:79]\n"
    "s_mov_b32 m0, s64\n"
    "s_add_u32 s60, s64, s68\n"
    "buffer_load_dwordx4 v240, s[52:55], s69 offen offset:0 lds\n"
    "v_mfma_f32_16x16x32_bf16 v[88:91], v[172:175], v[204:207], v[88:91]\n"
    "v_mfma_f32_16x16x32_bf16 v[56:59], v[160:163], v[208:211], v[56:59]\n"
    "ds_read_b128 v[160:163], v242 offset:16384\n"
    "v_mfma_f32_16x16x32_bf16 v[68:71], v[164:167], v[208:211], v[68:71]\n"
    "ds_read_b128 v[164:167], v243 offset:16384\n"
    "s_mov_b32 m0, s60\n"
    "s_add_u32 s60, s60, s68\n"
    "buffer_load_dwordx4 v240, s[52:55], s70 offen offset:0 lds\n"
    "v_mfma_f32_16x16x32_bf16 v[80:83], v[168:171], v[208:211], v[80:83]\n"
    "ds_read_b128 v[168:171], v242 offset:20480\n"
    "v_mfma_f32_16x16x32_bf16 v[92:95], v[172:175], v[208:211], v[92:95]\n"
    "ds_read_b128 v[172:175], v243 offset:20480\n"
    "v_mfma_f32_16x16x32_bf16 v[220:223], v[236:239], v[208:211], v[220:223]\n"
    "s_mov_b32 m0, s60\n"
    "s_add_u32 s64, s64, s67\n"
    "buffer_load_dwordx4 v240, s[52:55], s71 offen offset:0 lds\n"
    "s_waitcnt lgkmcnt(4)\n"
    "v_mfma_f32_16x16x32_bf16 v[176:179], v[144:147], v[96:99], v[224:227]\n"
    "v_add_u32_e32 v248, 64, v241\n"
    "v_mul_u32_u24_e32 v240, 0xccd, v248\n"
    "v_mfma_f32_16x16x32_bf16 v[180:183], v[144:147], v[100:103], v[228:231]\n"
    "v_lshrrev_b32_e32 v240, 16, v240\n"
    "v_mul_u32_u24_e32 v249, 20, v240\n"
    "v_mfma_f32_16x16x32_bf16 v[184:187], v[144:147], v[104:107], v[232:235]\n"
    "ds_read_b64_tr_b16 v[144:145], v244 offset:0\n"
    "ds_read_b64_tr_b16 v[146:147], v244 offset:16384\n"
    "v_sub_u32_e32 v248, v248, v249\n"
    "v_mfma_f32_16x16x32_bf16 v[176:179], v[148:151], v[108:111], v[176:179]\n"
    "v_lshrrev_b32_e32 v249, 1, v240\n"
    "v_and_b32_e32 v249, 2, v249\n"
    "v_mfma_f32_16x16x32_bf16 v[180:183], v[148:151], v[112:115], v[180:183]\n"
    "v_xor_b32_e32 v248, v248, v249\n"
    "v_mul_u32_u24_e32 v240, 0x500, v240\n"
    "v_mfma_f32_16x16x32_bf16 v[184:187], v[148:151], v[116:119], v[184:187]\n"
    "ds_read_b64_tr_b16 v[148:149], v245 offset:0\n"
    "ds_read_b64_tr_b16 v[150:151], v245 offset:16384\n"
    "s_add_u32 m0, s56, 131072\n"
    "s_add_u32 s61, s65, 122880\n"
    "s_and_b32 s61, s61, 0x1ffff\n"
    "buffer_load_dwordx4 v247, s[48:51], s61 offen offset:2048 nt lds\n"
    "v_lshl_add_u32 v240, v248, 4, v240\n"
    "v_mfma_f32_16x16x32_bf16 v[176:179], v[152:155], v[120:123], v[176:179]\n"
    "s_mov_b32 m0, s64\n"
    "s_add_u32 s60, s64, s68\n"
    "buffer_load_dwordx4 v240, s[52:55], s69 offen offset:0 lds\n"
    "v_mfma_f32_16x16x32_bf16 v[180:183], v[152:155], v[124:127], v[180:183]\n"
    "v_mfma_f32_16x16x32_bf16 v[184:187], v[152:155], v[128:131], v[184:187]\n"
    "ds_read_b64_tr_b16 v[152:153], v244 offset:512\n"
    "ds_read_b64_tr_b16 v[154:155], v244 offset:16896\n"
    "v_mfma_f32_16x16x32_bf16 v[176:179], v[156:159], v[132:135], v[176:179]\n"
    "v_add_u32_e32 v242, 32768, v242\n"
    "s_mov_b32 m0, s60\n"
    "s_add_u32 s60, s60, s68\n"
    "buffer_load_dwordx4 v240, s[52:55], s70 offen offset:0 lds\n"
    "v_mfma_f32_16x16x32_bf16 v[180:183], v[156:159], v[136:139], v[180:183]\n"
    "v_xor_b32_e32 v243, 64, v242\n"
    "v_mfma_f32_16x16x32_bf16 v[184:187], v[156:159], v[140:143], v[184:187]\n"
    "ds_read_b64_tr_b16 v[156:157], v245 offset:512\n"
    "ds_read_b64_tr_b16 v[158:159], v245 offset:16896\n"
    "s_waitcnt lgkmcnt(8)\n"
    "v_mfma_f32_16x16x32_bf16 v[188:191], v[160:163], v[96:99], v[224:227]\n"
    "s_mov_b32 m0, s60\n"
    "s_add_u32 s64, s64, s67\n"
    "buffer_load_dwordx4 v240, s[52:55], s71 offen offset:0 lds\n"
    "v_mfma_f32_16x16x32_bf16 v[188:191], v[164:167], v[108:111], v[188:191]\n"
    "v_exp_f32_e32 v248, v176\n"
    "v_exp_f32_e32 v249, v177\n"
    "v_exp_f32_e32 v250, v178\n"
    "v_mfma_f32_16x16x32_bf16 v[188:191], v[168:171], v[120:123], v[188:191]\n"
    "v_cvt_pk_bf16_f32 v200, v248, v249\n"
    "v_exp_f32_e32 v251, v179\n"
    "v_mfma_f32_16x16x32_bf16 v[188:191], v[172:175], v[132:135], v[188:191]\n"
    "v_exp_f32_e32 v248, v180\n"
    "v_cvt_pk_bf16_f32 v201, v250, v251\n"
    "v_mfma_f32_16x16x32_bf16 v[192:195], v[160:163], v[100:103], v[228:231]\n"
    "v_exp_f32_e32 v249, v181\n"
    "v_exp_f32_e32 v250, v182\n"
    "v_mfma_f32_16x16x32_bf16 v[192:195], v[164:167], v[112:115], v[192:195]\n"
    "v_cvt_pk_bf16_f32 v204, v248, v249\n"
    "v_exp_f32_e32 v251, v183\n"
    "v_mfma_f32_16x16x32_bf16 v[192:195], v[168:171], v[124:127], v[192:195]\n"
    "v_exp_f32_e32 v248, v188\n"
    "v_cvt_pk_bf16_f32 v205, v250, v251\n"
    "v_mfma_f32_16x16x32_bf16 v[192:195], v[172:175], v[136:139], v[192:195]\n"
    "v_exp_f32_e32 v249, v189\n"
    "v_exp_f32_e32 v250, v190\n"
    "v_mfma_f32_16x16x32_bf16 v[196:199], v[160:163], v[104:107], v[232:235]\n"
    "ds_read_b64_tr_b16 v[160:161], v244 offset:4096\n"
    "ds_read_b64_tr_b16 v[162:163], v244 offset:20480\n"
    "v_cvt_pk_bf16_f32 v202, v248, v249\n"
    "v_exp_f32_e32 v251, v191\n"
    "v_mfma_f32_16x16x32_bf16 v[196:199], v[164:167], v[116:119], v[196:199]\n"
    "ds_read_b64_tr_b16 v[164:165], v245 offset:4096\n"
    "ds_read_b64_tr_b16 v[166:167], v245 offset:20480\n"
    "v_or3_b32 v255, v200, v201, v202\n"
    "v_cvt_pk_bf16_f32 v203, v250, v251\n"
    "s_add_u32 m0, s56, 131072\n"
    "s_add_u32 s61, s65, 122880\n"
    "s_and_b32 s61, s61, 0x1ffff\n"
    "s_add_u32 s61, s61, 130048\n"
    "buffer_load_dwordx4 v247, s[48:51], s61 offen offset:3072 nt lds\n"
    "v_mfma_f32_16x16x32_bf16 v[196:199], v[168:171], v[128:131], v[196:199]\n"
    "ds_read_b64_tr_b16 v[168:169], v244 offset:4608\n"
    "ds_read_b64_tr_b16 v[170:171], v244 offset:20992\n"
    "v_bitop3_b32 v255, v255, s63, v203 bitop3:0xc8\n"
    "v_cmp_ne_u32_e32 vcc, 0, v255\n"
    "v_mfma_f32_16x16x32_bf16 v[196:199], v[172:175], v[140:143], v[196:199]\n"
    "s_cbranch_vccnz .Lst13_0_%=\n"
    ".Lbk13_0_%=:\n"
    "ds_read_b64_tr_b16 v[172:173], v245 offset:4608\n"
    "ds_read_b64_tr_b16 v[174:175], v245 offset:20992\n"
    "v_exp_f32_e32 v248, v192\n"
    "v_exp_f32_e32 v249, v193\n"
    "s_waitcnt lgkmcnt(8)\n"
    "v_mfma_f32_16x16x32_bf16 v[0:3], v[144:147], v[200:203], v[0:3]\n"
    "v_exp_f32_e32 v250, v194\n"
    "v_cvt_pk_bf16_f32 v206, v248, v249\n"
    "v_mfma_f32_16x16x32_bf16 v[12:15], v[148:151], v[200:203], v[12:15]\n"
    "v_exp_f32_e32 v251, v195\n"
    "v_or3_b32 v255, v204, v205, v206\n"
    "v_mfma_f32_16x16x32_bf16 v[24:27], v[152:155], v[200:203], v[24:27]\n"
    "v_cvt_pk_bf16_f32 v207, v250, v251\n"
    "v_bitop3_b32 v255, v255, s63, v207 bitop3:0xc8\n"
    "v_mfma_f32_16x16x32_bf16 v[36:39], v[156:159], v[200:203], v[36:39]\n"
    "v_cmp_ne_u32_e32 vcc, 0, v255\n"
    "v_mfma_f32_16x16x32_bf16 v[212:215], v[236:239], v[200:203], v[212:215]\n"
    "s_cbranch_vccnz .Lst13_1_%=\n"
    ".Lbk13_1_%=:\n"
    "v_exp_f32_e32 v248, v184\n"
    "v_exp_f32_e32 v249, v185\n"
    "v_mfma_f32_16x16x32_bf16 v[4:7], v[144:147], v[204:207], v[4:7]\n"
    "v_exp_f32_e32 v250, v186\n"
    "v_cvt_pk_bf16_f32 v208, v248, v249\n"
    "v_mfma_f32_16x16x32_bf16 v[16:19], v[148:151], v[204:207], v[16:19]\n"
    "v_exp_f32_e32 v251, v187\n"
    "v_mfma_f32_16x16x32_bf16 v[28:31], v[152:155], v[204:207], v[28:31]\n"
    "v_exp_f32_e32 v248, v196\n"
    "v_cvt_pk_bf16_f32 v209, v250, v251\n"
    "v_mfma_f32_16x16x32_bf16 v[40:43], v[156:159], v[204:207], v[40:43]\n"
    "v_exp_f32_e32 v249, v197\n"
    "v_exp_f32_e32 v250, v198\n"
    "v_mfma_f32_16x16x32_bf16 v[216:219], v[236:239], v[204:207], v[216:219]\n"
    "v_cvt_pk_bf16_f32 v210, v248, v249\n"
    "s_waitcnt lgkmcnt(0)\n"
    "v_mfma_f32_16x16x32_bf16 v[48:51], v[160:163], v[200:203], v[48:51]\n"
    "v_exp_f32_e32 v251, v199\n"
    "v_or3_b32 v255, v208, v209, v210\n"
    "v_mfma_f32_16x16x32_bf16 v[60:63], v[164:167], v[200:203], v[60:63]\n"
    "v_cvt_pk_bf16_f32 v211, v250, v251\n"
    "v_bitop3_b32 v255, v255, s63, v211 bitop3:0xc8\n"
    "v_mfma_f32_16x16x32_bf16 v[72:75], v[168:171], v[200:203], v[72:75]\n"
    "v_cmp_ne_u32_e32 vcc, 0, v255\n"
    "v_mfma_f32_16x16x32_bf16 v[84:87], v[172:175], v[200:203], v[84:87]\n"
    "s_cbranch_vccnz .Lst13_2_%=\n"
    ".Lbk13_2_%=:\n"
    "s_waitcnt vmcnt(10)\n"
    "s_barrier\n"
    "v_mfma_f32_16x16x32_bf16 v[8:11], v[144:147], v[208:211], v[8:11]\n"
    "ds_read_b128 v[144:147], v242 offset:0\n"
    "v_add_u32_e32 v248, 128, v241\n"
    "v_mul_u32_u24_e32 v240, 0xccd, v248\n"
    "v_mfma_f32_16x16x32_bf16 v[20:23], v[148:151], v[208:211], v[20:23]\n"
    "ds_read_b128 v[148:151], v243 offset:0\n"
    "v_add_u32_e32 v244, 32768, v244\n"
    "v_lshrrev_b32_e32 v240, 16, v240\n"
    "v_mul_u32_u24_e32 v249, 20, v240\n"
    "v_mfma_f32_16x16x32_bf16 v[32:35], v[152:155], v[208:211], v[32:35]\n"
    "ds_read_b128 v[152:155], v242 offset:4096\n"
    "v_xor_b32_e32 v245, 32, v244\n"
    "v_sub_u32_e32 v248, v248, v249\n"
    "v_mfma_f32_16x16x32_bf16 v[44:47], v[156:159], v[208:211], v[44:47]\n"
    "ds_read_b128 v[156:159], v243 offset:4096\n"
    "v_lshrrev_b32_e32 v249, 1, v240\n"
    "v_and_b32_e32 v249, 2, v249\n"
    "v_mfma_f32_16x16x32_bf16 v[52:55], v[160:163], v[204:207], v[52:55]\n"
    "v_xor_b32_e32 v248, v248, v249\n"
    "v_mul_u32_u24_e32 v240, 0x500, v240\n"
    "v_mfma_f32_16x16x32_bf16 v[64:67], v[164:167], v[204:207], v[64:67]\n"
    "v_lshl_add_u32 v240, v248, 4, v240\n"
    "v_mfma_f32_16x16x32_bf16 v[76:79], v[168:171], v[204:207], v[76:79]\n"
    "s_mov_b32 m0, s64\n"
    "s_add_u32 s60, s64, s68\n"
    "buffer_load_dwordx4 v240, s[52:55], s69 offen offset:0 lds\n"
    "v_mfma_f32_16x16x32_bf16 v[88:91], v[172:175], v[204:207], v[88:91]\n"
    "v_mfma_f32_16x16x32_bf16 v[56:59], v[160:163], v[208:211], v[56:59]\n"
    "ds_read_b128 v[160:163], v242 offset:16384\n"
    "v_mfma_f32_16x16x32_bf16 v[68:71], v[164:167], v[208:211], v[68:71]\n"
    "ds_read_b128 v[164:167], v243 offset:16384\n"
    "s_mov_b32 m0, s60\n"
    "s_add_u32 s60, s60, s68\n"
    "buffer_load_dwordx4 v240, s[52:55], s70 offen offset:0 lds\n"
    "v_mfma_f32_16x16x32_bf16 v[80:83], v[168:171], v[208:211], v[80:83]\n"
    "ds_read_b128 v[168:171], v242 offset:20480\n"
    "v_mfma_f32_16x16x32_bf16 v[92:95], v[172:175], v[208:211], v[92:95]\n"
    "ds_read_b128 v[172:175], v243 offset:20480\n"
    "v_mfma_f32_16x16x32_bf16 v[220:223], v[236:239], v[208:211], v[220:223]\n"
    "s_mov_b32 m0, s60\n"
    "s_add_u32 s64, s64, s67\n"
    "buffer_load_dwordx4 v240, s[52:55], s71 offen offset:0 lds\n"
    "s_waitcnt lgkmcnt(4)\n"
    "v_mfma_f32_16x16x32_bf16 v[176:179], v[144:147], v[96:99], v[224:227]\n"
    "v_add_u32_e32 v248, 192, v241\n"
    "v_mul_u32_u24_e32 v240, 0xccd, v248\n"
    "v_mfma_f32_16x16x32_bf16 v[180:183], v[144:147], v[100:103], v[228:231]\n"
    "v_lshrrev_b32_e32 v240, 16, v240\n"
    "v_mul_u32_u24_e32 v249, 20, v240\n"
    "v_mfma_f32_16x16x32_bf16 v[184:187], v[144:147], v[104:107], v[232:235]\n"
    "ds_read_b64_tr_b16 v[144:145], v244 offset:0\n"
    "ds_read_b64_tr_b16 v[146:147], v244 offset:16384\n"
    "v_sub_u32_e32 v248, v248, v249\n"
    "v_mfma_f32_16x16x32_bf16 v[176:179], v[148:151], v[108:111], v[176:179]\n"
    "v_lshrrev_b32_e32 v249, 1, v240\n"
    "v_and_b32_e32 v249, 2, v249\n"
    "v_mfma_f32_16x16x32_bf16 v[180:183], v[148:151], v[112:115], v[180:183]\n"
    "v_xor_b32_e32 v248, v248, v249\n"
    "v_mul_u32_u24_e32 v240, 0x500, v240\n"
    "v_mfma_f32_16x16x32_bf16 v[184:187], v[148:151], v[116:119], v[184:187]\n"
    "ds_read_b64_tr_b16 v[148:149], v245 offset:0\n"
    "ds_read_b64_tr_b16 v[150:151], v245 offset:16384\n"
    "v_lshl_add_u32 v240, v248, 4, v240\n"
    "v_mfma_f32_16x16x32_bf16 v[176:179], v[152:155], v[120:123], v[176:179]\n"
    "s_mov_b32 m0, s64\n"
    "s_add_u32 s60, s64, s68\n"
    "buffer_load_dwordx4 v240, s[52:55], s69 offen offset:0 lds\n"
    "v_mfma_f32_16x16x32_bf16 v[180:183], v[152:155], v[124:127], v[180:183]\n"
    "v_mfma_f32_16x16x32_bf16 v[184:187], v[152:155], v[128:131], v[184:187]\n"
    "ds_read_b64_tr_b16 v[152:153], v244 offset:512\n"
    "ds_read_b64_tr_b16 v[154:155], v244 offset:16896\n"
    "v_mfma_f32_16x16x32_bf16 v[176:179], v[156:159], v[132:135], v[176:179]\n"
    "v_add_u32_e32 v242, 32768, v242\n"
    "s_mov_b32 m0, s60\n"
    "s_add_u32 s60, s60, s68\n"
    "buffer_load_dwordx4 v240, s[52:55], s70 offen offset:0 lds\n"
    "v_mfma_f32_16x16x32_bf16 v[180:183], v[156:159], v[136:139], v[180:183]\n"
    "v_xor_b32_e32 v243, 64, v242\n"
    "v_mfma_f32_16x16x32_bf16 v[184:187], v[156:159], v[140:143], v[184:187]\n"
    "ds_read_b64_tr_b16 v[156:157], v245 offset:512\n"
    "ds_read_b64_tr_b16 v[158:159], v245 offset:16896\n"
    "s_waitcnt lgkmcnt(8)\n"
    "v_mfma_f32_16x16x32_bf16 v[188:191], v[160:163], v[96:99], v[224:227]\n"
    "s_mov_b32 m0, s60\n"
    "s_add_u32 s64, s64, s67\n"
    "buffer_load_dwordx4 v240, s[52:55], s71 offen offset:0 lds\n"
    "v_mfma_f32_16x16x32_bf16 v[188:191], v[164:167], v[108:111], v[188:191]\n"
    "v_exp_f32_e32 v248, v176\n"
    "v_exp_f32_e32 v249, v177\n"
    "v_exp_f32_e32 v250, v178\n"
    "v_mfma_f32_16x16x32_bf16 v[188:191], v[168:171], v[120:123], v[188:191]\n"
    "v_cvt_pk_bf16_f32 v200, v248, v249\n"
    "v_exp_f32_e32 v251, v179\n"
    "v_mfma_f32_16x16x32_bf16 v[188:191], v[172:175], v[132:135], v[188:191]\n"
    "v_exp_f32_e32 v248, v180\n"
    "v_cvt_pk_bf16_f32 v201, v250, v251\n"
    "v_mfma_f32_16x16x32_bf16 v[192:195], v[160:163], v[100:103], v[228:231]\n"
    "v_exp_f32_e32 v249, v181\n"
    "v_exp_f32_e32 v250, v182\n"
    "v_mfma_f32_16x16x32_bf16 v[192:195], v[164:167], v[112:115], v[192:195]\n"
    "v_cvt_pk_bf16_f32 v204, v248, v249\n"
    "v_exp_f32_e32 v251, v183\n"
    "v_mfma_f32_16x16x32_bf16 v[192:195], v[168:171], v[124:127], v[192:195]\n"
    "v_exp_f32_e32 v248, v188\n"
    "v_cvt_pk_bf16_f32 v205, v250, v251\n"
    "v_mfma_f32_16x16x32_bf16 v[192:195], v[172:175], v[136:139], v[192:195]\n"
    "v_exp_f32_e32 v249, v189\n"
    "v_exp_f32_e32 v250, v190\n"
    "v_mfma_f32_16x16x32_bf16 v[196:199], v[160:163], v[104:107], v[232:235]\n"
    "ds_read_b64_tr_b16 v[160:161], v244 offset:4096\n"
    "ds_read_b64_tr_b16 v[162:163], v244 offset:20480\n"
    "v_cvt_pk_bf16_f32 v202, v248, v249\n"
    "v_exp_f32_e32 v251, v191\n"
    "v_mfma_f32_16x16x32_bf16 v[196:199], v[164:167], v[116:119], v[196:199]\n"
    "ds_read_b64_tr_b16 v[164:165], v245 offset:4096\n"
    "ds_read_b64_tr_b16 v[166:167], v245 offset:20480\n"
    "v_or3_b32 v255, v200, v201, v202\n"
    "v_cvt_pk_bf16_f32 v203, v250, v251\n"
    "v_mfma_f32_16x16x32_bf16 v[196:199], v[168:171], v[128:131], v[196:199]\n"
    "ds_read_b64_tr_b16 v[168:169], v244 offset:4608\n"
    "ds_read_b64_tr_b16 v[170:171], v244 offset:20992\n"
    "v_bitop3_b32 v255, v255, s63, v203 bitop3:0xc8\n"
    "v_cmp_ne_u32_e32 vcc, 0, v255\n"
    "v_mfma_f32_16x16x32_bf16 v[196:199], v[172:175], v[140:143], v[196:199]\n"
    "s_cbranch_vccnz .Lst14_0_%=\n"
    ".Lbk14_0_%=:\n"
    "ds_read_b64_tr_b16 v[172:173], v245 offset:4608\n"
    "ds_read_b64_tr_b16 v[174:175], v245 offset:20992\n"
    "v_exp_f32_e32 v248, v192\n"
    "v_exp_f32_e32 v249, v193\n"
    "s_waitcnt lgkmcnt(8)\n"
    "v_mfma_f32_16x16x32_bf16 v[0:3], v[144:147], v[200:203], v[0:3]\n"
    "v_exp_f32_e32 v250, v194\n"
    "v_cvt_pk_bf16_f32 v206, v248, v249\n"
    "v_mfma_f32_16x16x32_bf16 v[12:15], v[148:151], v[200:203], v[12:15]\n"
    "v_exp_f32_e32 v251, v195\n"
    "v_or3_b32 v255, v204, v205, v206\n"
    "v_mfma_f32_16x16x32_bf16 v[24:27], v[152:155], v[200:203], v[24:27]\n"
    "v_cvt_pk_bf16_f32 v207, v250, v251\n"
    "v_bitop3_b32 v255, v255, s63, v207 bitop3:0xc8\n"
    "v_mfma_f32_16x16x32_bf16 v[36:39], v[156:159], v[200:203], v[36:39]\n"
    "v_cmp_ne_u32_e32 vcc, 0, v255\n"
    "v_mfma_f32_16x16x32_bf16 v[212:215], v[236:239], v[200:203], v[212:215]\n"
    "s_cbranch_vccnz .Lst14_1_%=\n"
    ".Lbk14_1_%=:\n"
    "v_exp_f32_e32 v248, v184\n"
    "v_exp_f32_e32 v249, v185\n"
    "v_mfma_f32_16x16x32_bf16 v[4:7], v[144:147], v[204:207], v[4:7]\n"
    "v_exp_f32_e32 v250, v186\n"
    "v_cvt_pk_bf16_f32 v208, v248, v249\n"
    "v_mfma_f32_16x16x32_bf16 v[16:19], v[148:151], v[204:207], v[16:19]\n"
    "v_exp_f32_e32 v251, v187\n"
    "v_mfma_f32_16x16x32_bf16 v[28:31], v[152:155], v[204:207], v[28:31]\n"
    "v_exp_f32_e32 v248, v196\n"
    "v_cvt_pk_bf16_f32 v209, v250, v251\n"
    "v_mfma_f32_16x16x32_bf16 v[40:43], v[156:159], v[204:207], v[40:43]\n"
    "v_exp_f32_e32 v249, v197\n"
    "v_exp_f32_e32 v250, v198\n"
    "v_mfma_f32_16x16x32_bf16 v[216:219], v[236:239], v[204:207], v[216:219]\n"
    "v_cvt_pk_bf16_f32 v210, v248, v249\n"
    "s_waitcnt lgkmcnt(0)\n"
    "v_mfma_f32_16x16x32_bf16 v[48:51], v[160:163], v[200:203], v[48:51]\n"
    "v_exp_f32_e32 v251, v199\n"
    "v_or3_b32 v255, v208, v209, v210\n"
    "v_mfma_f32_16x16x32_bf16 v[60:63], v[164:167], v[200:203], v[60:63]\n"
    "v_cvt_pk_bf16_f32 v211, v250, v251\n"
    "v_bitop3_b32 v255, v255, s63, v211 bitop3:0xc8\n"
    "v_mfma_f32_16x16x32_bf16 v[72:75], v[168:171], v[200:203], v[72:75]\n"
    "v_cmp_ne_u32_e32 vcc, 0, v255\n"
    "v_mfma_f32_16x16x32_bf16 v[84:87], v[172:175], v[200:203], v[84:87]\n"
    "s_cbranch_vccnz .Lst14_2_%=\n"
    ".Lbk14_2_%=:\n"
    "s_waitcnt vmcnt(6)\n"
    "s_barrier\n"
    "v_mfma_f32_16x16x32_bf16 v[8:11], v[144:147], v[208:211], v[8:11]\n"
    "ds_read_b128 v[144:147], v242 offset:0\n"
    "v_add_u32_e32 v248, 256, v241\n"
    "v_mul_u32_u24_e32 v240, 0xccd, v248\n"
    "v_mfma_f32_16x16x32_bf16 v[20:23], v[148:151], v[208:211], v[20:23]\n"
    "ds_read_b128 v[148:151], v243 offset:0\n"
    "v_add_u32_e32 v244, 32768, v244\n"
    "v_lshrrev_b32_e32 v240, 16, v240\n"
    "v_mul_u32_u24_e32 v249, 20, v240\n"
    "v_mfma_f32_16x16x32_bf16 v[32:35], v[152:155], v[208:211], v[32:35]\n"
    "ds_read_b128 v[152:155], v242 offset:4096\n"
    "v_xor_b32_e32 v245, 32, v244\n"
    "v_sub_u32_e32 v248, v248, v249\n"
    "v_mfma_f32_16x16x32_bf16 v[44:47], v[156:159], v[208:211], v[44:47]\n"
    "ds_read_b128 v[156:159], v243 offset:4096\n"
    "v_lshrrev_b32_e32 v249, 1, v240\n"
    "v_and_b32_e32 v249, 2, v249\n"
    "v_mfma_f32_16x16x32_bf16 v[52:55], v[160:163], v[204:207], v[52:55]\n"
    "v_xor_b32_e32 v248, v248, v249\n"
    "v_mul_u32_u24_e32 v240, 0x500, v240\n"
    "v_mfma_f32_16x16x32_bf16 v[64:67], v[164:167], v[204:207], v[64:67]\n"
    "v_lshl_add_u32 v240, v248, 4, v240\n"
    "v_mfma_f32_16x16x32_bf16 v[76:79], v[168:171], v[204:207], v[76:79]\n"
    "s_mov_b32 m0, s64\n"
    "s_add_u32 s60, s64, s68\n"
    "buffer_load_dwordx4 v240, s[52:55], s69 offen offset:0 lds\n"
    "v_mfma_f32_16x16x32_bf16 v[88:91], v[172:175], v[204:207], v[88:91]\n"
    "v_mfma_f32_16x16x32_bf16 v[56:59], v[160:163], v[208:211], v[56:59]\n"
    "ds_read_b128 v[160:163], v242 offset:16384\n"
    "v_mfma_f32_16x16x32_bf16 v[68:71], v[164:167], v[208:211], v[68:71]\n"
    "ds_read_b128 v[164:167], v243 offset:16384\n"
    "s_mov_b32 m0, s60\n"
    "s_add_u32 s60, s60, s68\n"
    "buffer_load_dwordx4 v240, s[52:55], s70 offen offset:0 lds\n"
    "v_mfma_f32_16x16x32_bf16 v[80:83], v[168:171], v[208:211], v[80:83]\n"
    "ds_read_b128 v[168:171], v242 offset:20480\n"
    "v_mfma_f32_16x16x32_bf16 v[92:95], v[172:175], v[208:211], v[92:95]\n"
    "ds_read_b128 v[172:175], v243 offset:20480\n"
    "v_mfma_f32_16x16x32_bf16 v[220:223], v[236:239], v[208:211], v[220:223]\n"
    "s_mov_b32 m0, s60\n"
    "s_add_u32 s64, s64, s67\n"
    "buffer_load_dwordx4 v240, s[52:55], s71 offen offset:0 lds\n"
    "s_waitcnt lgkmcnt(4)\n"
    "v_mfma_f32_16x16x32_bf16 v[176:179], v[144:147], v[96:99], v[224:227]\n"
    "v_mfma_f32_16x16x32_bf16 v[180:183], v[144:147], v[100:103], v[228:231]\n"
    "v_mfma_f32_16x16x32_bf16 v[184:187], v[144:147], v[104:107], v[232:235]\n"
    "ds_read_b64_tr_b16 v[144:145], v244 offset:0\n"
    "ds_read_b64_tr_b16 v[146:147], v244 offset:16384\n"
    "v_mfma_f32_16x16x32_bf16 v[176:179], v[148:151], v[108:111], v[176:179]\n"
    "v_mfma_f32_16x16x32_bf16 v[180:183], v[148:151], v[112:115], v[180:183]\n"
    "v_mfma_f32_16x16x32_bf16 v[184:187], v[148:151], v[116:119], v[184:187]\n"
    "ds_read_b64_tr_b16 v[148:149], v245 offset:0\n"
    "ds_read_b64_tr_b16 v[150:151], v245 offset:16384\n"
    "v_mfma_f32_16x16x32_bf16 v[176:179], v[152:155], v[120:123], v[176:179]\n"
    "v_mfma_f32_16x16x32_bf16 v[180:183], v[152:155], v[124:127], v[180:183]\n"
    "v_lshlrev_b32_e32 v241, 4, v241\n"
    "v_mfma_f32_16x16x32_bf16 v[184:187], v[152:155], v[128:131], v[184:187]\n"
    "ds_read_b64_tr_b16 v[152:153], v244 offset:512\n"
    "ds_read_b64_tr_b16 v[154:155], v244 offset:16896\n"
    "v_add_u32_e32 v242, s88, v241\n"
    "v_mfma_f32_16x16x32_bf16 v[176:179], v[156:159], v[132:135], v[176:179]\n"
    "v_add_u32_e32 v243, s89, v241\n"
    "v_mfma_f32_16x16x32_bf16 v[180:183], v[156:159], v[136:139], v[180:183]\n"
    "v_mfma_f32_16x16x32_bf16 v[184:187], v[156:159], v[140:143], v[184:187]\n"
    "ds_read_b64_tr_b16 v[156:157], v245 offset:512\n"
    "ds_read_b64_tr_b16 v[158:159], v245 offset:16896\n"
    "s_waitcnt lgkmcnt(8)\n"
    "v_mfma_f32_16x16x32_bf16 v[188:191], v[160:163], v[96:99], v[224:227]\n"
    "v_mfma_f32_16x16x32_bf16 v[188:191], v[164:167], v[108:111], v[188:191]\n"
    "v_exp_f32_e32 v248, v176\n"
    "v_exp_f32_e32 v249, v177\n"
    "v_exp_f32_e32 v250, v178\n"
    "v_mfma_f32_16x16x32_bf16 v[188:191], v[168:171], v[120:123], v[188:191]\n"
    "v_cvt_pk_bf16_f32 v200, v248, v249\n"
    "v_exp_f32_e32 v251, v179\n"
    "v_mfma_f32_16x16x32_bf16 v[188:191], v[172:175], v[132:135], v[188:191]\n"
    "v_exp_f32_e32 v248, v180\n"
    "v_cvt_pk_bf16_f32 v201, v250, v251\n"
    "v_mfma_f32_16x16x32_bf16 v[192:195], v[160:163], v[100:103], v[228:231]\n"
    "v_exp_f32_e32 v249, v181\n"
    "v_exp_f32_e32 v250, v182\n"
    "v_mfma_f32_16x16x32_bf16 v[192:195], v[164:167], v[112:115], v[192:195]\n"
    "v_cvt_pk_bf16_f32 v204, v248, v249\n"
    "v_exp_f32_e32 v251, v183\n"
    "v_mfma_f32_16x16x32_bf16 v[192:195], v[168:171], v[124:127], v[192:195]\n"
    "v_exp_f32_e32 v248, v188\n"
    "v_cvt_pk_bf16_f32 v205, v250, v251\n"
    "v_mfma_f32_16x16x32_bf16 v[192:195], v[172:175], v[136:139], v[192:195]\n"
    "v_exp_f32_e32 v249, v189\n"
    "v_exp_f32_e32 v250, v190\n"
    "v_mfma_f32_16x16x32_bf16 v[196:199], v[160:163], v[104:107], v[232:235]\n"
    "ds_read_b64_tr_b16 v[160:161], v244 offset:4096\n"
    "ds_read_b64_tr_b16 v[162:163], v244 offset:20480\n"
    "v_cvt_pk_bf16_f32 v202, v248, v249\n"
    "v_exp_f32_e32 v251, v191\n"
    "v_mfma_f32_16x16x32_bf16 v[196:199], v[164:167], v[116:119], v[196:199]\n"
    "ds_read_b64_tr_b16 v[164:165], v245 offset:4096\n"
    "ds_read_b64_tr_b16 v[166:167], v245 offset:20480\n"
    "v_or3_b32 v255, v200, v201, v202\n"
    "v_cvt_pk_bf16_f32 v203, v250, v251\n"
    "v_mfma_f32_16x16x32_bf16 v[196:199], v[168:171], v[128:131], v[196:199]\n"
    "ds_read_b64_tr_b16 v[168:169], v244 offset:4608\n"
    "ds_read_b64_tr_b16 v[170:171], v244 offset:20992\n"
    "v_bitop3_b32 v255, v255, s63, v203 bitop3:0xc8\n"
    "v_cmp_ne_u32_e32 vcc, 0, v255\n"
    "v_mfma_f32_16x16x32_bf16 v[196:199], v[172:175], v[140:143], v[196:199]\n"
    "s_cbranch_vccnz .Lst15_0_%=\n"
    ".Lbk15_0_%=:\n"
    "ds_read_b64_tr_b16 v[172:173], v245 offset:4608\n"
    "ds_read_b64_tr_b16 v[174:175], v245 offset:20992\n"
    "v_exp_f32_e32 v248, v192\n"
    "v_exp_f32_e32 v249, v193\n"
    "s_waitcnt lgkmcnt(8)\n"
    "v_mfma_f32_16x16x32_bf16 v[0:3], v[144:147], v[200:203], v[0:3]\n"
    "v_exp_f32_e32 v250, v194\n"
    "v_cvt_pk_bf16_f32 v206, v248, v249\n"
    "v_mfma_f32_16x16x32_bf16 v[12:15], v[148:151], v[200:203], v[12:15]\n"
    "v_exp_f32_e32 v251, v195\n"
    "v_or3_b32 v255, v204, v205, v206\n"
    "v_mfma_f32_16x16x32_bf16 v[24:27], v[152:155], v[200:203], v[24:27]\n"
    "v_cvt_pk_bf16_f32 v207, v250, v251\n"
    "v_bitop3_b32 v255, v255, s63, v207 bitop3:0xc8\n"
    "v_mfma_f32_16x16x32_bf16 v[36:39], v[156:159], v[200:203], v[36:39]\n"
    "v_cmp_ne_u32_e32 vcc, 0, v255\n"
    "v_mfma_f32_16x16x32_bf16 v[212:215], v[236:239], v[200:203], v[212:215]\n"
    "s_cbranch_vccnz .Lst15_1_%=\n"
    ".Lbk15_1_%=:\n"
    "v_exp_f32_e32 v248, v184\n"
    "v_exp_f32_e32 v249, v185\n"
    "v_mfma_f32_16x16x32_bf16 v[4:7], v[144:147], v[204:207], v[4:7]\n"
    "v_exp_f32_e32 v250, v186\n"
    "v_cvt_pk_bf16_f32 v208, v248, v249\n"
    "v_mfma_f32_16x16x32_bf16 v[16:19], v[148:151], v[204:207], v[16:19]\n"
    "v_exp_f32_e32 v251, v187\n"
    "v_mfma_f32_16x16x32_bf16 v[28:31], v[152:155], v[204:207], v[28:31]\n"
    "v_exp_f32_e32 v248, v196\n"
    "v_cvt_pk_bf16_f32 v209, v250, v251\n"
    "v_mfma_f32_16x16x32_bf16 v[40:43], v[156:159], v[204:207], v[40:43]\n"
    "v_exp_f32_e32 v249, v197\n"
    "v_exp_f32_e32 v250, v198\n"
    "v_mfma_f32_16x16x32_bf16 v[216:219], v[236:239], v[204:207], v[216:219]\n"
    "v_cvt_pk_bf16_f32 v210, v248, v249\n"
    "s_waitcnt lgkmcnt(0)\n"
    "v_mfma_f32_16x16x32_bf16 v[48:51], v[160:163], v[200:203], v[48:51]\n"
    "v_exp_f32_e32 v251, v199\n"
    "v_or3_b32 v255, v208, v209, v210\n"
    "v_mfma_f32_16x16x32_bf16 v[60:63], v[164:167], v[200:203], v[60:63]\n"
    "v_cvt_pk_bf16_f32 v211, v250, v251\n"
    "v_bitop3_b32 v255, v255, s63, v211 bitop3:0xc8\n"
    "v_mfma_f32_16x16x32_bf16 v[72:75], v[168:171], v[200:203], v[72:75]\n"
    "v_cmp_ne_u32_e32 vcc, 0, v255\n"
    "v_mfma_f32_16x16x32_bf16 v[84:87], v[172:175], v[200:203], v[84:87]\n"
    "s_cbranch_vccnz .Lst15_2_%=\n"
    ".Lbk15_2_%=:\n"
    "s_barrier\n"
    "v_mfma_f32_16x16x32_bf16 v[8:11], v[144:147], v[208:211], v[8:11]\n"
    "v_mfma_f32_16x16x32_bf16 v[20:23], v[148:151], v[208:211], v[20:23]\n"
    "v_mfma_f32_16x16x32_bf16 v[32:35], v[152:155], v[208:211], v[32:35]\n"
    "v_rcp_f32_e32 v252, v212\n"
    "s_nop 0\n"
    "v_mul_f32_e32 v0, v252, v0\n"
    "v_mul_f32_e32 v1, v252, v1\n"
    "v_mul_f32_e32 v2, v252, v2\n"
    "v_mul_f32_e32 v3, v252, v3\n"
    "v_mul_f32_e32 v12, v252, v12\n"
    "v_mfma_f32_16x16x32_bf16 v[44:47], v[156:159], v[208:211], v[44:47]\n"
    "v_mul_f32_e32 v13, v252, v13\n"
    "v_mul_f32_e32 v14, v252, v14\n"
    "v_mul_f32_e32 v15, v252, v15\n"
    "v_cvt_pk_bf16_f32 v0, v0, v1\n"
    "v_cvt_pk_bf16_f32 v1, v2, v3\n"
    "v_cvt_pk_bf16_f32 v2, v12, v13\n"
    "v_mfma_f32_16x16x32_bf16 v[52:55], v[160:163], v[204:207], v[52:55]\n"
    "v_cvt_pk_bf16_f32 v3, v14, v15\n"
    "ds_write_b128 v242, v[0:3] offset:0\n"
    "s_mov_b32 s60, 0\n"
    "buffer_store_dwordx4 v[0:3], v243, s[84:87], s60 offen offset:0 sc1\n"
    "v_mul_f32_e32 v24, v252, v24\n"
    "v_mul_f32_e32 v25, v252, v25\n"
    "v_mul_f32_e32 v26, v252, v26\n"
    "v_mfma_f32_16x16x32_bf16 v[64:67], v[164:167], v[204:207], v[64:67]\n"
    "v_mul_f32_e32 v27, v252, v27\n"
    "v_mul_f32_e32 v36, v252, v36\n"
    "v_mul_f32_e32 v37, v252, v37\n"
    "v_mul_f32_e32 v38, v252, v38\n"
    "v_mul_f32_e32 v39, v252, v39\n"
    "v_mfma_f32_16x16x32_bf16 v[76:79], v[168:171], v[204:207], v[76:79]\n"
    "v_cvt_pk_bf16_f32 v24, v24, v25\n"
    "v_cvt_pk_bf16_f32 v25, v26, v27\n"
    "v_cvt_pk_bf16_f32 v26, v36, v37\n"
    "v_cvt_pk_bf16_f32 v27, v38, v39\n"
    "ds_write_b128 v242, v[24:27] offset:12288\n"
    "s_mov_b32 s60, 12288\n"
    "buffer_store_dwordx4 v[24:27], v243, s[84:87], s60 offen offset:0 sc1\n"
    "v_mfma_f32_16x16x32_bf16 v[88:91], v[172:175], v[204:207], v[88:91]\n"
    "v_mul_f32_e32 v48, v252, v48\n"
    "v_mul_f32_e32 v49, v252, v49\n"
    "v_mul_f32_e32 v50, v252, v50\n"
    "v_mul_f32_e32 v51, v252, v51\n"
    "v_mul_f32_e32 v60, v252, v60\n"
    "v_mul_f32_e32 v61, v252, v61\n"
    "v_mfma_f32_16x16x32_bf16 v[56:59], v[160:163], v[208:211], v[56:59]\n"
    "v_mul_f32_e32 v62, v252, v62\n"
    "v_mul_f32_e32 v63, v252, v63\n"
    "v_cvt_pk_bf16_f32 v48, v48, v49\n"
    "v_cvt_pk_bf16_f32 v49, v50, v51\n"
    "v_cvt_pk_bf16_f32 v50, v60, v61\n"
    "v_mfma_f32_16x16x32_bf16 v[68:71], v[164:167], v[208:211], v[68:71]\n"
    "v_cvt_pk_bf16_f32 v51, v62, v63\n"
    "ds_write_b128 v242, v[48:51] offset:24576\n"
    "s_mov_b32 s60, 24576\n"
    "buffer_store_dwordx4 v[48:51], v243, s[84:87], s60 offen offset:0 sc1\n"
    "v_mul_f32_e32 v72, v252, v72\n"
    "v_mul_f32_e32 v73, v252, v73\n"
    "v_mul_f32_e32 v74, v252, v74\n"
    "v_mfma_f32_16x16x32_bf16 v[80:83], v[168:171], v[208:211], v[80:83]\n"
    "v_mul_f32_e32 v75, v252, v75\n"
    "v_mul_f32_e32 v84, v252, v84\n"
    "v_mul_f32_e32 v85, v252, v85\n"
    "v_mul_f32_e32 v86, v252, v86\n"
    "v_mul_f32_e32 v87, v252, v87\n"
    "v_cvt_pk_bf16_f32 v72, v72, v73\n"
    "v_mfma_f32_16x16x32_bf16 v[92:95], v[172:175], v[208:211], v[92:95]\n"
    "v_cvt_pk_bf16_f32 v73, v74, v75\n"
    "v_cvt_pk_bf16_f32 v74, v84, v85\n"
    "v_cvt_pk_bf16_f32 v75, v86, v87\n"
    "ds_write_b128 v242, v[72:75] offset:36864\n"
    "s_mov_b32 s60, 36864\n"
    "buffer_store_dwordx4 v[72:75], v243, s[84:87], s60 offen offset:0 sc1\n"
    "v_mfma_f32_16x16x32_bf16 v[220:223], v[236:239], v[208:211], v[220:223]\n"
    "v_rcp_f32_e32 v253, v216\n"
    "s_nop 0\n"
    "v_mul_f32_e32 v4, v253, v4\n"
    "v_mul_f32_e32 v5, v253, v5\n"
    "v_mul_f32_e32 v6, v253, v6\n"
    "v_mul_f32_e32 v7, v253, v7\n"
    "v_mul_f32_e32 v16, v253, v16\n"
    "v_mul_f32_e32 v17, v253, v17\n"
    "v_mul_f32_e32 v18, v253, v18\n"
    "v_mul_f32_e32 v19, v253, v19\n"
    "v_cvt_pk_bf16_f32 v4, v4, v5\n"
    "v_cvt_pk_bf16_f32 v5, v6, v7\n"
    "v_cvt_pk_bf16_f32 v6, v16, v17\n"
    "v_cvt_pk_bf16_f32 v7, v18, v19\n"
    "ds_write_b128 v242, v[4:7] offset:1024\n"
    "s_mov_b32 s60, 0\n"
    "buffer_store_dwordx4 v[4:7], v243, s[84:87], s60 offen offset:1024 sc1\n"
    "v_mul_f32_e32 v28, v253, v28\n"
    "v_mul_f32_e32 v29, v253, v29\n"
    "v_mul_f32_e32 v30, v253, v30\n"
    "v_mul_f32_e32 v31, v253, v31\n"
    "v_mul_f32_e32 v40, v253, v40\n"
    "v_mul_f32_e32 v41, v253, v41\n"
    "v_mul_f32_e32 v42, v253, v42\n"
    "v_mul_f32_e32 v43, v253, v43\n"
    "v_cvt_pk_bf16_f32 v28, v28, v29\n"
    "v_cvt_pk_bf16_f32 v29, v30, v31\n"
    "v_cvt_pk_bf16_f32 v30, v40, v41\n"
    "v_cvt_pk_bf16_f32 v31, v42, v43\n"
    "ds_write_b128 v242, v[28:31] offset:13312\n"
    "s_mov_b32 s60, 12288\n"
    "buffer_store_dwordx4 v[28:31], v243, s[84:87], s60 offen offset:1024 sc1\n"
    "v_mul_f32_e32 v52, v253, v52\n"
    "v_mul_f32_e32 v53, v253, v53\n"
    "v_mul_f32_e32 v54, v253, v54\n"
    "v_mul_f32_e32 v55, v253, v55\n"
    "v_mul_f32_e32 v64, v253, v64\n"
    "v_mul_f32_e32 v65, v253, v65\n"
    "v_mul_f32_e32 v66, v253, v66\n"
    "v_mul_f32_e32 v67, v253, v67\n"
    "v_cvt_pk_bf16_f32 v52, v52, v53\n"
    "v_cvt_pk_bf16_f32 v53, v54, v55\n"
    "v_cvt_pk_bf16_f32 v54, v64, v65\n"
    "v_cvt_pk_bf16_f32 v55, v66, v67\n"
    "ds_write_b128 v242, v[52:55] offset:25600\n"
    "s_mov_b32 s60, 24576\n"
    "buffer_store_dwordx4 v[52:55], v243, s[84:87], s60 offen offset:1024 sc1\n"
    "v_mul_f32_e32 v76, v253, v76\n"
    "v_mul_f32_e32 v77, v253, v77\n"
    "v_mul_f32_e32 v78, v253, v78\n"
    "v_mul_f32_e32 v79, v253, v79\n"
    "v_mul_f32_e32 v88, v253, v88\n"
    "v_mul_f32_e32 v89, v253, v89\n"
    "v_mul_f32_e32 v90, v253, v90\n"
    "v_mul_f32_e32 v91, v253, v91\n"
    "v_cvt_pk_bf16_f32 v76, v76, v77\n"
    "v_cvt_pk_bf16_f32 v77, v78, v79\n"
    "v_cvt_pk_bf16_f32 v78, v88, v89\n"
    "v_cvt_pk_bf16_f32 v79, v90, v91\n"
    "ds_write_b128 v242, v[76:79] offset:37888\n"
    "s_mov_b32 s60, 36864\n"
    "buffer_store_dwordx4 v[76:79], v243, s[84:87], s60 offen offset:1024 sc1\n"
    "v_rcp_f32_e32 v254, v220\n"
    "s_nop 0\n"
    "v_mul_f32_e32 v8, v254, v8\n"
    "v_mul_f32_e32 v9, v254, v9\n"
    "v_mul_f32_e32 v10, v254, v10\n"
    "v_mul_f32_e32 v11, v254, v11\n"
    "v_mul_f32_e32 v20, v254, v20\n"
    "v_mul_f32_e32 v21, v254, v21\n"
    "v_mul_f32_e32 v22, v254, v22\n"
    "v_mul_f32_e32 v23, v254, v23\n"
    "v_cvt_pk_bf16_f32 v8, v8, v9\n"
    "v_cvt_pk_bf16_f32 v9, v10, v11\n"
    "v_cvt_pk_bf16_f32 v10, v20, v21\n"
    "v_cvt_pk_bf16_f32 v11, v22, v23\n"
    "ds_write_b128 v242, v[8:11] offset:2048\n"
    "s_mov_b32 s60, 0\n"
    "buffer_store_dwordx4 v[8:11], v243, s[84:87], s60 offen offset:2048 sc1\n"
    "v_mul_f32_e32 v32, v254, v32\n"
    "v_mul_f32_e32 v33, v254, v33\n"
    "v_mul_f32_e32 v34, v254, v34\n"
    "v_mul_f32_e32 v35, v254, v35\n"
    "v_mul_f32_e32 v44, v254, v44\n"
    "v_mul_f32_e32 v45, v254, v45\n"
    "v_mul_f32_e32 v46, v254, v46\n"
    "v_mul_f32_e32 v47, v254, v47\n"
    "v_cvt_pk_bf16_f32 v32, v32, v33\n"
    "v_cvt_pk_bf16_f32 v33, v34, v35\n"
    "v_cvt_pk_bf16_f32 v34, v44, v45\n"
    "v_cvt_pk_bf16_f32 v35, v46, v47\n"
    "ds_write_b128 v242, v[32:35] offset:14336\n"
    "s_mov_b32 s60, 12288\n"
    "buffer_store_dwordx4 v[32:35], v243, s[84:87], s60 offen offset:2048 sc1\n"
    "v_mul_f32_e32 v56, v254, v56\n"
    "v_mul_f32_e32 v57, v254, v57\n"
    "v_mul_f32_e32 v58, v254, v58\n"
    "v_mul_f32_e32 v59, v254, v59\n"
    "v_mul_f32_e32 v68, v254, v68\n"
    "v_mul_f32_e32 v69, v254, v69\n"
    "v_mul_f32_e32 v70, v254, v70\n"
    "v_mul_f32_e32 v71, v254, v71\n"
    "v_cvt_pk_bf16_f32 v56, v56, v57\n"
    "v_cvt_pk_bf16_f32 v57, v58, v59\n"
    "v_cvt_pk_bf16_f32 v58, v68, v69\n"
    "v_cvt_pk_bf16_f32 v59, v70, v71\n"
    "ds_write_b128 v242, v[56:59] offset:26624\n"
    "s_mov_b32 s60, 24576\n"
    "buffer_store_dwordx4 v[56:59], v243, s[84:87], s60 offen offset:2048 sc1\n"
    "v_mul_f32_e32 v80, v254, v80\n"
    "v_mul_f32_e32 v81, v254, v81\n"
    "v_mul_f32_e32 v82, v254, v82\n"
    "v_mul_f32_e32 v83, v254, v83\n"
    "v_mul_f32_e32 v92, v254, v92\n"
    "v_mul_f32_e32 v93, v254, v93\n"
    "v_mul_f32_e32 v94, v254, v94\n"
    "v_mul_f32_e32 v95, v254, v95\n"
    "v_cvt_pk_bf16_f32 v80, v80, v81\n"
    "v_cvt_pk_bf16_f32 v81, v82, v83\n"
    "v_cvt_pk_bf16_f32 v82, v92, v93\n"
    "v_cvt_pk_bf16_f32 v83, v94, v95\n"
    "ds_write_b128 v242, v[80:83] offset:38912\n"
    "s_mov_b32 s60, 36864\n"
    "buffer_store_dwordx4 v[80:83], v243, s[84:87], s60 offen offset:2048 sc1\n"
    "s_branch .Ldone_%=\n"
    ".Lst1_0_%=:\n"
    "s_getpc_b64 s[72:73]\n"
    "s_add_u32 s72, s72, 12\n"
    "s_addc_u32 s73, s73, 0\n"
    "s_branch .Lrare0_%=\n"
    "s_branch .Lbk1_0_%=\n"
    ".Lst1_1_%=:\n"
    "s_getpc_b64 s[72:73]\n"
    "s_add_u32 s72, s72, 12\n"
    "s_addc_u32 s73, s73, 0\n"
    "s_branch .Lrare1_%=\n"
    "s_branch .Lbk1_1_%=\n"
    ".Lst1_2_%=:\n"
    "s_getpc_b64 s[72:73]\n"
    "s_add_u32 s72, s72, 12\n"
    "s_addc_u32 s73, s73, 0\n"
    "s_branch .Lrare2_%=\n"
    "s_branch .Lbk1_2_%=\n"
    ".Lst2_0_%=:\n"
    "s_getpc_b64 s[72:73]\n"
    "s_add_u32 s72, s72, 12\n"
    "s_addc_u32 s73, s73, 0\n"
    "s_branch .Lrare0_%=\n"
    "s_branch .Lbk2_0_%=\n"
    ".Lst2_1_%=:\n"
    "s_getpc_b64 s[72:73]\n"
    "s_add_u32 s72, s72, 12\n"
    "s_addc_u32 s73, s73, 0\n"
    "s_branch .Lrare1_%=\n"
    "s_branch .Lbk2_1_%=\n"
    ".Lst2_2_%=:\n"
    "s_getpc_b64 s[72:73]\n"
    "s_add_u32 s72, s72, 12\n"
    "s_addc_u32 s73, s73, 0\n"
    "s_branch .Lrare2_%=\n"
    "s_branch .Lbk2_2_%=\n"
    ".Lst3_0_%=:\n"
    "s_getpc_b64 s[72:73]\n"
    "s_add_u32 s72, s72, 12\n"
    "s_addc_u32 s73, s73, 0\n"
    "s_branch .Lrare0_%=\n"
    "s_branch .Lbk3_0_%=\n"
    ".Lst3_1_%=:\n"
    "s_getpc_b64 s[72:73]\n"
    "s_add_u32 s72, s72, 12\n"
    "s_addc_u32 s73, s73, 0\n"
    "s_branch .Lrare1_%=\n"
    "s_branch .Lbk3_1_%=\n"
    ".Lst3_2_%=:\n"
    "s_getpc_b64 s[72:73]\n"
    "s_add_u32 s72, s72, 12\n"
    "s_addc_u32 s73, s73, 0\n"
    "s_branch .Lrare2_%=\n"
    "s_branch .Lbk3_2_%=\n"
    ".Lst4_0_%=:\n"
    "s_getpc_b64 s[72:73]\n"
    "s_add_u32 s72, s72, 12\n"
    "s_addc_u32 s73, s73, 0\n"
    "s_branch .Lrare0_%=\n"
    "s_branch .Lbk4_0_%=\n"
    ".Lst4_1_%=:\n"
    "s_getpc_b64 s[72:73]\n"
    "s_add_u32 s72, s72, 12\n"
    "s_addc_u32 s73, s73, 0\n"
    "s_branch .Lrare1_%=\n"
    "s_branch .Lbk4_1_%=\n"
    ".Lst4_2_%=:\n"
    "s_getpc_b64 s[72:73]\n"
    "s_add_u32 s72, s72, 12\n"
    "s_addc_u32 s73, s73, 0\n"
    "s_branch .Lrare2_%=\n"
    "s_branch .Lbk4_2_%=\n"
    ".Lst5_0_%=:\n"
    "s_getpc_b64 s[72:73]\n"
    "s_add_u32 s72, s72, 12\n"
    "s_addc_u32 s73, s73, 0\n"
    "s_branch .Lrare0_%=\n"
    "s_branch .Lbk5_0_%=\n"
    ".Lst5_1_%=:\n"
    "s_getpc_b64 s[72:73]\n"
    "s_add_u32 s72, s72, 12\n"
    "s_addc_u32 s73, s73, 0\n"
    "s_branch .Lrare1_%=\n"
    "s_branch .Lbk5_1_%=\n"
    ".Lst5_2_%=:\n"
    "s_getpc_b64 s[72:73]\n"
    "s_add_u32 s72, s72, 12\n"
    "s_addc_u32 s73, s73, 0\n"
    "s_branch .Lrare2_%=\n"
    "s_branch .Lbk5_2_%=\n"
    ".Lst6_0_%=:\n"
    "s_getpc_b64 s[72:73]\n"
    "s_add_u32 s72, s72, 12\n"
    "s_addc_u32 s73, s73, 0\n"
    "s_branch .Lrare0_%=\n"
    "s_branch .Lbk6_0_%=\n"
    ".Lst6_1_%=:\n"
    "s_getpc_b64 s[72:73]\n"
    "s_add_u32 s72, s72, 12\n"
    "s_addc_u32 s73, s73, 0\n"
    "s_branch .Lrare1_%=\n"
    "s_branch .Lbk6_1_%=\n"
    ".Lst6_2_%=:\n"
    "s_getpc_b64 s[72:73]\n"
    "s_add_u32 s72, s72, 12\n"
    "s_addc_u32 s73, s73, 0\n"
    "s_branch .Lrare2_%=\n"
    "s_branch .Lbk6_2_%=\n"
    ".Lst7_0_%=:\n"
    "s_getpc_b64 s[72:73]\n"
    "s_add_u32 s72, s72, 12\n"
    "s_addc_u32 s73, s73, 0\n"
    "s_branch .Lrare0_%=\n"
    "s_branch .Lbk7_0_%=\n"
    ".Lst7_1_%=:\n"
    "s_getpc_b64 s[72:73]\n"
    "s_add_u32 s72, s72, 12\n"
    "s_addc_u32 s73, s73, 0\n"
    "s_branch .Lrare1_%=\n"
    "s_branch .Lbk7_1_%=\n"
    ".Lst7_2_%=:\n"
    "s_getpc_b64 s[72:73]\n"
    "s_add_u32 s72, s72, 12\n"
    "s_addc_u32 s73, s73, 0\n"
    "s_branch .Lrare2_%=\n"
    "s_branch .Lbk7_2_%=\n"
    ".Lst8_0_%=:\n"
    "s_getpc_b64 s[72:73]\n"
    "s_add_u32 s72, s72, 12\n"
    "s_addc_u32 s73, s73, 0\n"
    "s_branch .Lrare0_%=\n"
    "s_branch .Lbk8_0_%=\n"
    ".Lst8_1_%=:\n"
    "s_getpc_b64 s[72:73]\n"
    "s_add_u32 s72, s72, 12\n"
    "s_addc_u32 s73, s73, 0\n"
    "s_branch .Lrare1_%=\n"
    "s_branch .Lbk8_1_%=\n"
    ".Lst8_2_%=:\n"
    "s_getpc_b64 s[72:73]\n"
    "s_add_u32 s72, s72, 12\n"
    "s_addc_u32 s73, s73, 0\n"
    "s_branch .Lrare2_%=\n"
    "s_branch .Lbk8_2_%=\n"
    ".Lst9_0_%=:\n"
    "s_getpc_b64 s[72:73]\n"
    "s_add_u32 s72, s72, 12\n"
    "s_addc_u32 s73, s73, 0\n"
    "s_branch .Lrare0_%=\n"
    "s_branch .Lbk9_0_%=\n"
    ".Lst9_1_%=:\n"
    "s_getpc_b64 s[72:73]\n"
    "s_add_u32 s72, s72, 12\n"
    "s_addc_u32 s73, s73, 0\n"
    "s_branch .Lrare1_%=\n"
    "s_branch .Lbk9_1_%=\n"
    ".Lst9_2_%=:\n"
    "s_getpc_b64 s[72:73]\n"
    "s_add_u32 s72, s72, 12\n"
    "s_addc_u32 s73, s73, 0\n"
    "s_branch .Lrare2_%=\n"
    "s_branch .Lbk9_2_%=\n"
    ".Lst10_0_%=:\n"
    "s_getpc_b64 s[72:73]\n"
    "s_add_u32 s72, s72, 12\n"
    "s_addc_u32 s73, s73, 0\n"
    "s_branch .Lrare0_%=\n"
    "s_branch .Lbk10_0_%=\n"
    ".Lst10_1_%=:\n"
    "s_getpc_b64 s[72:73]\n"
    "s_add_u32 s72, s72, 12\n"
    "s_addc_u32 s73, s73, 0\n"
    "s_branch .Lrare1_%=\n"
    "s_branch .Lbk10_1_%=\n"
    ".Lst10_2_%=:\n"
    "s_getpc_b64 s[72:73]\n"
    "s_add_u32 s72, s72, 12\n"
    "s_addc_u32 s73, s73, 0\n"
    "s_branch .Lrare2_%=\n"
    "s_branch .Lbk10_2_%=\n"
    ".Lst11_0_%=:\n"
    "s_getpc_b64 s[72:73]\n"
    "s_add_u32 s72, s72, 12\n"
    "s_addc_u32 s73, s73, 0\n"
    "s_branch .Lrare0_%=\n"
    "s_branch .Lbk11_0_%=\n"
    ".Lst11_1_%=:\n"
    "s_getpc_b64 s[72:73]\n"
    "s_add_u32 s72, s72, 12\n"
    "s_addc_u32 s73, s73, 0\n"
    "s_branch .Lrare1_%=\n"
    "s_branch .Lbk11_1_%=\n"
    ".Lst11_2_%=:\n"
    "s_getpc_b64 s[72:73]\n"
    "s_add_u32 s72, s72, 12\n"
    "s_addc_u32 s73, s73, 0\n"
    "s_branch .Lrare2_%=\n"
    "s_branch .Lbk11_2_%=\n"
    ".Lst12_0_%=:\n"
    "s_getpc_b64 s[72:73]\n"
    "s_add_u32 s72, s72, 12\n"
    "s_addc_u32 s73, s73, 0\n"
    "s_branch .Lrare0_%=\n"
    "s_branch .Lbk12_0_%=\n"
    ".Lst12_1_%=:\n"
    "s_getpc_b64 s[72:73]\n"
    "s_add_u32 s72, s72, 12\n"
    "s_addc_u32 s73, s73, 0\n"
    "s_branch .Lrare1_%=\n"
    "s_branch .Lbk12_1_%=\n"
    ".Lst12_2_%=:\n"
    "s_getpc_b64 s[72:73]\n"
    "s_add_u32 s72, s72, 12\n"
    "s_addc_u32 s73, s73, 0\n"
    "s_branch .Lrare2_%=\n"
    "s_branch .Lbk12_2_%=\n"
    ".Lst13_0_%=:\n"
    "s_getpc_b64 s[72:73]\n"
    "s_add_u32 s72, s72, 12\n"
    "s_addc_u32 s73, s73, 0\n"
    "s_branch .Lrare0_%=\n"
    "s_branch .Lbk13_0_%=\n"
    ".Lst13_1_%=:\n"
    "s_getpc_b64 s[72:73]\n"
    "s_add_u32 s72, s72, 12\n"
    "s_addc_u32 s73, s73, 0\n"
    "s_branch .Lrare1_%=\n"
    "s_branch .Lbk13_1_%=\n"
    ".Lst13_2_%=:\n"
    "s_getpc_b64 s[72:73]\n"
    "s_add_u32 s72, s72, 12\n"
    "s_addc_u32 s73, s73, 0\n"
    "s_branch .Lrare2_%=\n"
    "s_branch .Lbk13_2_%=\n"
    ".Lst14_0_%=:\n"
    "s_getpc_b64 s[72:73]\n"
    "s_add_u32 s72, s72, 12\n"
    "s_addc_u32 s73, s73, 0\n"
    "s_branch .Lrare0_%=\n"
    "s_branch .Lbk14_0_%=\n"
    ".Lst14_1_%=:\n"
    "s_getpc_b64 s[72:73]\n"
    "s_add_u32 s72, s72, 12\n"
    "s_addc_u32 s73, s73, 0\n"
    "s_branch .Lrare1_%=\n"
    "s_branch .Lbk14_1_%=\n"
    ".Lst14_2_%=:\n"
    "s_getpc_b64 s[72:73]\n"
    "s_add_u32 s72, s72, 12\n"
    "s_addc_u32 s73, s73, 0\n"
    "s_branch .Lrare2_%=\n"
    "s_branch .Lbk14_2_%=\n"
    ".Lst15_0_%=:\n"
    "s_getpc_b64 s[72:73]\n"
    "s_add_u32 s72, s72, 12\n"
    "s_addc_u32 s73, s73, 0\n"
    "s_branch .Lrare0_%=\n"
    "s_branch .Lbk15_0_%=\n"
    ".Lst15_1_%=:\n"
    "s_getpc_b64 s[72:73]\n"
    "s_add_u32 s72, s72, 12\n"
    "s_addc_u32 s73, s73, 0\n"
    "s_branch .Lrare1_%=\n"
    "s_branch .Lbk15_1_%=\n"
    ".Lst15_2_%=:\n"
    "s_getpc_b64 s[72:73]\n"
    "s_add_u32 s72, s72, 12\n"
    "s_addc_u32 s73, s73, 0\n"
    "s_branch .Lrare2_%=\n"
    "s_branch .Lbk15_2_%=\n"
    ".Lrare0_%=:\n"
    "s_nop 7\n"
    "v_max3_f32 v252, v176, v177, v178\n"
    "v_max3_f32 v253, v179, v188, v189\n"
    "v_max3_f32 v252, v252, v190, v191\n"
    "v_max_f32_e32 v252, v252, v253\n"
    "v_mov_b32_e32 v253, v252\n"
    "s_nop 1\n"
    "v_permlane16_swap_b32 v252, v253\n"
    "s_nop 1\n"
    "v_max_f32_e32 v252, v252, v253\n"
    "v_mov_b32_e32 v253, v252\n"
    "s_nop 1\n"
    "v_permlane32_swap_b32 v252, v253\n"
    "s_nop 1\n"
    "v_max_f32_e32 v252, v252, v253\n"
    "v_add_f32_e32 v252, 0x40e00000, v252\n"
    "v_max_f32_e32 v252, 0, v252\n"
    "v_exp_f32_e64 v253, -v252\n"
    "v_sub_f32_e32 v224, v224, v252\n"
    "v_sub_f32_e32 v225, v225, v252\n"
    "v_sub_f32_e32 v226, v226, v252\n"
    "v_sub_f32_e32 v227, v227, v252\n"
    "v_sub_f32_e32 v176, v176, v252\n"
    "v_sub_f32_e32 v177, v177, v252\n"
    "v_sub_f32_e32 v178, v178, v252\n"
    "v_sub_f32_e32 v179, v179, v252\n"
    "v_sub_f32_e32 v188, v188, v252\n"
    "v_sub_f32_e32 v189, v189, v252\n"
    "v_sub_f32_e32 v190, v190, v252\n"
    "v_sub_f32_e32 v191, v191, v252\n"
    "v_mul_f32_e32 v212, v212, v253\n"
    "v_mul_f32_e32 v213, v213, v253\n"
    "v_mul_f32_e32 v214, v214, v253\n"
    "v_mul_f32_e32 v215, v215, v253\n"
    "v_mul_f32_e32 v0, v0, v253\n"
    "v_mul_f32_e32 v1, v1, v253\n"
    "v_mul_f32_e32 v2, v2, v253\n"
    "v_mul_f32_e32 v3, v3, v253\n"
    "v_mul_f32_e32 v12, v12, v253\n"
    "v_mul_f32_e32 v13, v13, v253\n"
    "v_mul_f32_e32 v14, v14, v253\n"
    "v_mul_f32_e32 v15, v15, v253\n"
    "v_mul_f32_e32 v24, v24, v253\n"
    "v_mul_f32_e32 v25, v25, v253\n"
    "v_mul_f32_e32 v26, v26, v253\n"
    "v_mul_f32_e32 v27, v27, v253\n"
    "v_mul_f32_e32 v36, v36, v253\n"
    "v_mul_f32_e32 v37, v37, v253\n"
    "v_mul_f32_e32 v38, v38, v253\n"
    "v_mul_f32_e32 v39, v39, v253\n"
    "v_mul_f32_e32 v48, v48, v253\n"
    "v_mul_f32_e32 v49, v49, v253\n"
    "v_mul_f32_e32 v50, v50, v253\n"
    "v_mul_f32_e32 v51, v51, v253\n"
    "v_mul_f32_e32 v60, v60, v253\n"
    "v_mul_f32_e32 v61, v61, v253\n"
    "v_mul_f32_e32 v62, v62, v253\n"
    "v_mul_f32_e32 v63, v63, v253\n"
    "v_mul_f32_e32 v72, v72, v253\n"
    "v_mul_f32_e32 v73, v73, v253\n"
    "v_mul_f32_e32 v74, v74, v253\n"
    "v_mul_f32_e32 v75, v75, v253\n"
    "v_mul_f32_e32 v84, v84, v253\n"
    "v_mul_f32_e32 v85, v85, v253\n"
    "v_mul_f32_e32 v86, v86, v253\n"
    "v_mul_f32_e32 v87, v87, v253\n"
    "v_exp_f32_e32 v252, v176\n"
    "v_exp_f32_e32 v254, v177\n"
    "s_nop 0\n"
    "v_cvt_pk_bf16_f32 v200, v252, v254\n"
    "v_exp_f32_e32 v252, v178\n"
    "v_exp_f32_e32 v254, v179\n"
    "s_nop 0\n"
    "v_cvt_pk_bf16_f32 v201, v252, v254\n"
    "v_exp_f32_e32 v252, v188\n"
    "v_exp_f32_e32 v254, v189\n"
    "s_nop 0\n"
    "v_cvt_pk_bf16_f32 v202, v252, v254\n"
    "v_exp_f32_e32 v252, v190\n"
    "v_exp_f32_e32 v254, v191\n"
    "s_nop 0\n"
    "v_cvt_pk_bf16_f32 v203, v252, v254\n"
    "s_nop 3\n"
    "s_setpc_b64 s[72:73]\n"
    ".Lrare1_%=:\n"
    "s_nop 7\n"
    "v_max3_f32 v252, v180, v181, v182\n"
    "v_max3_f32 v253, v183, v192, v193\n"
    "v_max3_f32 v252, v252, v194, v195\n"
    "v_max_f32_e32 v252, v252, v253\n"
    "v_mov_b32_e32 v253, v252\n"
    "s_nop 1\n"
    "v_permlane16_swap_b32 v252, v253\n"
    "s_nop 1\n"
    "v_max_f32_e32 v252, v252, v253\n"
    "v_mov_b32_e32 v253, v252\n"
    "s_nop 1\n"
    "v_permlane32_swap_b32 v252, v253\n"
    "s_nop 1\n"
    "v_max_f32_e32 v252, v252, v253\n"
    "v_add_f32_e32 v252, 0x40e00000, v252\n"
    "v_max_f32_e32 v252, 0, v252\n"
    "v_exp_f32_e64 v253, -v252\n"
    "v_sub_f32_e32 v228, v228, v252\n"
    "v_sub_f32_e32 v229, v229, v252\n"
    "v_sub_f32_e32 v230, v230, v252\n"
    "v_sub_f32_e32 v231, v231, v252\n"
    "v_sub_f32_e32 v180, v180, v252\n"
    "v_sub_f32_e32 v181, v181, v252\n"
    "v_sub_f32_e32 v182, v182, v252\n"
    "v_sub_f32_e32 v183, v183, v252\n"
    "v_sub_f32_e32 v192, v192, v252\n"
    "v_sub_f32_e32 v193, v193, v252\n"
    "v_sub_f32_e32 v194, v194, v252\n"
    "v_sub_f32_e32 v195, v195, v252\n"
    "v_mul_f32_e32 v216, v216, v253\n"
    "v_mul_f32_e32 v217, v217, v253\n"
    "v_mul_f32_e32 v218, v218, v253\n"
    "v_mul_f32_e32 v219, v219, v253\n"
    "v_mul_f32_e32 v4, v4, v253\n"
    "v_mul_f32_e32 v5, v5, v253\n"
    "v_mul_f32_e32 v6, v6, v253\n"
    "v_mul_f32_e32 v7, v7, v253\n"
    "v_mul_f32_e32 v16, v16, v253\n"
    "v_mul_f32_e32 v17, v17, v253\n"
    "v_mul_f32_e32 v18, v18, v253\n"
    "v_mul_f32_e32 v19, v19, v253\n"
    "v_mul_f32_e32 v28, v28, v253\n"
    "v_mul_f32_e32 v29, v29, v253\n"
    "v_mul_f32_e32 v30, v30, v253\n"
    "v_mul_f32_e32 v31, v31, v253\n"
    "v_mul_f32_e32 v40, v40, v253\n"
    "v_mul_f32_e32 v41, v41, v253\n"
    "v_mul_f32_e32 v42, v42, v253\n"
    "v_mul_f32_e32 v43, v43, v253\n"
    "v_mul_f32_e32 v52, v52, v253\n"
    "v_mul_f32_e32 v53, v53, v253\n"
    "v_mul_f32_e32 v54, v54, v253\n"
    "v_mul_f32_e32 v55, v55, v253\n"
    "v_mul_f32_e32 v64, v64, v253\n"
    "v_mul_f32_e32 v65, v65, v253\n"
    "v_mul_f32_e32 v66, v66, v253\n"
    "v_mul_f32_e32 v67, v67, v253\n"
    "v_mul_f32_e32 v76, v76, v253\n"
    "v_mul_f32_e32 v77, v77, v253\n"
    "v_mul_f32_e32 v78, v78, v253\n"
    "v_mul_f32_e32 v79, v79, v253\n"
    "v_mul_f32_e32 v88, v88, v253\n"
    "v_mul_f32_e32 v89, v89, v253\n"
    "v_mul_f32_e32 v90, v90, v253\n"
    "v_mul_f32_e32 v91, v91, v253\n"
    "v_exp_f32_e32 v252, v180\n"
    "v_exp_f32_e32 v254, v181\n"
    "s_nop 0\n"
    "v_cvt_pk_bf16_f32 v204, v252, v254\n"
    "v_exp_f32_e32 v252, v182\n"
    "v_exp_f32_e32 v254, v183\n"
    "s_nop 0\n"
    "v_cvt_pk_bf16_f32 v205, v252, v254\n"
    "v_exp_f32_e32 v252, v192\n"
    "v_exp_f32_e32 v254, v193\n"
    "s_nop 0\n"
    "v_cvt_pk_bf16_f32 v206, v252, v254\n"
    "v_exp_f32_e32 v252, v194\n"
    "v_exp_f32_e32 v254, v195\n"
    "s_nop 0\n"
    "v_cvt_pk_bf16_f32 v207, v252, v254\n"
    "s_nop 3\n"
    "s_setpc_b64 s[72:73]\n"
    ".Lrare2_%=:\n"
    "s_nop 7\n"
    "v_max3_f32 v252, v184, v185, v186\n"
    "v_max3_f32 v253, v187, v196, v197\n"
    "v_max3_f32 v252, v252, v198, v199\n"
    "v_max_f32_e32 v252, v252, v253\n"
    "v_mov_b32_e32 v253, v252\n"
    "s_nop 1\n"
    "v_permlane16_swap_b32 v252, v253\n"
    "s_nop 1\n"
    "v_max_f32_e32 v252, v252, v253\n"
    "v_mov_b32_e32 v253, v252\n"
    "s_nop 1\n"
    "v_permlane32_swap_b32 v252, v253\n"
    "s_nop 1\n"
    "v_max_f32_e32 v252, v252, v253\n"
    "v_add_f32_e32 v252, 0x40e00000, v252\n"
    "v_max_f32_e32 v252, 0, v252\n"
    "v_exp_f32_e64 v253, -v252\n"
    "v_sub_f32_e32 v232, v232, v252\n"
    "v_sub_f32_e32 v233, v233, v252\n"
    "v_sub_f32_e32 v234, v234, v252\n"
    "v_sub_f32_e32 v235, v235, v252\n"
    "v_sub_f32_e32 v184, v184, v252\n"
    "v_sub_f32_e32 v185, v185, v252\n"
    "v_sub_f32_e32 v186, v186, v252\n"
    "v_sub_f32_e32 v187, v187, v252\n"
    "v_sub_f32_e32 v196, v196, v252\n"
    "v_sub_f32_e32 v197, v197, v252\n"
    "v_sub_f32_e32 v198, v198, v252\n"
    "v_sub_f32_e32 v199, v199, v252\n"
    "v_mul_f32_e32 v220, v220, v253\n"
    "v_mul_f32_e32 v221, v221, v253\n"
    "v_mul_f32_e32 v222, v222, v253\n"
    "v_mul_f32_e32 v223, v223, v253\n"
    "v_mul_f32_e32 v8, v8, v253\n"
    "v_mul_f32_e32 v9, v9, v253\n"
    "v_mul_f32_e32 v10, v10, v253\n"
    "v_mul_f32_e32 v11, v11, v253\n"
    "v_mul_f32_e32 v20, v20, v253\n"
    "v_mul_f32_e32 v21, v21, v253\n"
    "v_mul_f32_e32 v22, v22, v253\n"
    "v_mul_f32_e32 v23, v23, v253\n"
    "v_mul_f32_e32 v32, v32, v253\n"
    "v_mul_f32_e32 v33, v33, v253\n"
    "v_mul_f32_e32 v34, v34, v253\n"
    "v_mul_f32_e32 v35, v35, v253\n"
    "v_mul_f32_e32 v44, v44, v253\n"
    "v_mul_f32_e32 v45, v45, v253\n"
    "v_mul_f32_e32 v46, v46, v253\n"
    "v_mul_f32_e32 v47, v47, v253\n"
    "v_mul_f32_e32 v56, v56, v253\n"
    "v_mul_f32_e32 v57, v57, v253\n"
    "v_mul_f32_e32 v58, v58, v253\n"
    "v_mul_f32_e32 v59, v59, v253\n"
    "v_mul_f32_e32 v68, v68, v253\n"
    "v_mul_f32_e32 v69, v69, v253\n"
    "v_mul_f32_e32 v70, v70, v253\n"
    "v_mul_f32_e32 v71, v71, v253\n"
    "v_mul_f32_e32 v80, v80, v253\n"
    "v_mul_f32_e32 v81, v81, v253\n"
    "v_mul_f32_e32 v82, v82, v253\n"
    "v_mul_f32_e32 v83, v83, v253\n"
    "v_mul_f32_e32 v92, v92, v253\n"
    "v_mul_f32_e32 v93, v93, v253\n"
    "v_mul_f32_e32 v94, v94, v253\n"
    "v_mul_f32_e32 v95, v95, v253\n"
    "v_exp_f32_e32 v252, v184\n"
    "v_exp_f32_e32 v254, v185\n"
    "s_nop 0\n"
    "v_cvt_pk_bf16_f32 v208, v252, v254\n"
    "v_exp_f32_e32 v252, v186\n"
    "v_exp_f32_e32 v254, v187\n"
    "s_nop 0\n"
    "v_cvt_pk_bf16_f32 v209, v252, v254\n"
    "v_exp_f32_e32 v252, v196\n"
    "v_exp_f32_e32 v254, v197\n"
    "s_nop 0\n"
    "v_cvt_pk_bf16_f32 v210, v252, v254\n"
    "v_exp_f32_e32 v252, v198\n"
    "v_exp_f32_e32 v254, v199\n"
    "s_nop 0\n"
    "v_cvt_pk_bf16_f32 v211, v252, v254\n"
    "s_nop 3\n"
    "s_setpc_b64 s[72:73]\n"
    ".Ldone_%=:\n"
    "s_nop 1\n"
      :
        "+{v[136:143]}"(qa), "+{v135}"(vtch),
 "+{v[240:247]}"(ka), "+{s64}"(wm0), "={s80}"(nonce)
      : "{s[36:39]}"(srdX), "{s[40:43]}"(srdW), "{s[44:47]}"(srdK), "{s[48:51]}"(srdV), "{s[52:55]}"(srdWo), "{s[76:79]}"(srdM),
        "{s56}"(ldsb + 4096u * w), "{s57}"(ldsb + 2048u * w), "{s58}"(ldsb + 3072u * w), "{s59}"(w * (24u * 1280u)),
        "{s65}"(KVROT * 8192u), "{s67}"(w < 4u ? 0u : 1024u), "{s68}"(w < 4u ? 0u : 20480u), "{s69}"(ws0), "{s70}"(ws1), "{s71}"(ws2),
        "{s[84:87]}"(srdO), "{s88}"(ldsb + P3O + 3072u * rg + 4u * P3S * hh), "{s89}"(w < 4u ? 8u * jq * P3S + 3072u * rg : 0x40000000u)
      : "v0", "v1", "v2", "v3", "v4", "v5", "v6", "v7", "v8", "v9", "v10", "v11", "v12", "v13", "v14", "v15", "v16", "v17", "v18", "v19", "v20", "v21", "v22", "v23", "v24", "v25", "v26", "v27", "v28", "v29", "v30", "v31", "v32", "v33", "v34", "v35", "v36", "v37", "v38", "v39", "v40", "v41", "v42", "v43", "v44", "v45", "v46", "v47", "v48", "v49", "v50", "v51", "v52", "v53", "v54", "v55", "v56", "v57", "v58", "v59", "v60", "v61", "v62", "v63", "v64", "v65", "v66", "v67", "v68", "v69", "v70", "v71", "v72", "v73", "v74", "v75", "v76", "v77", "v78", "v79", "v80", "v81", "v82", "v83", "v84", "v85", "v86", "v87", "v88", "v89", "v90", "v91", "v92", "v93", "v94", "v95", "v212", "v213", "v214", "v215", "v216", "v217", "v218", "v219", "v220", "v221", "v222", "v223", "v96", "v97", "v98", "v99",
 "v100",
 "v101", "v102", "v103", "v104", "v105", "v106", "v107", "v108", "v109", "v110", "v111", "v112", "v113", "v114", "v115", "v116", "v117", "v118", "v119", "v120", "v121", "v122", "v123", "v124", "v125", "v126", "v127", "v128", "v129", "v130", "v131", "v132", "v133", "v134", "v144", "v145", "v146", "v147", "v148", "v149", "v150", "v151", "v152", "v153", "v154", "v155", "v156", "v157", "v158", "v159", "v160", "v161", "v162", "v163", "v164", "v165", "v166", "v167", "v168", "v169", "v170", "v171", "v172", "v173", "v174", "v175", "v176", "v177", "v178", "v179", "v180", "v181", "v182", "v183", "v184", "v185", "v186", "v187", "v188", "v189", "v190", "v191", "v192", "v193", "v194", "v195", "v196", "v197", "v198", "v199", "v200", "v201", "v202", "v203", "v204", "v205", "v206", "v207", "v208", "v209", "v210", "v211", "v224", "v225", "v226", "v227", "v228", "v229", "v230", "v231", "v232", "v233", "v234", "v235", "v236", "v237", "v238", "v239", "v248", "v249", "v250", "v251", "v252", "v253", "v254", "v255", "s60", "s61", "s62", "s63", "s72", "s73", "s74", "s75", "vcc", "scc", "memory");
    ln16 = ka[1];
  }
  f32x32 ac0, ac1;
  {
    u32x4 sob, swb;
#pragma unroll
    for (int u = 0; u < 4; ++u) { sob[u] = ((jq + u) & 3u) * (8u * P3S); swb[u] = ((jq + u) & 3u) * 327680u; }
    asm volatile(
    "s_cmp_lt_u32 s63, 4\n"
    "s_cbranch_scc1 .LS_%=\n"
    "v_lshrrev_b32_e32 v121, 4, v241\n"
    "v_add_u32_e32 v106, s37, v241\n"
    "v_and_b32_e32 v122, 3, v121\n"
    "v_lshrrev_b32_e32 v123, 2, v121\n"
    "v_mul_u32_u24_e32 v123, 0x140, v123\n"
    "v_lshl_add_u32 v123, v122, 3, v123\n"
    "v_add_u32_e32 v123, s69, v123\n"
    "v_lshlrev_b32_e32 v105, 1, v121\n"
    "v_and_b32_e32 v105, 32, v105\n"
    "v_xor_b32_e32 v104, s36, v105\n"
    "s_add_u32 s64, s36, 32\n"
    "v_add_u32_e32 v104, v104, v123\n"
    "v_xor_b32_e32 v105, s64, v105\n"
    "v_add_u32_e32 v105, v105, v123\n"
    "v_add_u32_e32 v107, 0xc000, v106\n"
    "v_add_u32_e32 v113, s38, v241\n"
    "v_lshl_add_u32 v114, v122, 10, s39\n"
    "s_setprio 1\n"
    "buffer_load_dword v115, v114, s[48:51], 0 offen sc1\n"
    "s_waitcnt vmcnt(15) lgkmcnt(0)\n"
    "s_barrier\n"
    "ds_read_b128 v[80:83], v106 offset:0\n"
    "ds_read_b128 v[84:87], v106 offset:1024\n"
    "ds_read_b128 v[88:91], v106 offset:2048\n"
    "ds_read_b64_tr_b16 v[60:61], v104 offset:0\n"
    "ds_read_b64_tr_b16 v[62:63], v104 offset:5120\n"
    "ds_read_b64_tr_b16 v[64:65], v105 offset:0\n"
    "ds_read_b64_tr_b16 v[66:67], v105 offset:5120\n"
    "ds_read_b64_tr_b16 v[68:69], v104 offset:64\n"
    "ds_read_b64_tr_b16 v[70:71], v104 offset:5184\n"
    "ds_read_b64_tr_b16 v[72:73], v105 offset:64\n"
    "ds_read_b64_tr_b16 v[74:75], v105 offset:5184\n"
    "ds_read_b64_tr_b16 v[76:77], v104 offset:128\n"
    "ds_read_b64_tr_b16 v[78:79], v104 offset:5248\n"
    "s_waitcnt lgkmcnt(8)\n"
    "v_mfma_f32_16x16x32_bf16 v[0:3], v[60:63], v[80:83], 0\n"
    "v_lshl_add_u32 v119, v121, 2, s88\n"
    "v_cmp_gt_u32_e32 vcc, 4, v121\n"
    "v_mov_b32_e32 v123, 0x40000000\n"
    "v_xor_b32_e32 v120, 0x400, v114\n"
    "ds_read_b128 v[92:95], v106 offset:12288\n"
    "v_mfma_f32_16x16x32_bf16 v[4:7], v[60:63], v[84:87], 0\n"
    "v_cndmask_b32_e32 v119, v123, v119, vcc\n"
    "v_cmp_eq_u32_e32 vcc, s68, v122\n"
    "v_nop\n"
    "v_nop\n"
    "ds_read_b128 v[96:99], v106 offset:13312\n"
    "v_mfma_f32_16x16x32_bf16 v[8:11], v[60:63], v[88:91], 0\n"
    "v_cndmask_b32_e32 v120, v114, v120, vcc\n"
    "v_add_u32_e32 v120, s67, v120\n"
    "v_add_u32_e32 v122, 0, v121\n"
    "v_mul_u32_u24_e32 v108, 0xccd, v122\n"
    "ds_read_b64_tr_b16 v[60:61], v104 offset:10240\n"
    "ds_read_b64_tr_b16 v[62:63], v104 offset:15360\n"
    "s_waitcnt lgkmcnt(10)\n"
    "v_mfma_f32_16x16x32_bf16 v[12:15], v[64:67], v[80:83], 0\n"
    "v_lshrrev_b32_e32 v108, 16, v108\n"
    "v_mul_u32_u24_e32 v123, 20, v108\n"
    "v_sub_u32_e32 v122, v122, v123\n"
    "v_lshrrev_b32_e32 v123, 1, v108\n"
    "ds_read_b128 v[100:103], v106 offset:14336\n"
    "v_mfma_f32_16x16x32_bf16 v[16:19], v[64:67], v[84:87], 0\n"
    "v_and_b32_e32 v123, 2, v123\n"
    "v_xor_b32_e32 v122, v122, v123\n"
    "v_mul_u32_u24_e32 v108, 0x500, v108\n"
    "v_lshl_add_u32 v108, v122, 4, v108\n"
    "v_mfma_f32_16x16x32_bf16 v[20:23], v[64:67], v[88:91], 0\n"
    "v_add_u32_e32 v108, s89, v108\n"
    "v_add_u32_e32 v122, 64, v121\n"
    "v_mul_u32_u24_e32 v109, 0xccd, v122\n"
    "v_lshrrev_b32_e32 v109, 16, v109\n"
    "ds_read_b64_tr_b16 v[64:65], v105 offset:10240\n"
    "ds_read_b64_tr_b16 v[66:67], v105 offset:15360\n"
    "s_waitcnt lgkmcnt(11)\n"
    "v_mfma_f32_16x16x32_bf16 v[24:27], v[68:71], v[80:83], 0\n"
    "v_mul_u32_u24_e32 v123, 20, v109\n"
    "v_sub_u32_e32 v122, v122, v123\n"
    "v_lshrrev_b32_e32 v123, 1, v109\n"
    "v_and_b32_e32 v123, 2, v123\n"
    "v_mfma_f32_16x16x32_bf16 v[28:31], v[68:71], v[84:87], 0\n"
    "v_xor_b32_e32 v122, v122, v123\n"
    "v_mul_u32_u24_e32 v109, 0x500, v109\n"
    "v_lshl_add_u32 v109, v122, 4, v109\n"
    "v_add_u32_e32 v109, s89, v109\n"
    "v_mfma_f32_16x16x32_bf16 v[32:35], v[68:71], v[88:91], 0\n"
    "v_subrev_u32_e32 v109, 1024, v109\n"
    "v_add_u32_e32 v122, 128, v121\n"
    "v_mul_u32_u24_e32 v110, 0xccd, v122\n"
    "v_lshrrev_b32_e32 v110, 16, v110\n"
    "ds_read_b64_tr_b16 v[68:69], v104 offset:10304\n"
    "ds_read_b64_tr_b16 v[70:71], v104 offset:15424\n"
    "s_waitcnt lgkmcnt(11)\n"
    "v_mfma_f32_16x16x32_bf16 v[36:39], v[72:75], v[80:83], 0\n"
    "v_mul_u32_u24_e32 v123, 20, v110\n"
    "v_sub_u32_e32 v122, v122, v123\n"
    "v_lshrrev_b32_e32 v123, 1, v110\n"
    "v_and_b32_e32 v123, 2, v123\n"
    "v_mfma_f32_16x16x32_bf16 v[40:43], v[72:75], v[84:87], 0\n"
    "v_xor_b32_e32 v122, v122, v123\n"
    "v_mul_u32_u24_e32 v110, 0x500, v110\n"
    "v_lshl_add_u32 v110, v122, 4, v110\n"
    "v_add_u32_e32 v110, s89, v110\n"
    "v_mfma_f32_16x16x32_bf16 v[44:47], v[72:75], v[88:91], 0\n"
    "v_subrev_u32_e32 v110, 2048, v110\n"
    "v_add_u32_e32 v122, 192, v121\n"
    "v_mul_u32_u24_e32 v111, 0xccd, v122\n"
    "v_lshrrev_b32_e32 v111, 16, v111\n"
    "ds_read_b64_tr_b16 v[72:73], v105 offset:10304\n"
    "ds_read_b64_tr_b16 v[74:75], v105 offset:15424\n"
    "s_waitcnt lgkmcnt(11)\n"
    "v_mfma_f32_16x16x32_bf16 v[48:51], v[76:79], v[80:83], 0\n"
    "v_mul_u32_u24_e32 v123, 20, v111\n"
    "v_sub_u32_e32 v122, v122, v123\n"
    "v_lshrrev_b32_e32 v123, 1, v111\n"
    "v_and_b32_e32 v123, 2, v123\n"
    "v_mfma_f32_16x16x32_bf16 v[52:55], v[76:79], v[84:87], 0\n"
    "v_xor_b32_e32 v122, v122, v123\n"
    "v_mul_u32_u24_e32 v111, 0x500, v111\n"
    "v_lshl_add_u32 v111, v122, 4, v111\n"
    "v_add_u32_e32 v111, s89, v111\n"
    "v_mfma_f32_16x16x32_bf16 v[56:59], v[76:79], v[88:91], 0\n"
    "v_subrev_u32_e32 v111, 3072, v111\n"
    "v_add_u32_e32 v122, 256, v121\n"
    "v_mul_u32_u24_e32 v112, 0xccd, v122\n"
    "v_lshrrev_b32_e32 v112, 16, v112\n"
    "ds_read_b64_tr_b16 v[76:77], v104 offset:10368\n"
    "ds_read_b64_tr_b16 v[78:79], v104 offset:15488\n"
    "s_waitcnt lgkmcnt(9)\n"
    "v_mfma_f32_16x16x32_bf16 v[0:3], v[60:63], v[92:95], v[0:3]\n"
    "v_mul_u32_u24_e32 v123, 20, v112\n"
    "v_sub_u32_e32 v122, v122, v123\n"
    "v_lshrrev_b32_e32 v123, 1, v112\n"
    "v_and_b32_e32 v123, 2, v123\n"
    "v_mfma_f32_16x16x32_bf16 v[4:7], v[60:63], v[96:99], v[4:7]\n"
    "v_xor_b32_e32 v122, v122, v123\n"
    "v_mul_u32_u24_e32 v112, 0x500, v112\n"
    "v_lshl_add_u32 v112, v122, 4, v112\n"
    "v_add_u32_e32 v112, s89, v112\n"
    "s_waitcnt lgkmcnt(8)\n"
    "v_mfma_f32_16x16x32_bf16 v[8:11], v[60:63], v[100:103], v[8:11]\n"
    "s_waitcnt lgkmcnt(6)\n"
    "v_mfma_f32_16x16x32_bf16 v[12:15], v[64:67], v[92:95], v[12:15]\n"
    "v_mfma_f32_16x16x32_bf16 v[16:19], v[64:67], v[96:99], v[16:19]\n"
    "v_mfma_f32_16x16x32_bf16 v[20:23], v[64:67], v[100:103], v[20:23]\n"
    "s_waitcnt vmcnt(0) lgkmcnt(0)\n"
    "s_barrier\n"
    "s_cmp_lg_u32 s63, 4\n"
    "s_cbranch_scc1 .Lnpb0_%=\n"
    "v_lshl_or_b32 v118, v115, 8, 1\n"
    "buffer_store_dword v118, v119, s[48:51], 0 offen sc1\n"
    ".Lnpb0_%=:\n"
    "v_readlane_b32 s62, v115, s68\n"
    "s_nop 3\n"
    "s_lshl_b32 s62, s62, 8\n"
    "ds_read_b64_tr_b16 v[60:61], v104 offset:20480\n"
    "ds_read_b64_tr_b16 v[62:63], v104 offset:25600\n"
    "v_mfma_f32_16x16x32_bf16 v[24:27], v[68:71], v[92:95], v[24:27]\n"
    "ds_read_b128 v[80:83], v106 offset:24576\n"
    "ds_read_b64_tr_b16 v[64:65], v105 offset:20480\n"
    "ds_read_b64_tr_b16 v[66:67], v105 offset:25600\n"
    "s_add_u32 m0, s52, 0\n"
    "s_add_u32 s64, s58, 245760\n"
    "buffer_load_dwordx4 v108, s[40:43], s64 offen offset:0 lds\n"
    "v_mfma_f32_16x16x32_bf16 v[28:31], v[68:71], v[96:99], v[28:31]\n"
    "ds_read_b128 v[84:87], v106 offset:25600\n"
    "v_mfma_f32_16x16x32_bf16 v[32:35], v[68:71], v[100:103], v[32:35]\n"
    "ds_read_b128 v[88:91], v106 offset:26624\n"
    "ds_read_b64_tr_b16 v[68:69], v104 offset:20544\n"
    "ds_read_b64_tr_b16 v[70:71], v104 offset:25664\n"
    "buffer_load_dwordx4 v109, s[40:43], s64 offen offset:1024 lds\n"
    "v_mfma_f32_16x16x32_bf16 v[36:39], v[72:75], v[92:95], v[36:39]\n"
    "v_mfma_f32_16x16x32_bf16 v[40:43], v[72:75], v[96:99], v[40:43]\n"
    "buffer_load_dwordx4 v110, s[40:43], s64 offen offset:2048 lds\n"
    "v_mfma_f32_16x16x32_bf16 v[44:47], v[72:75], v[100:103], v[44:47]\n"
    "ds_read_b64_tr_b16 v[72:73], v105 offset:20544\n"
    "ds_read_b64_tr_b16 v[74:75], v105 offset:25664\n"
    "v_mfma_f32_16x16x32_bf16 v[48:51], v[76:79], v[92:95], v[48:51]\n"
    "buffer_load_dwordx4 v111, s[40:43], s64 offen offset:3072 lds\n"
    "s_add_u32 m0, s52, 4096\n"
    "v_mfma_f32_16x16x32_bf16 v[52:55], v[76:79], v[96:99], v[52:55]\n"
    "v_mfma_f32_16x16x32_bf16 v[56:59], v[76:79], v[100:103], v[56:59]\n"
    "ds_read_b64_tr_b16 v[76:77], v104 offset:20608\n"
    "ds_read_b64_tr_b16 v[78:79], v104 offset:25728\n"
    "buffer_load_dwordx4 v112, s[40:43], s64 offen offset:0 lds\n"
    "s_waitcnt lgkmcnt(10)\n"
    "v_mfma_f32_16x16x32_bf16 v[0:3], v[60:63], v[80:83], v[0:3]\n"
    "ds_read_b128 v[92:95], v106 offset:36864\n"
    "s_waitcnt lgkmcnt(8)\n"
    "v_mfma_f32_16x16x32_bf16 v[4:7], v[60:63], v[84:87], v[4:7]\n"
    "ds_read_b128 v[96:99], v106 offset:37888\n"
    "s_waitcnt lgkmcnt(8)\n"
    "v_mfma_f32_16x16x32_bf16 v[8:11], v[60:63], v[88:91], v[8:11]\n"
    "ds_read_b64_tr_b16 v[60:61], v104 offset:30720\n"
    "ds_read_b64_tr_b16 v[62:63], v104 offset:35840\n"
    "v_mfma_f32_16x16x32_bf16 v[12:15], v[64:67], v[80:83], v[12:15]\n"
    "s_sleep 1\n"
    "ds_read_b128 v[100:103], v106 offset:38912\n"
    "v_mfma_f32_16x16x32_bf16 v[16:19], v[64:67], v[84:87], v[16:19]\n"
    "v_mfma_f32_16x16x32_bf16 v[20:23], v[64:67], v[88:91], v[20:23]\n"
    "ds_read_b64_tr_b16 v[64:65], v105 offset:30720\n"
    "ds_read_b64_tr_b16 v[66:67], v105 offset:35840\n"
    "s_waitcnt lgkmcnt(11)\n"
    "v_mfma_f32_16x16x32_bf16 v[24:27], v[68:71], v[80:83], v[24:27]\n"
    "v_mfma_f32_16x16x32_bf16 v[28:31], v[68:71], v[84:87], v[28:31]\n"
    "v_mfma_f32_16x16x32_bf16 v[32:35], v[68:71], v[88:91], v[32:35]\n"
    "ds_read_b64_tr_b16 v[68:69], v104 offset:30784\n"
    "ds_read_b64_tr_b16 v[70:71], v104 offset:35904\n"
    "s_waitcnt lgkmcnt(11)\n"
    "v_mfma_f32_16x16x32_bf16 v[36:39], v[72:75], v[80:83], v[36:39]\n"
    "v_mfma_f32_16x16x32_bf16 v[40:43], v[72:75], v[84:87], v[40:43]\n"
    "s_sleep 1\n"
    "v_mfma_f32_16x16x32_bf16 v[44:47], v[72:75], v[88:91], v[44:47]\n"
    "ds_read_b64_tr_b16 v[72:73], v105 offset:30784\n"
    "ds_read_b64_tr_b16 v[74:75], v105 offset:35904\n"
    "s_waitcnt lgkmcnt(11)\n"
    "v_mfma_f32_16x16x32_bf16 v[48:51], v[76:79], v[80:83], v[48:51]\n"
    "v_mfma_f32_16x16x32_bf16 v[52:55], v[76:79], v[84:87], v[52:55]\n"
    "v_mfma_f32_16x16x32_bf16 v[56:59], v[76:79], v[88:91], v[56:59]\n"
    "ds_read_b64_tr_b16 v[76:77], v104 offset:30848\n"
    "ds_read_b64_tr_b16 v[78:79], v104 offset:35968\n"
    "s_waitcnt lgkmcnt(9)\n"
    "v_mfma_f32_16x16x32_bf16 v[0:3], v[60:63], v[92:95], v[0:3]\n"
    "v_mfma_f32_16x16x32_bf16 v[4:7], v[60:63], v[96:99], v[4:7]\n"
    "s_waitcnt lgkmcnt(8)\n"
    "v_mfma_f32_16x16x32_bf16 v[8:11], v[60:63], v[100:103], v[8:11]\n"
    "s_waitcnt lgkmcnt(6)\n"
    "v_mfma_f32_16x16x32_bf16 v[12:15], v[64:67], v[92:95], v[12:15]\n"
    "s_sleep 1\n"
    "v_mfma_f32_16x16x32_bf16 v[16:19], v[64:67], v[96:99], v[16:19]\n"
    "v_mfma_f32_16x16x32_bf16 v[20:23], v[64:67], v[100:103], v[20:23]\n"
    "s_waitcnt vmcnt(18) lgkmcnt(0)\n"
    "s_barrier\n"
    "ds_read_b64_tr_b16 v[60:61], v104 offset:40960\n"
    "ds_read_b64_tr_b16 v[62:63], v104 offset:46080\n"
    "v_mfma_f32_16x16x32_bf16 v[24:27], v[68:71], v[92:95], v[24:27]\n"
    "ds_read_b128 v[80:83], v107 offset:0\n"
    "ds_read_b64_tr_b16 v[64:65], v105 offset:40960\n"
    "ds_read_b64_tr_b16 v[66:67], v105 offset:46080\n"
    "s_add_u32 m0, s52, 20480\n"
    "s_add_u32 s64, s59, 0\n"
    "buffer_load_dwordx4 v108, s[40:43], s64 offen offset:0 lds\n"
    "v_mfma_f32_16x16x32_bf16 v[28:31], v[68:71], v[96:99], v[28:31]\n"
    "ds_read_b128 v[84:87], v107 offset:1024\n"
    "v_mfma_f32_16x16x32_bf16 v[32:35], v[68:71], v[100:103], v[32:35]\n"
    "ds_read_b128 v[88:91], v107 offset:2048\n"
    "ds_read_b64_tr_b16 v[68:69], v104 offset:41024\n"
    "ds_read_b64_tr_b16 v[70:71], v104 offset:46144\n"
    "buffer_load_dwordx4 v109, s[40:43], s64 offen offset:1024 lds\n"
    "v_mfma_f32_16x16x32_bf16 v[36:39], v[72:75], v[92:95], v[36:39]\n"
    "v_mfma_f32_16x16x32_bf16 v[40:43], v[72:75], v[96:99], v[40:43]\n"
    "s_sleep 1\n"
    "buffer_load_dwordx4 v110, s[40:43], s64 offen offset:2048 lds\n"
    "v_mfma_f32_16x16x32_bf16 v[44:47], v[72:75], v[100:103], v[44:47]\n"
    "ds_read_b64_tr_b16 v[72:73], v105 offset:41024\n"
    "ds_read_b64_tr_b16 v[74:75], v105 offset:46144\n"
    "v_mfma_f32_16x16x32_bf16 v[48:51], v[76:79], v[92:95], v[48:51]\n"
    "buffer_load_dwordx4 v111, s[40:43], s64 offen offset:3072 lds\n"
    "s_add_u32 m0, s52, 24576\n"
    "v_mfma_f32_16x16x32_bf16 v[52:55], v[76:79], v[96:99], v[52:55]\n"
    "v_mfma_f32_16x16x32_bf16 v[56:59], v[76:79], v[100:103], v[56:59]\n"
    "ds_read_b64_tr_b16 v[76:77], v104 offset:41088\n"
    "ds_read_b64_tr_b16 v[78:79], v104 offset:46208\n"
    "buffer_load_dwordx4 v112, s[40:43], s64 offen offset:0 lds\n"
    "s_waitcnt lgkmcnt(10)\n"
    "v_mfma_f32_16x16x32_bf16 v[0:3], v[60:63], v[80:83], v[0:3]\n"
    "ds_read_b128 v[92:95], v107 offset:12288\n"
    "s_waitcnt lgkmcnt(8)\n"
    "v_mfma_f32_16x16x32_bf16 v[4:7], v[60:63], v[84:87], v[4:7]\n"
    "ds_read_b128 v[96:99], v107 offset:13312\n"
    "s_waitcnt lgkmcnt(8)\n"
    "v_mfma_f32_16x16x32_bf16 v[8:11], v[60:63], v[88:91], v[8:11]\n"
    "ds_read_b64_tr_b16 v[60:61], v104 offset:51200\n"
    "ds_read_b64_tr_b16 v[62:63], v104 offset:56320\n"
    "v_mfma_f32_16x16x32_bf16 v[12:15], v[64:67], v[80:83], v[12:15]\n"
    "s_sleep 1\n"
    "ds_read_b128 v[100:103], v107 offset:14336\n"
    "v_mfma_f32_16x16x32_bf16 v[16:19], v[64:67], v[84:87], v[16:19]\n"
    "v_mfma_f32_16x16x32_bf16 v[20:23], v[64:67], v[88:91], v[20:23]\n"
    "ds_read_b64_tr_b16 v[64:65], v105 offset:51200\n"
    "ds_read_b64_tr_b16 v[66:67], v105 offset:56320\n"
    "s_waitcnt lgkmcnt(11)\n"
    "v_mfma_f32_16x16x32_bf16 v[24:27], v[68:71], v[80:83], v[24:27]\n"
    "buffer_load_dword v116, v120, s[48:51], 0 offen sc1\n"
    "buffer_load_dword v117, v114, s[48:51], 0 offen sc1\n"
    "v_mfma_f32_16x16x32_bf16 v[28:31], v[68:71], v[84:87], v[28:31]\n"
    "v_mfma_f32_16x16x32_bf16 v[32:35], v[68:71], v[88:91], v[32:35]\n"
    "ds_read_b64_tr_b16 v[68:69], v104 offset:51264\n"
    "ds_read_b64_tr_b16 v[70:71], v104 offset:56384\n"
    "s_waitcnt lgkmcnt(11)\n"
    "v_mfma_f32_16x16x32_bf16 v[36:39], v[72:75], v[80:83], v[36:39]\n"
    "v_mfma_f32_16x16x32_bf16 v[40:43], v[72:75], v[84:87], v[40:43]\n"
    "s_sleep 1\n"
    "v_mfma_f32_16x16x32_bf16 v[44:47], v[72:75], v[88:91], v[44:47]\n"
    "ds_read_b64_tr_b16 v[72:73], v105 offset:51264\n"
    "ds_read_b64_tr_b16 v[74:75], v105 offset:56384\n"
    "s_waitcnt lgkmcnt(11)\n"
    "v_mfma_f32_16x16x32_bf16 v[48:51], v[76:79], v[80:83], v[48:51]\n"
    "v_mfma_f32_16x16x32_bf16 v[52:55], v[76:79], v[84:87], v[52:55]\n"
    "v_mfma_f32_16x16x32_bf16 v[56:59], v[76:79], v[88:91], v[56:59]\n"
    "ds_read_b64_tr_b16 v[76:77], v104 offset:51328\n"
    "ds_read_b64_tr_b16 v[78:79], v104 offset:56448\n"
    "s_waitcnt lgkmcnt(9)\n"
    "v_mfma_f32_16x16x32_bf16 v[0:3], v[60:63], v[92:95], v[0:3]\n"
    "v_mfma_f32_16x16x32_bf16 v[4:7], v[60:63], v[96:99], v[4:7]\n"
    "s_waitcnt lgkmcnt(8)\n"
    "v_mfma_f32_16x16x32_bf16 v[8:11], v[60:63], v[100:103], v[8:11]\n"
    "s_waitcnt lgkmcnt(6)\n"
    "v_mfma_f32_16x16x32_bf16 v[12:15], v[64:67], v[92:95], v[12:15]\n"
    "s_sleep 1\n"
    "v_mfma_f32_16x16x32_bf16 v[16:19], v[64:67], v[96:99], v[16:19]\n"
    "v_mfma_f32_16x16x32_bf16 v[20:23], v[64:67], v[100:103], v[20:23]\n"
    "s_waitcnt vmcnt(0)\n"
    "v_subrev_u32_e32 v118, s62, v116\n"
    "v_subrev_u32_e32 v118, 1, v118\n"
    "v_cmp_lt_u32_e32 vcc, 1, v118\n"
    "s_cbranch_vccnz .Lslow1_%=\n"
    "v_cmp_ne_u32_e32 vcc, v117, v115\n"
    "s_cbranch_vccnz .Lslow1_%=\n"
    ".Lok1_%=:\n"
    "s_add_u32 m0, s53, 0\n"
    "s_add_u32 s65, s55, 0\n"
    "buffer_load_dwordx4 v113, s[44:47], s65 offen offset:0 sc1 lds\n"
    "buffer_load_dwordx4 v113, s[44:47], s65 offen offset:1024 sc1 lds\n"
    "buffer_load_dwordx4 v113, s[44:47], s65 offen offset:2048 sc1 lds\n"
    "s_add_u32 m0, s53, 3072\n"
    "s_add_u32 s65, s55, 3072\n"
    "buffer_load_dwordx4 v113, s[44:47], s65 offen offset:0 sc1 lds\n"
    "buffer_load_dwordx4 v113, s[44:47], s65 offen offset:1024 sc1 lds\n"
    "buffer_load_dwordx4 v113, s[44:47], s65 offen offset:2048 sc1 lds\n"
    "s_add_u32 m0, s53, 24576\n"
    "s_add_u32 s65, s56, 0\n"
    "buffer_load_dwordx4 v113, s[44:47], s65 offen offset:0 sc1 lds\n"
    "buffer_load_dwordx4 v113, s[44:47], s65 offen offset:1024 sc1 lds\n"
    "buffer_load_dwordx4 v113, s[44:47], s65 offen offset:2048 sc1 lds\n"
    "s_add_u32 m0, s53, 27648\n"
    "s_add_u32 s65, s56, 3072\n"
    "buffer_load_dwordx4 v113, s[44:47], s65 offen offset:0 sc1 lds\n"
    "buffer_load_dwordx4 v113, s[44:47], s65 offen offset:1024 sc1 lds\n"
    "buffer_load_dwordx4 v113, s[44:47], s65 offen offset:2048 sc1 lds\n"
    "s_waitcnt vmcnt(19) lgkmcnt(0)\n"
    "s_barrier\n"
    "ds_read_b64_tr_b16 v[60:61], v104 offset:0\n"
    "ds_read_b64_tr_b16 v[62:63], v104 offset:5120\n"
    "v_mfma_f32_16x16x32_bf16 v[24:27], v[68:71], v[92:95], v[24:27]\n"
    "ds_read_b128 v[80:83], v107 offset:24576\n"
    "ds_read_b64_tr_b16 v[64:65], v105 offset:0\n"
    "ds_read_b64_tr_b16 v[66:67], v105 offset:5120\n"
    "s_add_u32 m0, s52, 40960\n"
    "s_add_u32 s64, s60, 0\n"
    "buffer_load_dwordx4 v108, s[40:43], s64 offen offset:0 lds\n"
    "v_mfma_f32_16x16x32_bf16 v[28:31], v[68:71], v[96:99], v[28:31]\n"
    "ds_read_b128 v[84:87], v107 offset:25600\n"
    "buffer_load_dwordx4 v109, s[40:43], s64 offen offset:1024 lds\n"
    "v_mfma_f32_16x16x32_bf16 v[32:35], v[68:71], v[100:103], v[32:35]\n"
    "ds_read_b128 v[88:91], v107 offset:26624\n"
    "ds_read_b64_tr_b16 v[68:69], v104 offset:64\n"
    "ds_read_b64_tr_b16 v[70:71], v104 offset:5184\n"
    "buffer_load_dwordx4 v110, s[40:43], s64 offen offset:2048 lds\n"
    "v_mfma_f32_16x16x32_bf16 v[36:39], v[72:75], v[92:95], v[36:39]\n"
    "v_mfma_f32_16x16x32_bf16 v[40:43], v[72:75], v[96:99], v[40:43]\n"
    "s_sleep 1\n"
    "buffer_load_dwordx4 v111, s[40:43], s64 offen offset:3072 lds\n"
    "s_add_u32 m0, s52, 45056\n"
    "v_mfma_f32_16x16x32_bf16 v[44:47], v[72:75], v[100:103], v[44:47]\n"
    "ds_read_b64_tr_b16 v[72:73], v105 offset:64\n"
    "ds_read_b64_tr_b16 v[74:75], v105 offset:5184\n"
    "v_mfma_f32_16x16x32_bf16 v[48:51], v[76:79], v[92:95], v[48:51]\n"
    "buffer_load_dwordx4 v112, s[40:43], s64 offen offset:0 lds\n"
    "v_mfma_f32_16x16x32_bf16 v[52:55], v[76:79], v[96:99], v[52:55]\n"
    "v_mfma_f32_16x16x32_bf16 v[56:59], v[76:79], v[100:103], v[56:59]\n"
    "ds_read_b64_tr_b16 v[76:77], v104 offset:128\n"
    "ds_read_b64_tr_b16 v[78:79], v104 offset:5248\n"
    "s_add_u32 m0, s53, 49152\n"
    "s_add_u32 s65, s57, 0\n"
    "buffer_load_dwordx4 v113, s[44:47], s65 offen offset:0 sc1 lds\n"
    "s_waitcnt lgkmcnt(10)\n"
    "v_mfma_f32_16x16x32_bf16 v[0:3], v[60:63], v[80:83], v[0:3]\n"
    "ds_read_b128 v[92:95], v107 offset:36864\n"
    "buffer_load_dwordx4 v113, s[44:47], s65 offen offset:1024 sc1 lds\n"
    "s_waitcnt lgkmcnt(8)\n"
    "v_mfma_f32_16x16x32_bf16 v[4:7], v[60:63], v[84:87], v[4:7]\n"
    "ds_read_b128 v[96:99], v107 offset:37888\n"
    "s_waitcnt lgkmcnt(8)\n"
    "v_mfma_f32_16x16x32_bf16 v[8:11], v[60:63], v[88:91], v[8:11]\n"
    "ds_read_b64_tr_b16 v[60:61], v104 offset:10240\n"
    "ds_read_b64_tr_b16 v[62:63], v104 offset:15360\n"
    "buffer_load_dwordx4 v113, s[44:47], s65 offen offset:2048 sc1 lds\n"
    "v_mfma_f32_16x16x32_bf16 v[12:15], v[64:67], v[80:83], v[12:15]\n"
    "s_sleep 1\n"
    "ds_read_b128 v[100:103], v107 offset:38912\n"
    "v_mfma_f32_16x16x32_bf16 v[16:19], v[64:67], v[84:87], v[16:19]\n"
    "s_add_u32 m0, s53, 52224\n"
    "s_add_u32 s65, s57, 3072\n"
    "buffer_load_dwordx4 v113, s[44:47], s65 offen offset:0 sc1 lds\n"
    "v_mfma_f32_16x16x32_bf16 v[20:23], v[64:67], v[88:91], v[20:23]\n"
    "ds_read_b64_tr_b16 v[64:65], v105 offset:10240\n"
    "ds_read_b64_tr_b16 v[66:67], v105 offset:15360\n"
    "s_waitcnt lgkmcnt(11)\n"
    "v_mfma_f32_16x16x32_bf16 v[24:27], v[68:71], v[80:83], v[24:27]\n"
    "buffer_load_dwordx4 v113, s[44:47], s65 offen offset:1024 sc1 lds\n"
    "v_mfma_f32_16x16x32_bf16 v[28:31], v[68:71], v[84:87], v[28:31]\n"
    "v_mfma_f32_16x16x32_bf16 v[32:35], v[68:71], v[88:91], v[32:35]\n"
    "ds_read_b64_tr_b16 v[68:69], v104 offset:10304\n"
    "ds_read_b64_tr_b16 v[70:71], v104 offset:15424\n"
    "buffer_load_dwordx4 v113, s[44:47], s65 offen offset:2048 sc1 lds\n"
    "s_waitcnt lgkmcnt(11)\n"
    "v_mfma_f32_16x16x32_bf16 v[36:39], v[72:75], v[80:83], v[36:39]\n"
    "v_mfma_f32_16x16x32_bf16 v[40:43], v[72:75], v[84:87], v[40:43]\n"
    "s_sleep 1\n"
    "v_mfma_f32_16x16x32_bf16 v[44:47], v[72:75], v[88:91], v[44:47]\n"
    "ds_read_b64_tr_b16 v[72:73], v105 offset:10304\n"
    "ds_read_b64_tr_b16 v[74:75], v105 offset:15424\n"
    "s_waitcnt lgkmcnt(11)\n"
    "v_mfma_f32_16x16x32_bf16 v[48:51], v[76:79], v[80:83], v[48:51]\n"
    "v_mfma_f32_16x16x32_bf16 v[52:55], v[76:79], v[84:87], v[52:55]\n"
    "v_mfma_f32_16x16x32_bf16 v[56:59], v[76:79], v[88:91], v[56:59]\n"
    "ds_read_b64_tr_b16 v[76:77], v104 offset:10368\n"
    "ds_read_b64_tr_b16 v[78:79], v104 offset:15488\n"
    "s_waitcnt lgkmcnt(9)\n"
    "v_mfma_f32_16x16x32_bf16 v[0:3], v[60:63], v[92:95], v[0:3]\n"
    "v_mfma_f32_16x16x32_bf16 v[4:7], v[60:63], v[96:99], v[4:7]\n"
    "s_waitcnt lgkmcnt(8)\n"
    "v_mfma_f32_16x16x32_bf16 v[8:11], v[60:63], v[100:103], v[8:11]\n"
    "s_waitcnt lgkmcnt(6)\n"
    "v_mfma_f32_16x16x32_bf16 v[12:15], v[64:67], v[92:95], v[12:15]\n"
    "s_sleep 1\n"
    "v_mfma_f32_16x16x32_bf16 v[16:19], v[64:67], v[96:99], v[16:19]\n"
    "v_mfma_f32_16x16x32_bf16 v[20:23], v[64:67], v[100:103], v[20:23]\n"
    "s_waitcnt vmcnt(17) lgkmcnt(0)\n"
    "s_barrier\n"
    "ds_read_b64_tr_b16 v[60:61], v104 offset:20480\n"
    "ds_read_b64_tr_b16 v[62:63], v104 offset:25600\n"
    "v_mfma_f32_16x16x32_bf16 v[24:27], v[68:71], v[92:95], v[24:27]\n"
    "ds_read_b128 v[80:83], v106 offset:0\n"
    "ds_read_b64_tr_b16 v[64:65], v105 offset:20480\n"
    "ds_read_b64_tr_b16 v[66:67], v105 offset:25600\n"
    "s_add_u32 m0, s52, 0\n"
    "s_add_u32 s64, s61, 0\n"
    "buffer_load_dwordx4 v108, s[40:43], s64 offen offset:0 lds\n"
    "v_mfma_f32_16x16x32_bf16 v[28:31], v[68:71], v[96:99], v[28:31]\n"
    "ds_read_b128 v[84:87], v106 offset:1024\n"
    "buffer_load_dwordx4 v109, s[40:43], s64 offen offset:1024 lds\n"
    "v_mfma_f32_16x16x32_bf16 v[32:35], v[68:71], v[100:103], v[32:35]\n"
    "ds_read_b128 v[88:91], v106 offset:2048\n"
    "ds_read_b64_tr_b16 v[68:69], v104 offset:20544\n"
    "ds_read_b64_tr_b16 v[70:71], v104 offset:25664\n"
    "buffer_load_dwordx4 v110, s[40:43], s64 offen offset:2048 lds\n"
    "v_mfma_f32_16x16x32_bf16 v[36:39], v[72:75], v[92:95], v[36:39]\n"
    "v_mfma_f32_16x16x32_bf16 v[40:43], v[72:75], v[96:99], v[40:43]\n"
    "s_sleep 1\n"
    "buffer_load_dwordx4 v111, s[40:43], s64 offen offset:3072 lds\n"
    "s_add_u32 m0, s52, 4096\n"
    "v_mfma_f32_16x16x32_bf16 v[44:47], v[72:75], v[100:103], v[44:47]\n"
    "ds_read_b64_tr_b16 v[72:73], v105 offset:20544\n"
    "ds_read_b64_tr_b16 v[74:75], v105 offset:25664\n"
    "v_mfma_f32_16x16x32_bf16 v[48:51], v[76:79], v[92:95], v[48:51]\n"
    "buffer_load_dwordx4 v112, s[40:43], s64 offen offset:0 lds\n"
    "v_mfma_f32_16x16x32_bf16 v[52:55], v[76:79], v[96:99], v[52:55]\n"
    "v_mfma_f32_16x16x32_bf16 v[56:59], v[76:79], v[100:103], v[56:59]\n"
    "ds_read_b64_tr_b16 v[76:77], v104 offset:20608\n"
    "ds_read_b64_tr_b16 v[78:79], v104 offset:25728\n"
    "s_add_u32 m0, s53, 73728\n"
    "s_add_u32 s65, s55, 24576\n"
    "buffer_load_dwordx4 v113, s[44:47], s65 offen offset:0 sc1 lds\n"
    "s_waitcnt lgkmcnt(10)\n"
    "v_mfma_f32_16x16x32_bf16 v[0:3], v[60:63], v[80:83], v[0:3]\n"
    "ds_read_b128 v[92:95], v106 offset:12288\n"
    "buffer_load_dwordx4 v113, s[44:47], s65 offen offset:1024 sc1 lds\n"
    "s_waitcnt lgkmcnt(8)\n"
    "v_mfma_f32_16x16x32_bf16 v[4:7], v[60:63], v[84:87], v[4:7]\n"
    "ds_read_b128 v[96:99], v106 offset:13312\n"
    "s_waitcnt lgkmcnt(8)\n"
    "v_mfma_f32_16x16x32_bf16 v[8:11], v[60:63], v[88:91], v[8:11]\n"
    "ds_read_b64_tr_b16 v[60:61], v104 offset:30720\n"
    "ds_read_b64_tr_b16 v[62:63], v104 offset:35840\n"
    "buffer_load_dwordx4 v113, s[44:47], s65 offen offset:2048 sc1 lds\n"
    "v_mfma_f32_16x16x32_bf16 v[12:15], v[64:67], v[80:83], v[12:15]\n"
    "s_sleep 1\n"
    "ds_read_b128 v[100:103], v106 offset:14336\n"
    "v_mfma_f32_16x16x32_bf16 v[16:19], v[64:67], v[84:87], v[16:19]\n"
    "s_add_u32 m0, s53, 76800\n"
    "s_add_u32 s65, s55, 27648\n"
    "buffer_load_dwordx4 v113, s[44:47], s65 offen offset:0 sc1 lds\n"
    "v_mfma_f32_16x16x32_bf16 v[20:23], v[64:67], v[88:91], v[20:23]\n"
    "ds_read_b64_tr_b16 v[64:65], v105 offset:30720\n"
    "ds_read_b64_tr_b16 v[66:67], v105 offset:35840\n"
    "s_waitcnt lgkmcnt(11)\n"
    "v_mfma_f32_16x16x32_bf16 v[24:27], v[68:71], v[80:83], v[24:27]\n"
    "buffer_load_dwordx4 v113, s[44:47], s65 offen offset:1024 sc1 lds\n"
    "v_mfma_f32_16x16x32_bf16 v[28:31], v[68:71], v[84:87], v[28:31]\n"
    "v_mfma_f32_16x16x32_bf16 v[32:35], v[68:71], v[88:91], v[32:35]\n"
    "ds_read_b64_tr_b16 v[68:69], v104 offset:30784\n"
    "ds_read_b64_tr_b16 v[70:71], v104 offset:35904\n"
    "buffer_load_dwordx4 v113, s[44:47], s65 offen offset:2048 sc1 lds\n"
    "s_waitcnt lgkmcnt(11)\n"
    "v_mfma_f32_16x16x32_bf16 v[36:39], v[72:75], v[80:83], v[36:39]\n"
    "v_mfma_f32_16x16x32_bf16 v[40:43], v[72:75], v[84:87], v[40:43]\n"
    "s_sleep 1\n"
    "v_mfma_f32_16x16x32_bf16 v[44:47], v[72:75], v[88:91], v[44:47]\n"
    "ds_read_b64_tr_b16 v[72:73], v105 offset:30784\n"
    "ds_read_b64_tr_b16 v[74:75], v105 offset:35904\n"
    "s_waitcnt lgkmcnt(11)\n"
    "v_mfma_f32_16x16x32_bf16 v[48:51], v[76:79], v[80:83], v[48:51]\n"
    "v_mfma_f32_16x16x32_bf16 v[52:55], v[76:79], v[84:87], v[52:55]\n"
    "v_mfma_f32_16x16x32_bf16 v[56:59], v[76:79], v[88:91], v[56:59]\n"
    "ds_read_b64_tr_b16 v[76:77], v104 offset:30848\n"
    "ds_read_b64_tr_b16 v[78:79], v104 offset:35968\n"
    "s_waitcnt lgkmcnt(9)\n"
    "v_mfma_f32_16x16x32_bf16 v[0:3], v[60:63], v[92:95], v[0:3]\n"
    "v_mfma_f32_16x16x32_bf16 v[4:7], v[60:63], v[96:99], v[4:7]\n"
    "s_waitcnt lgkmcnt(8)\n"
    "v_mfma_f32_16x16x32_bf16 v[8:11], v[60:63], v[100:103], v[8:11]\n"
    "s_waitcnt lgkmcnt(6)\n"
    "v_mfma_f32_16x16x32_bf16 v[12:15], v[64:67], v[92:95], v[12:15]\n"
    "s_sleep 1\n"
    "v_mfma_f32_16x16x32_bf16 v[16:19], v[64:67], v[96:99], v[16:19]\n"
    "v_mfma_f32_16x16x32_bf16 v[20:23], v[64:67], v[100:103], v[20:23]\n"
    "s_waitcnt vmcnt(17) lgkmcnt(0)\n"
    "s_barrier\n"
    "s_cmp_lg_u32 s63, 4\n"
    "s_cbranch_scc1 .Lnpb4_%=\n"
    "v_lshl_or_b32 v118, v115, 8, 2\n"
    "buffer_store_dword v118, v119, s[48:51], 0 offen sc1\n"
    ".Lnpb4_%=:\n"
    "ds_read_b64_tr_b16 v[60:61], v104 offset:40960\n"
    "ds_read_b64_tr_b16 v[62:63], v104 offset:46080\n"
    "v_mfma_f32_16x16x32_bf16 v[24:27], v[68:71], v[92:95], v[24:27]\n"
    "ds_read_b128 v[80:83], v106 offset:24576\n"
    "ds_read_b64_tr_b16 v[64:65], v105 offset:40960\n"
    "ds_read_b64_tr_b16 v[66:67], v105 offset:46080\n"
    "s_add_u32 m0, s52, 20480\n"
    "s_add_u32 s64, s59, 81920\n"
    "buffer_load_dwordx4 v108, s[40:43], s64 offen offset:0 lds\n"
    "v_mfma_f32_16x16x32_bf16 v[28:31], v[68:71], v[96:99], v[28:31]\n"
    "ds_read_b128 v[84:87], v106 offset:25600\n"
    "buffer_load_dwordx4 v109, s[40:43], s64 offen offset:1024 lds\n"
    "v_mfma_f32_16x16x32_bf16 v[32:35], v[68:71], v[100:103], v[32:35]\n"
    "ds_read_b128 v[88:91], v106 offset:26624\n"
    "ds_read_b64_tr_b16 v[68:69], v104 offset:41024\n"
    "ds_read_b64_tr_b16 v[70:71], v104 offset:46144\n"
    "buffer_load_dwordx4 v110, s[40:43], s64 offen offset:2048 lds\n"
    "v_mfma_f32_16x16x32_bf16 v[36:39], v[72:75], v[92:95], v[36:39]\n"
    "v_mfma_f32_16x16x32_bf16 v[40:43], v[72:75], v[96:99], v[40:43]\n"
    "s_sleep 1\n"
    "buffer_load_dwordx4 v111, s[40:43], s64 offen offset:3072 lds\n"
    "s_add_u32 m0, s52, 24576\n"
    "v_mfma_f32_16x16x32_bf16 v[44:47], v[72:75], v[100:103], v[44:47]\n"
    "ds_read_b64_tr_b16 v[72:73], v105 offset:41024\n"
    "ds_read_b64_tr_b16 v[74:75], v105 offset:46144\n"
    "v_mfma_f32_16x16x32_bf16 v[48:51], v[76:79], v[92:95], v[48:51]\n"
    "buffer_load_dwordx4 v112, s[40:43], s64 offen offset:0 lds\n"
    "v_mfma_f32_16x16x32_bf16 v[52:55], v[76:79], v[96:99], v[52:55]\n"
    "v_mfma_f32_16x16x32_bf16 v[56:59], v[76:79], v[100:103], v[56:59]\n"
    "ds_read_b64_tr_b16 v[76:77], v104 offset:41088\n"
    "ds_read_b64_tr_b16 v[78:79], v104 offset:46208\n"
    "s_add_u32 m0, s53, 0\n"
    "s_add_u32 s65, s56, 24576\n"
    "buffer_load_dwordx4 v113, s[44:47], s65 offen offset:0 sc1 lds\n"
    "s_waitcnt lgkmcnt(10)\n"
    "v_mfma_f32_16x16x32_bf16 v[0:3], v[60:63], v[80:83], v[0:3]\n"
    "ds_read_b128 v[92:95], v106 offset:36864\n"
    "buffer_load_dwordx4 v113, s[44:47], s65 offen offset:1024 sc1 lds\n"
    "s_waitcnt lgkmcnt(8)\n"
    "v_mfma_f32_16x16x32_bf16 v[4:7], v[60:63], v[84:87], v[4:7]\n"
    "ds_read_b128 v[96:99], v106 offset:37888\n"
    "s_waitcnt lgkmcnt(8)\n"
    "v_mfma_f32_16x16x32_bf16 v[8:11], v[60:63], v[88:91], v[8:11]\n"
    "ds_read_b64_tr_b16 v[60:61], v104 offset:51200\n"
    "ds_read_b64_tr_b16 v[62:63], v104 offset:56320\n"
    "buffer_load_dwordx4 v113, s[44:47], s65 offen offset:2048 sc1 lds\n"
    "v_mfma_f32_16x16x32_bf16 v[12:15], v[64:67], v[80:83], v[12:15]\n"
    "ds_read_b128 v[100:103], v106 offset:38912\n"
    "v_mfma_f32_16x16x32_bf16 v[16:19], v[64:67], v[84:87], v[16:19]\n"
    "s_add_u32 m0, s53, 3072\n"
    "s_add_u32 s65, s56, 27648\n"
    "buffer_load_dwordx4 v113, s[44:47], s65 offen offset:0 sc1 lds\n"
    "v_mfma_f32_16x16x32_bf16 v[20:23], v[64:67], v[88:91], v[20:23]\n"
    "ds_read_b64_tr_b16 v[64:65], v105 offset:51200\n"
    "ds_read_b64_tr_b16 v[66:67], v105 offset:56320\n"
    "s_waitcnt lgkmcnt(11)\n"
    "v_mfma_f32_16x16x32_bf16 v[24:27], v[68:71], v[80:83], v[24:27]\n"
    "buffer_load_dwordx4 v113, s[44:47], s65 offen offset:1024 sc1 lds\n"
    "v_mfma_f32_16x16x32_bf16 v[28:31], v[68:71], v[84:87], v[28:31]\n"
    "v_mfma_f32_16x16x32_bf16 v[32:35], v[68:71], v[88:91], v[32:35]\n"
    "ds_read_b64_tr_b16 v[68:69], v104 offset:51264\n"
    "ds_read_b64_tr_b16 v[70:71], v104 offset:56384\n"
    "buffer_load_dwordx4 v113, s[44:47], s65 offen offset:2048 sc1 lds\n"
    "s_waitcnt lgkmcnt(11)\n"
    "v_mfma_f32_16x16x32_bf16 v[36:39], v[72:75], v[80:83], v[36:39]\n"
    "v_mfma_f32_16x16x32_bf16 v[40:43], v[72:75], v[84:87], v[40:43]\n"
    "v_mfma_f32_16x16x32_bf16 v[44:47], v[72:75], v[88:91], v[44:47]\n"
    "ds_read_b64_tr_b16 v[72:73], v105 offset:51264\n"
    "ds_read_b64_tr_b16 v[74:75], v105 offset:56384\n"
    "s_waitcnt lgkmcnt(11)\n"
    "v_mfma_f32_16x16x32_bf16 v[48:51], v[76:79], v[80:83], v[48:51]\n"
    "v_mfma_f32_16x16x32_bf16 v[52:55], v[76:79], v[84:87], v[52:55]\n"
    "v_mfma_f32_16x16x32_bf16 v[56:59], v[76:79], v[88:91], v[56:59]\n"
    "ds_read_b64_tr_b16 v[76:77], v104 offset:51328\n"
    "ds_read_b64_tr_b16 v[78:79], v104 offset:56448\n"
    "s_waitcnt lgkmcnt(9)\n"
    "v_mfma_f32_16x16x32_bf16 v[0:3], v[60:63], v[92:95], v[0:3]\n"
    "v_mfma_f32_16x16x32_bf16 v[4:7], v[60:63], v[96:99], v[4:7]\n"
    "s_waitcnt lgkmcnt(8)\n"
    "v_mfma_f32_16x16x32_bf16 v[8:11], v[60:63], v[100:103], v[8:11]\n"
    "s_waitcnt lgkmcnt(6)\n"
    "v_mfma_f32_16x16x32_bf16 v[12:15], v[64:67], v[92:95], v[12:15]\n"
    "v_mfma_f32_16x16x32_bf16 v[16:19], v[64:67], v[96:99], v[16:19]\n"
    "v_mfma_f32_16x16x32_bf16 v[20:23], v[64:67], v[100:103], v[20:23]\n"
    "s_waitcnt vmcnt(17) lgkmcnt(0)\n"
    "s_barrier\n"
    "ds_read_b64_tr_b16 v[60:61], v104 offset:0\n"
    "ds_read_b64_tr_b16 v[62:63], v104 offset:5120\n"
    "v_mfma_f32_16x16x32_bf16 v[24:27], v[68:71], v[92:95], v[24:27]\n"
    "ds_read_b128 v[80:83], v107 offset:0\n"
    "ds_read_b64_tr_b16 v[64:65], v105 offset:0\n"
    "ds_read_b64_tr_b16 v[66:67], v105 offset:5120\n"
    "buffer_load_dword v116, v120, s[48:51], 0 offen sc1\n"
    "buffer_load_dword v117, v114, s[48:51], 0 offen sc1\n"
    "v_mfma_f32_16x16x32_bf16 v[28:31], v[68:71], v[96:99], v[28:31]\n"
    "ds_read_b128 v[84:87], v107 offset:1024\n"
    "s_add_u32 m0, s52, 40960\n"
    "s_add_u32 s64, s60, 81920\n"
    "buffer_load_dwordx4 v108, s[40:43], s64 offen offset:0 lds\n"
    "v_mfma_f32_16x16x32_bf16 v[32:35], v[68:71], v[100:103], v[32:35]\n"
    "ds_read_b128 v[88:91], v107 offset:2048\n"
    "ds_read_b64_tr_b16 v[68:69], v104 offset:64\n"
    "ds_read_b64_tr_b16 v[70:71], v104 offset:5184\n"
    "buffer_load_dwordx4 v109, s[40:43], s64 offen offset:1024 lds\n"
    "v_mfma_f32_16x16x32_bf16 v[36:39], v[72:75], v[92:95], v[36:39]\n"
    "buffer_load_dwordx4 v110, s[40:43], s64 offen offset:2048 lds\n"
    "v_mfma_f32_16x16x32_bf16 v[40:43], v[72:75], v[96:99], v[40:43]\n"
    "buffer_load_dwordx4 v111, s[40:43], s64 offen offset:3072 lds\n"
    "s_add_u32 m0, s52, 45056\n"
    "v_mfma_f32_16x16x32_bf16 v[44:47], v[72:75], v[100:103], v[44:47]\n"
    "ds_read_b64_tr_b16 v[72:73], v105 offset:64\n"
    "ds_read_b64_tr_b16 v[74:75], v105 offset:5184\n"
    "v_mfma_f32_16x16x32_bf16 v[48:51], v[76:79], v[92:95], v[48:51]\n"
    "buffer_load_dwordx4 v112, s[40:43], s64 offen offset:0 lds\n"
    "v_mfma_f32_16x16x32_bf16 v[52:55], v[76:79], v[96:99], v[52:55]\n"
    "v_mfma_f32_16x16x32_bf16 v[56:59], v[76:79], v[100:103], v[56:59]\n"
    "ds_read_b64_tr_b16 v[76:77], v104 offset:128\n"
    "ds_read_b64_tr_b16 v[78:79], v104 offset:5248\n"
    "s_add_u32 m0, s53, 24576\n"
    "s_add_u32 s65, s57, 24576\n"
    "buffer_load_dwordx4 v113, s[44:47], s65 offen offset:0 sc1 lds\n"
    "s_waitcnt lgkmcnt(10)\n"
    "v_mfma_f32_16x16x32_bf16 v[0:3], v[60:63], v[80:83], v[0:3]\n"
    "ds_read_b128 v[92:95], v107 offset:12288\n"
    "buffer_load_dwordx4 v113, s[44:47], s65 offen offset:1024 sc1 lds\n"
    "s_waitcnt lgkmcnt(8)\n"
    "v_mfma_f32_16x16x32_bf16 v[4:7], v[60:63], v[84:87], v[4:7]\n"
    "ds_read_b128 v[96:99], v107 offset:13312\n"
    "s_waitcnt lgkmcnt(8)\n"
    "v_mfma_f32_16x16x32_bf16 v[8:11], v[60:63], v[88:91], v[8:11]\n"
    "ds_read_b64_tr_b16 v[60:61], v104 offset:10240\n"
    "ds_read_b64_tr_b16 v[62:63], v104 offset:15360\n"
    "buffer_load_dwordx4 v113, s[44:47], s65 offen offset:2048 sc1 lds\n"
    "v_mfma_f32_16x16x32_bf16 v[12:15], v[64:67], v[80:83], v[12:15]\n"
    "ds_read_b128 v[100:103], v107 offset:14336\n"
    "v_mfma_f32_16x16x32_bf16 v[16:19], v[64:67], v[84:87], v[16:19]\n"
    "s_add_u32 m0, s53, 27648\n"
    "s_add_u32 s65, s57, 27648\n"
    "buffer_load_dwordx4 v113, s[44:47], s65 offen offset:0 sc1 lds\n"
    "v_mfma_f32_16x16x32_bf16 v[20:23], v[64:67], v[88:91], v[20:23]\n"
    "ds_read_b64_tr_b16 v[64:65], v105 offset:10240\n"
    "ds_read_b64_tr_b16 v[66:67], v105 offset:15360\n"
    "s_waitcnt lgkmcnt(11)\n"
    "v_mfma_f32_16x16x32_bf16 v[24:27], v[68:71], v[80:83], v[24:27]\n"
    "buffer_load_dwordx4 v113, s[44:47], s65 offen offset:1024 sc1 lds\n"
    "v_mfma_f32_16x16x32_bf16 v[28:31], v[68:71], v[84:87], v[28:31]\n"
    "v_mfma_f32_16x16x32_bf16 v[32:35], v[68:71], v[88:91], v[32:35]\n"
    "ds_read_b64_tr_b16 v[68:69], v104 offset:10304\n"
    "ds_read_b64_tr_b16 v[70:71], v104 offset:15424\n"
    "buffer_load_dwordx4 v113, s[44:47], s65 offen offset:2048 sc1 lds\n"
    "s_waitcnt lgkmcnt(11)\n"
    "v_mfma_f32_16x16x32_bf16 v[36:39], v[72:75], v[80:83], v[36:39]\n"
    "v_mfma_f32_16x16x32_bf16 v[40:43], v[72:75], v[84:87], v[40:43]\n"
    "v_mfma_f32_16x16x32_bf16 v[44:47], v[72:75], v[88:91], v[44:47]\n"
    "ds_read_b64_tr_b16 v[72:73], v105 offset:10304\n"
    "ds_read_b64_tr_b16 v[74:75], v105 offset:15424\n"
    "s_waitcnt lgkmcnt(11)\n"
    "v_mfma_f32_16x16x32_bf16 v[48:51], v[76:79], v[80:83], v[48:51]\n"
    "v_mfma_f32_16x16x32_bf16 v[52:55], v[76:79], v[84:87], v[52:55]\n"
    "v_mfma_f32_16x16x32_bf16 v[56:59], v[76:79], v[88:91], v[56:59]\n"
    "ds_read_b64_tr_b16 v[76:77], v104 offset:10368\n"
    "ds_read_b64_tr_b16 v[78:79], v104 offset:15488\n"
    "s_waitcnt lgkmcnt(9)\n"
    "v_mfma_f32_16x16x32_bf16 v[0:3], v[60:63], v[92:95], v[0:3]\n"
    "v_mfma_f32_16x16x32_bf16 v[4:7], v[60:63], v[96:99], v[4:7]\n"
    "s_waitcnt lgkmcnt(8)\n"
    "v_mfma_f32_16x16x32_bf16 v[8:11], v[60:63], v[100:103], v[8:11]\n"
    "s_waitcnt lgkmcnt(6)\n"
    "v_mfma_f32_16x16x32_bf16 v[12:15], v[64:67], v[92:95], v[12:15]\n"
    "v_mfma_f32_16x16x32_bf16 v[16:19], v[64:67], v[96:99], v[16:19]\n"
    "v_mfma_f32_16x16x32_bf16 v[20:23], v[64:67], v[100:103], v[20:23]\n"
    "s_waitcnt vmcnt(11)\n"
    "v_subrev_u32_e32 v118, s62, v116\n"
    "v_subrev_u32_e32 v118, 2, v118\n"
    "v_cmp_lt_u32_e32 vcc, 1, v118\n"
    "s_cbranch_vccnz .Lslow5_%=\n"
    "v_cmp_ne_u32_e32 vcc, v117, v115\n"
    "s_cbranch_vccnz .Lslow5_%=\n"
    ".Lok5_%=:\n"
    "s_waitcnt vmcnt(19) lgkmcnt(0)\n"
    "s_barrier\n"
    "ds_read_b64_tr_b16 v[60:61], v104 offset:20480\n"
    "ds_read_b64_tr_b16 v[62:63], v104 offset:25600\n"
    "v_mfma_f32_16x16x32_bf16 v[24:27], v[68:71], v[92:95], v[24:27]\n"
    "ds_read_b128 v[80:83], v107 offset:24576\n"
    "ds_read_b64_tr_b16 v[64:65], v105 offset:20480\n"
    "ds_read_b64_tr_b16 v[66:67], v105 offset:25600\n"
    "s_add_u32 m0, s52, 0\n"
    "s_add_u32 s64, s61, 81920\n"
    "buffer_load_dwordx4 v108, s[40:43], s64 offen offset:0 lds\n"
    "v_mfma_f32_16x16x32_bf16 v[28:31], v[68:71], v[96:99], v[28:31]\n"
    "ds_read_b128 v[84:87], v107 offset:25600\n"
    "buffer_load_dwordx4 v109, s[40:43], s64 offen offset:1024 lds\n"
    "v_mfma_f32_16x16x32_bf16 v[32:35], v[68:71], v[100:103], v[32:35]\n"
    "ds_read_b128 v[88:91], v107 offset:26624\n"
    "ds_read_b64_tr_b16 v[68:69], v104 offset:20544\n"
    "ds_read_b64_tr_b16 v[70:71], v104 offset:25664\n"
    "buffer_load_dwordx4 v110, s[40:43], s64 offen offset:2048 lds\n"
    "v_mfma_f32_16x16x32_bf16 v[36:39], v[72:75], v[92:95], v[36:39]\n"
    "v_mfma_f32_16x16x32_bf16 v[40:43], v[72:75], v[96:99], v[40:43]\n"
    "buffer_load_dwordx4 v111, s[40:43], s64 offen offset:3072 lds\n"
    "s_add_u32 m0, s52, 4096\n"
    "v_mfma_f32_16x16x32_bf16 v[44:47], v[72:75], v[100:103], v[44:47]\n"
    "ds_read_b64_tr_b16 v[72:73], v105 offset:20544\n"
    "ds_read_b64_tr_b16 v[74:75], v105 offset:25664\n"
    "v_mfma_f32_16x16x32_bf16 v[48:51], v[76:79], v[92:95], v[48:51]\n"
    "buffer_load_dwordx4 v112, s[40:43], s64 offen offset:0 lds\n"
    "v_mfma_f32_16x16x32_bf16 v[52:55], v[76:79], v[96:99], v[52:55]\n"
    "v_mfma_f32_16x16x32_bf16 v[56:59], v[76:79], v[100:103], v[56:59]\n"
    "ds_read_b64_tr_b16 v[76:77], v104 offset:20608\n"
    "ds_read_b64_tr_b16 v[78:79], v104 offset:25728\n"
    "s_add_u32 m0, s53, 49152\n"
    "s_add_u32 s65, s55, 49152\n"
    "buffer_load_dwordx4 v113, s[44:47], s65 offen offset:0 sc1 lds\n"
    "s_waitcnt lgkmcnt(10)\n"
    "v_mfma_f32_16x16x32_bf16 v[0:3], v[60:63], v[80:83], v[0:3]\n"
    "ds_read_b128 v[92:95], v107 offset:36864\n"
    "buffer_load_dwordx4 v113, s[44:47], s65 offen offset:1024 sc1 lds\n"
    "s_waitcnt lgkmcnt(8)\n"
    "v_mfma_f32_16x16x32_bf16 v[4:7], v[60:63], v[84:87], v[4:7]\n"
    "ds_read_b128 v[96:99], v107 offset:37888\n"
    "s_waitcnt lgkmcnt(8)\n"
    "v_mfma_f32_16x16x32_bf16 v[8:11], v[60:63], v[88:91], v[8:11]\n"
    "ds_read_b64_tr_b16 v[60:61], v104 offset:30720\n"
    "ds_read_b64_tr_b16 v[62:63], v104 offset:35840\n"
    "buffer_load_dwordx4 v113, s[44:47], s65 offen offset:2048 sc1 lds\n"
    "v_mfma_f32_16x16x32_bf16 v[12:15], v[64:67], v[80:83], v[12:15]\n"
    "ds_read_b128 v[100:103], v107 offset:38912\n"
    "v_mfma_f32_16x16x32_bf16 v[16:19], v[64:67], v[84:87], v[16:19]\n"
    "s_add_u32 m0, s53, 52224\n"
    "s_add_u32 s65, s55, 52224\n"
    "buffer_load_dwordx4 v113, s[44:47], s65 offen offset:0 sc1 lds\n"
    "v_mfma_f32_16x16x32_bf16 v[20:23], v[64:67], v[88:91], v[20:23]\n"
    "ds_read_b64_tr_b16 v[64:65], v105 offset:30720\n"
    "ds_read_b64_tr_b16 v[66:67], v105 offset:35840\n"
    "s_waitcnt lgkmcnt(11)\n"
    "v_mfma_f32_16x16x32_bf16 v[24:27], v[68:71], v[80:83], v[24:27]\n"
    "buffer_load_dwordx4 v113, s[44:47], s65 offen offset:1024 sc1 lds\n"
    "v_mfma_f32_16x16x32_bf16 v[28:31], v[68:71], v[84:87], v[28:31]\n"
    "v_mfma_f32_16x16x32_bf16 v[32:35], v[68:71], v[88:91], v[32:35]\n"
    "ds_read_b64_tr_b16 v[68:69], v104 offset:30784\n"
    "ds_read_b64_tr_b16 v[70:71], v104 offset:35904\n"
    "buffer_load_dwordx4 v113, s[44:47], s65 offen offset:2048 sc1 lds\n"
    "s_waitcnt lgkmcnt(11)\n"
    "v_mfma_f32_16x16x32_bf16 v[36:39], v[72:75], v[80:83], v[36:39]\n"
    "v_mfma_f32_16x16x32_bf16 v[40:43], v[72:75], v[84:87], v[40:43]\n"
    "v_mfma_f32_16x16x32_bf16 v[44:47], v[72:75], v[88:91], v[44:47]\n"
    "ds_read_b64_tr_b16 v[72:73], v105 offset:30784\n"
    "ds_read_b64_tr_b16 v[74:75], v105 offset:35904\n"
    "s_waitcnt lgkmcnt(11)\n"
    "v_mfma_f32_16x16x32_bf16 v[48:51], v[76:79], v[80:83], v[48:51]\n"
    "v_mfma_f32_16x16x32_bf16 v[52:55], v[76:79], v[84:87], v[52:55]\n"
    "v_mfma_f32_16x16x32_bf16 v[56:59], v[76:79], v[88:91], v[56:59]\n"
    "ds_read_b64_tr_b16 v[76:77], v104 offset:30848\n"
    "ds_read_b64_tr_b16 v[78:79], v104 offset:35968\n"
    "s_waitcnt lgkmcnt(9)\n"
    "v_mfma_f32_16x16x32_bf16 v[0:3], v[60:63], v[92:95], v[0:3]\n"
    "v_mfma_f32_16x16x32_bf16 v[4:7], v[60:63], v[96:99], v[4:7]\n"
    "s_waitcnt lgkmcnt(8)\n"
    "v_mfma_f32_16x16x32_bf16 v[8:11], v[60:63], v[100:103], v[8:11]\n"
    "s_waitcnt lgkmcnt(6)\n"
    "v_mfma_f32_16x16x32_bf16 v[12:15], v[64:67], v[92:95], v[12:15]\n"
    "v_mfma_f32_16x16x32_bf16 v[16:19], v[64:67], v[96:99], v[16:19]\n"
    "v_mfma_f32_16x16x32_bf16 v[20:23], v[64:67], v[100:103], v[20:23]\n"
    "s_waitcnt vmcnt(17) lgkmcnt(0)\n"
    "s_barrier\n"
    "ds_read_b64_tr_b16 v[60:61], v104 offset:40960\n"
    "ds_read_b64_tr_b16 v[62:63], v104 offset:46080\n"
    "v_mfma_f32_16x16x32_bf16 v[24:27], v[68:71], v[92:95], v[24:27]\n"
    "ds_read_b128 v[80:83], v106 offset:0\n"
    "ds_read_b64_tr_b16 v[64:65], v105 offset:40960\n"
    "ds_read_b64_tr_b16 v[66:67], v105 offset:46080\n"
    "s_add_u32 m0, s52, 20480\n"
    "s_add_u32 s64, s59, 163840\n"
    "buffer_load_dwordx4 v108, s[40:43], s64 offen offset:0 lds\n"
    "v_mfma_f32_16x16x32_bf16 v[28:31], v[68:71], v[96:99], v[28:31]\n"
    "ds_read_b128 v[84:87], v106 offset:1024\n"
    "buffer_load_dwordx4 v109, s[40:43], s64 offen offset:1024 lds\n"
    "v_mfma_f32_16x16x32_bf16 v[32:35], v[68:71], v[100:103], v[32:35]\n"
    "ds_read_b128 v[88:91], v106 offset:2048\n"
    "ds_read_b64_tr_b16 v[68:69], v104 offset:41024\n"
    "ds_read_b64_tr_b16 v[70:71], v104 offset:46144\n"
    "buffer_load_dwordx4 v110, s[40:43], s64 offen offset:2048 lds\n"
    "v_mfma_f32_16x16x32_bf16 v[36:39], v[72:75], v[92:95], v[36:39]\n"
    "v_mfma_f32_16x16x32_bf16 v[40:43], v[72:75], v[96:99], v[40:43]\n"
    "buffer_load_dwordx4 v111, s[40:43], s64 offen offset:3072 lds\n"
    "s_add_u32 m0, s52, 24576\n"
    "v_mfma_f32_16x16x32_bf16 v[44:47], v[72:75], v[100:103], v[44:47]\n"
    "ds_read_b64_tr_b16 v[72:73], v105 offset:41024\n"
    "ds_read_b64_tr_b16 v[74:75], v105 offset:46144\n"
    "v_mfma_f32_16x16x32_bf16 v[48:51], v[76:79], v[92:95], v[48:51]\n"
    "buffer_load_dwordx4 v112, s[40:43], s64 offen offset:0 lds\n"
    "v_mfma_f32_16x16x32_bf16 v[52:55], v[76:79], v[96:99], v[52:55]\n"
    "v_mfma_f32_16x16x32_bf16 v[56:59], v[76:79], v[100:103], v[56:59]\n"
    "ds_read_b64_tr_b16 v[76:77], v104 offset:41088\n"
    "ds_read_b64_tr_b16 v[78:79], v104 offset:46208\n"
    "s_add_u32 m0, s53, 73728\n"
    "s_add_u32 s65, s56, 49152\n"
    "buffer_load_dwordx4 v113, s[44:47], s65 offen offset:0 sc1 lds\n"
    "s_waitcnt lgkmcnt(10)\n"
    "v_mfma_f32_16x16x32_bf16 v[0:3], v[60:63], v[80:83], v[0:3]\n"
    "ds_read_b128 v[92:95], v106 offset:12288\n"
    "buffer_load_dwordx4 v113, s[44:47], s65 offen offset:1024 sc1 lds\n"
    "s_waitcnt lgkmcnt(8)\n"
    "v_mfma_f32_16x16x32_bf16 v[4:7], v[60:63], v[84:87], v[4:7]\n"
    "ds_read_b128 v[96:99], v106 offset:13312\n"
    "s_waitcnt lgkmcnt(8)\n"
    "v_mfma_f32_16x16x32_bf16 v[8:11], v[60:63], v[88:91], v[8:11]\n"
    "ds_read_b64_tr_b16 v[60:61], v104 offset:51200\n"
    "ds_read_b64_tr_b16 v[62:63], v104 offset:56320\n"
    "buffer_load_dwordx4 v113, s[44:47], s65 offen offset:2048 sc1 lds\n"
    "v_mfma_f32_16x16x32_bf16 v[12:15], v[64:67], v[80:83], v[12:15]\n"
    "ds_read_b128 v[100:103], v106 offset:14336\n"
    "v_mfma_f32_16x16x32_bf16 v[16:19], v[64:67], v[84:87], v[16:19]\n"
    "s_add_u32 m0, s53, 76800\n"
    "s_add_u32 s65, s56, 52224\n"
    "buffer_load_dwordx4 v113, s[44:47], s65 offen offset:0 sc1 lds\n"
    "v_mfma_f32_16x16x32_bf16 v[20:23], v[64:67], v[88:91], v[20:23]\n"
    "ds_read_b64_tr_b16 v[64:65], v105 offset:51200\n"
    "ds_read_b64_tr_b16 v[66:67], v105 offset:56320\n"
    "s_waitcnt lgkmcnt(11)\n"
    "v_mfma_f32_16x16x32_bf16 v[24:27], v[68:71], v[80:83], v[24:27]\n"
    "buffer_load_dwordx4 v113, s[44:47], s65 offen offset:1024 sc1 lds\n"
    "v_mfma_f32_16x16x32_bf16 v[28:31], v[68:71], v[84:87], v[28:31]\n"
    "v_mfma_f32_16x16x32_bf16 v[32:35], v[68:71], v[88:91], v[32:35]\n"
    "ds_read_b64_tr_b16 v[68:69], v104 offset:51264\n"
    "ds_read_b64_tr_b16 v[70:71], v104 offset:56384\n"
    "buffer_load_dwordx4 v113, s[44:47], s65 offen offset:2048 sc1 lds\n"
    "s_waitcnt lgkmcnt(11)\n"
    "v_mfma_f32_16x16x32_bf16 v[36:39], v[72:75], v[80:83], v[36:39]\n"
    "v_mfma_f32_16x16x32_bf16 v[40:43], v[72:75], v[84:87], v[40:43]\n"
    "v_mfma_f32_16x16x32_bf16 v[44:47], v[72:75], v[88:91], v[44:47]\n"
    "ds_read_b64_tr_b16 v[72:73], v105 offset:51264\n"
    "ds_read_b64_tr_b16 v[74:75], v105 offset:56384\n"
    "s_waitcnt lgkmcnt(11)\n"
    "v_mfma_f32_16x16x32_bf16 v[48:51], v[76:79], v[80:83], v[48:51]\n"
    "v_mfma_f32_16x16x32_bf16 v[52:55], v[76:79], v[84:87], v[52:55]\n"
    "v_mfma_f32_16x16x32_bf16 v[56:59], v[76:79], v[88:91], v[56:59]\n"
    "ds_read_b64_tr_b16 v[76:77], v104 offset:51328\n"
    "ds_read_b64_tr_b16 v[78:79], v104 offset:56448\n"
    "s_waitcnt lgkmcnt(9)\n"
    "v_mfma_f32_16x16x32_bf16 v[0:3], v[60:63], v[92:95], v[0:3]\n"
    "v_mfma_f32_16x16x32_bf16 v[4:7], v[60:63], v[96:99], v[4:7]\n"
    "s_waitcnt lgkmcnt(8)\n"
    "v_mfma_f32_16x16x32_bf16 v[8:11], v[60:63], v[100:103], v[8:11]\n"
    "s_waitcnt lgkmcnt(6)\n"
    "v_mfma_f32_16x16x32_bf16 v[12:15], v[64:67], v[92:95], v[12:15]\n"
    "v_mfma_f32_16x16x32_bf16 v[16:19], v[64:67], v[96:99], v[16:19]\n"
    "v_mfma_f32_16x16x32_bf16 v[20:23], v[64:67], v[100:103], v[20:23]\n"
    "s_waitcnt vmcnt(17) lgkmcnt(0)\n"
    "s_barrier\n"
    "ds_read_b64_tr_b16 v[60:61], v104 offset:0\n"
    "ds_read_b64_tr_b16 v[62:63], v104 offset:5120\n"
    "v_mfma_f32_16x16x32_bf16 v[24:27], v[68:71], v[92:95], v[24:27]\n"
    "ds_read_b128 v[80:83], v106 offset:24576\n"
    "ds_read_b64_tr_b16 v[64:65], v105 offset:0\n"
    "ds_read_b64_tr_b16 v[66:67], v105 offset:5120\n"
    "s_add_u32 m0, s52, 40960\n"
    "s_add_u32 s64, s60, 163840\n"
    "buffer_load_dwordx4 v108, s[40:43], s64 offen offset:0 lds\n"
    "v_mfma_f32_16x16x32_bf16 v[28:31], v[68:71], v[96:99], v[28:31]\n"
    "ds_read_b128 v[84:87], v106 offset:25600\n"
    "buffer_load_dwordx4 v109, s[40:43], s64 offen offset:1024 lds\n"
    "v_mfma_f32_16x16x32_bf16 v[32:35], v[68:71], v[100:103], v[32:35]\n"
    "ds_read_b128 v[88:91], v106 offset:26624\n"
    "ds_read_b64_tr_b16 v[68:69], v104 offset:64\n"
    "ds_read_b64_tr_b16 v[70:71], v104 offset:5184\n"
    "buffer_load_dwordx4 v110, s[40:43], s64 offen offset:2048 lds\n"
    "v_mfma_f32_16x16x32_bf16 v[36:39], v[72:75], v[92:95], v[36:39]\n"
    "v_mfma_f32_16x16x32_bf16 v[40:43], v[72:75], v[96:99], v[40:43]\n"
    "buffer_load_dwordx4 v111, s[40:43], s64 offen offset:3072 lds\n"
    "s_add_u32 m0, s52, 45056\n"
    "v_mfma_f32_16x16x32_bf16 v[44:47], v[72:75], v[100:103], v[44:47]\n"
    "ds_read_b64_tr_b16 v[72:73], v105 offset:64\n"
    "ds_read_b64_tr_b16 v[74:75], v105 offset:5184\n"
    "v_mfma_f32_16x16x32_bf16 v[48:51], v[76:79], v[92:95], v[48:51]\n"
    "buffer_load_dwordx4 v112, s[40:43], s64 offen offset:0 lds\n"
    "v_mfma_f32_16x16x32_bf16 v[52:55], v[76:79], v[96:99], v[52:55]\n"
    "v_mfma_f32_16x16x32_bf16 v[56:59], v[76:79], v[100:103], v[56:59]\n"
    "ds_read_b64_tr_b16 v[76:77], v104 offset:128\n"
    "ds_read_b64_tr_b16 v[78:79], v104 offset:5248\n"
    "s_add_u32 m0, s53, 0\n"
    "s_add_u32 s65, s57, 49152\n"
    "buffer_load_dwordx4 v113, s[44:47], s65 offen offset:0 sc1 lds\n"
    "s_waitcnt lgkmcnt(10)\n"
    "v_mfma_f32_16x16x32_bf16 v[0:3], v[60:63], v[80:83], v[0:3]\n"
    "ds_read_b128 v[92:95], v106 offset:36864\n"
    "buffer_load_dwordx4 v113, s[44:47], s65 offen offset:1024 sc1 lds\n"
    "s_waitcnt lgkmcnt(8)\n"
    "v_mfma_f32_16x16x32_bf16 v[4:7], v[60:63], v[84:87], v[4:7]\n"
    "ds_read_b128 v[96:99], v106 offset:37888\n"
    "s_waitcnt lgkmcnt(8)\n"
    "v_mfma_f32_16x16x32_bf16 v[8:11], v[60:63], v[88:91], v[8:11]\n"
    "ds_read_b64_tr_b16 v[60:61], v104 offset:10240\n"
    "ds_read_b64_tr_b16 v[62:63], v104 offset:15360\n"
    "buffer_load_dwordx4 v113, s[44:47], s65 offen offset:2048 sc1 lds\n"
    "v_mfma_f32_16x16x32_bf16 v[12:15], v[64:67], v[80:83], v[12:15]\n"
    "ds_read_b128 v[100:103], v106 offset:38912\n"
    "v_mfma_f32_16x16x32_bf16 v[16:19], v[64:67], v[84:87], v[16:19]\n"
    "s_add_u32 m0, s53, 3072\n"
    "s_add_u32 s65, s57, 52224\n"
    "buffer_load_dwordx4 v113, s[44:47], s65 offen offset:0 sc1 lds\n"
    "v_mfma_f32_16x16x32_bf16 v[20:23], v[64:67], v[88:91], v[20:23]\n"
    "ds_read_b64_tr_b16 v[64:65], v105 offset:10240\n"
    "ds_read_b64_tr_b16 v[66:67], v105 offset:15360\n"
    "s_waitcnt lgkmcnt(11)\n"
    "v_mfma_f32_16x16x32_bf16 v[24:27], v[68:71], v[80:83], v[24:27]\n"
    "buffer_load_dwordx4 v113, s[44:47], s65 offen offset:1024 sc1 lds\n"
    "v_mfma_f32_16x16x32_bf16 v[28:31], v[68:71], v[84:87], v[28:31]\n"
    "v_mfma_f32_16x16x32_bf16 v[32:35], v[68:71], v[88:91], v[32:35]\n"
    "ds_read_b64_tr_b16 v[68:69], v104 offset:10304\n"
    "ds_read_b64_tr_b16 v[70:71], v104 offset:15424\n"
    "buffer_load_dwordx4 v113, s[44:47], s65 offen offset:2048 sc1 lds\n"
    "s_waitcnt lgkmcnt(11)\n"
    "v_mfma_f32_16x16x32_bf16 v[36:39], v[72:75], v[80:83], v[36:39]\n"
    "v_mfma_f32_16x16x32_bf16 v[40:43], v[72:75], v[84:87], v[40:43]\n"
    "v_mfma_f32_16x16x32_bf16 v[44:47], v[72:75], v[88:91], v[44:47]\n"
    "ds_read_b64_tr_b16 v[72:73], v105 offset:10304\n"
    "ds_read_b64_tr_b16 v[74:75], v105 offset:15424\n"
    "s_waitcnt lgkmcnt(11)\n"
    "v_mfma_f32_16x16x32_bf16 v[48:51], v[76:79], v[80:83], v[48:51]\n"
    "v_mfma_f32_16x16x32_bf16 v[52:55], v[76:79], v[84:87], v[52:55]\n"
    "v_mfma_f32_16x16x32_bf16 v[56:59], v[76:79], v[88:91], v[56:59]\n"
    "ds_read_b64_tr_b16 v[76:77], v104 offset:10368\n"
    "ds_read_b64_tr_b16 v[78:79], v104 offset:15488\n"
    "s_waitcnt lgkmcnt(9)\n"
    "v_mfma_f32_16x16x32_bf16 v[0:3], v[60:63], v[92:95], v[0:3]\n"
    "v_mfma_f32_16x16x32_bf16 v[4:7], v[60:63], v[96:99], v[4:7]\n"
    "s_waitcnt lgkmcnt(8)\n"
    "v_mfma_f32_16x16x32_bf16 v[8:11], v[60:63], v[100:103], v[8:11]\n"
    "s_waitcnt lgkmcnt(6)\n"
    "v_mfma_f32_16x16x32_bf16 v[12:15], v[64:67], v[92:95], v[12:15]\n"
    "v_mfma_f32_16x16x32_bf16 v[16:19], v[64:67], v[96:99], v[16:19]\n"
    "v_mfma_f32_16x16x32_bf16 v[20:23], v[64:67], v[100:103], v[20:23]\n"
    "s_waitcnt vmcnt(17) lgkmcnt(0)\n"
    "s_barrier\n"
    "ds_read_b64_tr_b16 v[60:61], v104 offset:20480\n"
    "ds_read_b64_tr_b16 v[62:63], v104 offset:25600\n"
    "v_mfma_f32_16x16x32_bf16 v[24:27], v[68:71], v[92:95], v[24:27]\n"
    "ds_read_b128 v[80:83], v107 offset:0\n"
    "ds_read_b64_tr_b16 v[64:65], v105 offset:20480\n"
    "ds_read_b64_tr_b16 v[66:67], v105 offset:25600\n"
    "s_add_u32 m0, s52, 0\n"
    "s_add_u32 s64, s61, 163840\n"
    "buffer_load_dwordx4 v108, s[40:43], s64 offen offset:0 lds\n"
    "v_mfma_f32_16x16x32_bf16 v[28:31], v[68:71], v[96:99], v[28:31]\n"
    "ds_read_b128 v[84:87], v107 offset:1024\n"
    "buffer_load_dwordx4 v109, s[40:43], s64 offen offset:1024 lds\n"
    "v_mfma_f32_16x16x32_bf16 v[32:35], v[68:71], v[100:103], v[32:35]\n"
    "ds_read_b128 v[88:91], v107 offset:2048\n"
    "ds_read_b64_tr_b16 v[68:69], v104 offset:20544\n"
    "ds_read_b64_tr_b16 v[70:71], v104 offset:25664\n"
    "buffer_load_dwordx4 v110, s[40:43], s64 offen offset:2048 lds\n"
    "v_mfma_f32_16x16x32_bf16 v[36:39], v[72:75], v[92:95], v[36:39]\n"
    "v_mfma_f32_16x16x32_bf16 v[40:43], v[72:75], v[96:99], v[40:43]\n"
    "buffer_load_dwordx4 v111, s[40:43], s64 offen offset:3072 lds\n"
    "s_add_u32 m0, s52, 4096\n"
    "v_mfma_f32_16x16x32_bf16 v[44:47], v[72:75], v[100:103], v[44:47]\n"
    "ds_read_b64_tr_b16 v[72:73], v105 offset:20544\n"
    "ds_read_b64_tr_b16 v[74:75], v105 offset:25664\n"
    "v_mfma_f32_16x16x32_bf16 v[48:51], v[76:79], v[92:95], v[48:51]\n"
    "buffer_load_dwordx4 v112, s[40:43], s64 offen offset:0 lds\n"
    "v_mfma_f32_16x16x32_bf16 v[52:55], v[76:79], v[96:99], v[52:55]\n"
    "v_mfma_f32_16x16x32_bf16 v[56:59], v[76:79], v[100:103], v[56:59]\n"
    "ds_read_b64_tr_b16 v[76:77], v104 offset:20608\n"
    "ds_read_b64_tr_b16 v[78:79], v104 offset:25728\n"
    "s_add_u32 m0, s53, 24576\n"
    "s_add_u32 s65, s55, 73728\n"
    "buffer_load_dwordx4 v113, s[44:47], s65 offen offset:0 sc1 lds\n"
    "s_waitcnt lgkmcnt(10)\n"
    "v_mfma_f32_16x16x32_bf16 v[0:3], v[60:63], v[80:83], v[0:3]\n"
    "ds_read_b128 v[92:95], v107 offset:12288\n"
    "buffer_load_dwordx4 v113, s[44:47], s65 offen offset:1024 sc1 lds\n"
    "s_waitcnt lgkmcnt(8)\n"
    "v_mfma_f32_16x16x32_bf16 v[4:7], v[60:63], v[84:87], v[4:7]\n"
    "ds_read_b128 v[96:99], v107 offset:13312\n"
    "s_waitcnt lgkmcnt(8)\n"
    "v_mfma_f32_16x16x32_bf16 v[8:11], v[60:63], v[88:91], v[8:11]\n"
    "ds_read_b64_tr_b16 v[60:61], v104 offset:30720\n"
    "ds_read_b64_tr_b16 v[62:63], v104 offset:35840\n"
    "buffer_load_dwordx4 v113, s[44:47], s65 offen offset:2048 sc1 lds\n"
    "v_mfma_f32_16x16x32_bf16 v[12:15], v[64:67], v[80:83], v[12:15]\n"
    "ds_read_b128 v[100:103], v107 offset:14336\n"
    "v_mfma_f32_16x16x32_bf16 v[16:19], v[64:67], v[84:87], v[16:19]\n"
    "s_add_u32 m0, s53, 27648\n"
    "s_add_u32 s65, s55, 76800\n"
    "buffer_load_dwordx4 v113, s[44:47], s65 offen offset:0 sc1 lds\n"
    "v_mfma_f32_16x16x32_bf16 v[20:23], v[64:67], v[88:91], v[20:23]\n"
    "ds_read_b64_tr_b16 v[64:65], v105 offset:30720\n"
    "ds_read_b64_tr_b16 v[66:67], v105 offset:35840\n"
    "s_waitcnt lgkmcnt(11)\n"
    "v_mfma_f32_16x16x32_bf16 v[24:27], v[68:71], v[80:83], v[24:27]\n"
    "buffer_load_dwordx4 v113, s[44:47], s65 offen offset:1024 sc1 lds\n"
    "v_mfma_f32_16x16x32_bf16 v[28:31], v[68:71], v[84:87], v[28:31]\n"
    "v_mfma_f32_16x16x32_bf16 v[32:35], v[68:71], v[88:91], v[32:35]\n"
    "ds_read_b64_tr_b16 v[68:69], v104 offset:30784\n"
    "ds_read_b64_tr_b16 v[70:71], v104 offset:35904\n"
    "buffer_load_dwordx4 v113, s[44:47], s65 offen offset:2048 sc1 lds\n"
    "s_waitcnt lgkmcnt(11)\n"
    "v_mfma_f32_16x16x32_bf16 v[36:39], v[72:75], v[80:83], v[36:39]\n"
    "v_mfma_f32_16x16x32_bf16 v[40:43], v[72:75], v[84:87], v[40:43]\n"
    "v_mfma_f32_16x16x32_bf16 v[44:47], v[72:75], v[88:91], v[44:47]\n"
    "ds_read_b64_tr_b16 v[72:73], v105 offset:30784\n"
    "ds_read_b64_tr_b16 v[74:75], v105 offset:35904\n"
    "s_waitcnt lgkmcnt(11)\n"
    "v_mfma_f32_16x16x32_bf16 v[48:51], v[76:79], v[80:83], v[48:51]\n"
    "v_mfma_f32_16x16x32_bf16 v[52:55], v[76:79], v[84:87], v[52:55]\n"
    "v_mfma_f32_16x16x32_bf16 v[56:59], v[76:79], v[88:91], v[56:59]\n"
    "ds_read_b64_tr_b16 v[76:77], v104 offset:30848\n"
    "ds_read_b64_tr_b16 v[78:79], v104 offset:35968\n"
    "s_waitcnt lgkmcnt(9)\n"
    "v_mfma_f32_16x16x32_bf16 v[0:3], v[60:63], v[92:95], v[0:3]\n"
    "v_mfma_f32_16x16x32_bf16 v[4:7], v[60:63], v[96:99], v[4:7]\n"
    "s_waitcnt lgkmcnt(8)\n"
    "v_mfma_f32_16x16x32_bf16 v[8:11], v[60:63], v[100:103], v[8:11]\n"
    "s_waitcnt lgkmcnt(6)\n"
    "v_mfma_f32_16x16x32_bf16 v[12:15], v[64:67], v[92:95], v[12:15]\n"
    "v_mfma_f32_16x16x32_bf16 v[16:19], v[64:67], v[96:99], v[16:19]\n"
    "v_mfma_f32_16x16x32_bf16 v[20:23], v[64:67], v[100:103], v[20:23]\n"
    "s_waitcnt vmcnt(17) lgkmcnt(0)\n"
    "s_barrier\n"
    "ds_read_b64_tr_b16 v[60:61], v104 offset:40960\n"
    "ds_read_b64_tr_b16 v[62:63], v104 offset:46080\n"
    "v_mfma_f32_16x16x32_bf16 v[24:27], v[68:71], v[92:95], v[24:27]\n"
    "ds_read_b128 v[80:83], v107 offset:24576\n"
    "ds_read_b64_tr_b16 v[64:65], v105 offset:40960\n"
    "ds_read_b64_tr_b16 v[66:67], v105 offset:46080\n"
    "s_add_u32 m0, s52, 20480\n"
    "s_add_u32 s64, s59, 245760\n"
    "buffer_load_dwordx4 v108, s[40:43], s64 offen offset:0 lds\n"
    "v_mfma_f32_16x16x32_bf16 v[28:31], v[68:71], v[96:99], v[28:31]\n"
    "ds_read_b128 v[84:87], v107 offset:25600\n"
    "buffer_load_dwordx4 v109, s[40:43], s64 offen offset:1024 lds\n"
    "v_mfma_f32_16x16x32_bf16 v[32:35], v[68:71], v[100:103], v[32:35]\n"
    "ds_read_b128 v[88:91], v107 offset:26624\n"
    "ds_read_b64_tr_b16 v[68:69], v104 offset:41024\n"
    "ds_read_b64_tr_b16 v[70:71], v104 offset:46144\n"
    "buffer_load_dwordx4 v110, s[40:43], s64 offen offset:2048 lds\n"
    "v_mfma_f32_16x16x32_bf16 v[36:39], v[72:75], v[92:95], v[36:39]\n"
    "v_mfma_f32_16x16x32_bf16 v[40:43], v[72:75], v[96:99], v[40:43]\n"
    "buffer_load_dwordx4 v111, s[40:43], s64 offen offset:3072 lds\n"
    "s_add_u32 m0, s52, 24576\n"
    "v_mfma_f32_16x16x32_bf16 v[44:47], v[72:75], v[100:103], v[44:47]\n"
    "ds_read_b64_tr_b16 v[72:73], v105 offset:41024\n"
    "ds_read_b64_tr_b16 v[74:75], v105 offset:46144\n"
    "v_mfma_f32_16x16x32_bf16 v[48:51], v[76:79], v[92:95], v[48:51]\n"
    "buffer_load_dwordx4 v112, s[40:43], s64 offen offset:0 lds\n"
    "v_mfma_f32_16x16x32_bf16 v[52:55], v[76:79], v[96:99], v[52:55]\n"
    "v_mfma_f32_16x16x32_bf16 v[56:59], v[76:79], v[100:103], v[56:59]\n"
    "ds_read_b64_tr_b16 v[76:77], v104 offset:41088\n"
    "ds_read_b64_tr_b16 v[78:79], v104 offset:46208\n"
    "s_add_u32 m0, s53, 49152\n"
    "s_add_u32 s65, s56, 73728\n"
    "buffer_load_dwordx4 v113, s[44:47], s65 offen offset:0 sc1 lds\n"
    "s_waitcnt lgkmcnt(10)\n"
    "v_mfma_f32_16x16x32_bf16 v[0:3], v[60:63], v[80:83], v[0:3]\n"
    "ds_read_b128 v[92:95], v107 offset:36864\n"
    "buffer_load_dwordx4 v113, s[44:47], s65 offen offset:1024 sc1 lds\n"
    "s_waitcnt lgkmcnt(8)\n"
    "v_mfma_f32_16x16x32_bf16 v[4:7], v[60:63], v[84:87], v[4:7]\n"
    "ds_read_b128 v[96:99], v107 offset:37888\n"
    "s_waitcnt lgkmcnt(8)\n"
    "v_mfma_f32_16x16x32_bf16 v[8:11], v[60:63], v[88:91], v[8:11]\n"
    "ds_read_b64_tr_b16 v[60:61], v104 offset:51200\n"
    "ds_read_b64_tr_b16 v[62:63], v104 offset:56320\n"
    "buffer_load_dwordx4 v113, s[44:47], s65 offen offset:2048 sc1 lds\n"
    "v_mfma_f32_16x16x32_bf16 v[12:15], v[64:67], v[80:83], v[12:15]\n"
    "ds_read_b128 v[100:103], v107 offset:38912\n"
    "v_mfma_f32_16x16x32_bf16 v[16:19], v[64:67], v[84:87], v[16:19]\n"
    "s_add_u32 m0, s53, 52224\n"
    "s_add_u32 s65, s56, 76800\n"
    "buffer_load_dwordx4 v113, s[44:47], s65 offen offset:0 sc1 lds\n"
    "v_mfma_f32_16x16x32_bf16 v[20:23], v[64:67], v[88:91], v[20:23]\n"
    "ds_read_b64_tr_b16 v[64:65], v105 offset:51200\n"
    "ds_read_b64_tr_b16 v[66:67], v105 offset:56320\n"
    "s_waitcnt lgkmcnt(11)\n"
    "v_mfma_f32_16x16x32_bf16 v[24:27], v[68:71], v[80:83], v[24:27]\n"
    "buffer_load_dwordx4 v113, s[44:47], s65 offen offset:1024 sc1 lds\n"
    "v_mfma_f32_16x16x32_bf16 v[28:31], v[68:71], v[84:87], v[28:31]\n"
    "v_mfma_f32_16x16x32_bf16 v[32:35], v[68:71], v[88:91], v[32:35]\n"
    "ds_read_b64_tr_b16 v[68:69], v104 offset:51264\n"
    "ds_read_b64_tr_b16 v[70:71], v104 offset:56384\n"
    "buffer_load_dwordx4 v113, s[44:47], s65 offen offset:2048 sc1 lds\n"
    "s_waitcnt lgkmcnt(11)\n"
    "v_mfma_f32_16x16x32_bf16 v[36:39], v[72:75], v[80:83], v[36:39]\n"
    "v_mfma_f32_16x16x32_bf16 v[40:43], v[72:75], v[84:87], v[40:43]\n"
    "v_mfma_f32_16x16x32_bf16 v[44:47], v[72:75], v[88:91], v[44:47]\n"
    "ds_read_b64_tr_b16 v[72:73], v105 offset:51264\n"
    "ds_read_b64_tr_b16 v[74:75], v105 offset:56384\n"
    "s_waitcnt lgkmcnt(11)\n"
    "v_mfma_f32_16x16x32_bf16 v[48:51], v[76:79], v[80:83], v[48:51]\n"
    "v_mfma_f32_16x16x32_bf16 v[52:55], v[76:79], v[84:87], v[52:55]\n"
    "v_mfma_f32_16x16x32_bf16 v[56:59], v[76:79], v[88:91], v[56:59]\n"
    "ds_read_b64_tr_b16 v[76:77], v104 offset:51328\n"
    "ds_read_b64_tr_b16 v[78:79], v104 offset:56448\n"
    "s_waitcnt lgkmcnt(9)\n"
    "v_mfma_f32_16x16x32_bf16 v[0:3], v[60:63], v[92:95], v[0:3]\n"
    "v_mfma_f32_16x16x32_bf16 v[4:7], v[60:63], v[96:99], v[4:7]\n"
    "s_waitcnt lgkmcnt(8)\n"
    "v_mfma_f32_16x16x32_bf16 v[8:11], v[60:63], v[100:103], v[8:11]\n"
    "s_waitcnt lgkmcnt(6)\n"
    "v_mfma_f32_16x16x32_bf16 v[12:15], v[64:67], v[92:95], v[12:15]\n"
    "v_mfma_f32_16x16x32_bf16 v[16:19], v[64:67], v[96:99], v[16:19]\n"
    "v_mfma_f32_16x16x32_bf16 v[20:23], v[64:67], v[100:103], v[20:23]\n"
    "s_waitcnt vmcnt(17) lgkmcnt(0)\n"
    "s_barrier\n"
    "ds_read_b64_tr_b16 v[60:61], v104 offset:0\n"
    "ds_read_b64_tr_b16 v[62:63], v104 offset:5120\n"
    "v_mfma_f32_16x16x32_bf16 v[24:27], v[68:71], v[92:95], v[24:27]\n"
    "ds_read_b128 v[80:83], v106 offset:0\n"
    "ds_read_b64_tr_b16 v[64:65], v105 offset:0\n"
    "ds_read_b64_tr_b16 v[66:67], v105 offset:5120\n"
    "s_add_u32 m0, s52, 40960\n"
    "s_add_u32 s64, s60, 245760\n"
    "buffer_load_dwordx4 v108, s[40:43], s64 offen offset:0 lds\n"
    "v_mfma_f32_16x16x32_bf16 v[28:31], v[68:71], v[96:99], v[28:31]\n"
    "ds_read_b128 v[84:87], v106 offset:1024\n"
    "buffer_load_dwordx4 v109, s[40:43], s64 offen offset:1024 lds\n"
    "v_mfma_f32_16x16x32_bf16 v[32:35], v[68:71], v[100:103], v[32:35]\n"
    "ds_read_b128 v[88:91], v106 offset:2048\n"
    "ds_read_b64_tr_b16 v[68:69], v104 offset:64\n"
    "ds_read_b64_tr_b16 v[70:71], v104 offset:5184\n"
    "buffer_load_dwordx4 v110, s[40:43], s64 offen offset:2048 lds\n"
    "v_mfma_f32_16x16x32_bf16 v[36:39], v[72:75], v[92:95], v[36:39]\n"
    "v_mfma_f32_16x16x32_bf16 v[40:43], v[72:75], v[96:99], v[40:43]\n"
    "buffer_load_dwordx4 v111, s[40:43], s64 offen offset:3072 lds\n"
    "s_add_u32 m0, s52, 45056\n"
    "v_mfma_f32_16x16x32_bf16 v[44:47], v[72:75], v[100:103], v[44:47]\n"
    "ds_read_b64_tr_b16 v[72:73], v105 offset:64\n"
    "ds_read_b64_tr_b16 v[74:75], v105 offset:5184\n"
    "v_mfma_f32_16x16x32_bf16 v[48:51], v[76:79], v[92:95], v[48:51]\n"
    "buffer_load_dwordx4 v112, s[40:43], s64 offen offset:0 lds\n"
    "v_mfma_f32_16x16x32_bf16 v[52:55], v[76:79], v[96:99], v[52:55]\n"
    "v_mfma_f32_16x16x32_bf16 v[56:59], v[76:79], v[100:103], v[56:59]\n"
    "ds_read_b64_tr_b16 v[76:77], v104 offset:128\n"
    "ds_read_b64_tr_b16 v[78:79], v104 offset:5248\n"
    "s_add_u32 m0, s53, 73728\n"
    "s_add_u32 s65, s57, 73728\n"
    "buffer_load_dwordx4 v113, s[44:47], s65 offen offset:0 sc1 lds\n"
    "s_waitcnt lgkmcnt(10)\n"
    "v_mfma_f32_16x16x32_bf16 v[0:3], v[60:63], v[80:83], v[0:3]\n"
    "ds_read_b128 v[92:95], v106 offset:12288\n"
    "buffer_load_dwordx4 v113, s[44:47], s65 offen offset:1024 sc1 lds\n"
    "s_waitcnt lgkmcnt(8)\n"
    "v_mfma_f32_16x16x32_bf16 v[4:7], v[60:63], v[84:87], v[4:7]\n"
    "ds_read_b128 v[96:99], v106 offset:13312\n"
    "s_waitcnt lgkmcnt(8)\n"
    "v_mfma_f32_16x16x32_bf16 v[8:11], v[60:63], v[88:91], v[8:11]\n"
    "ds_read_b64_tr_b16 v[60:61], v104 offset:10240\n"
    "ds_read_b64_tr_b16 v[62:63], v104 offset:15360\n"
    "buffer_load_dwordx4 v113, s[44:47], s65 offen offset:2048 sc1 lds\n"
    "v_mfma_f32_16x16x32_bf16 v[12:15], v[64:67], v[80:83], v[12:15]\n"
    "ds_read_b128 v[100:103], v106 offset:14336\n"
    "v_mfma_f32_16x16x32_bf16 v[16:19], v[64:67], v[84:87], v[16:19]\n"
    "s_add_u32 m0, s53, 76800\n"
    "s_add_u32 s65, s57, 76800\n"
    "buffer_load_dwordx4 v113, s[44:47], s65 offen offset:0 sc1 lds\n"
    "v_mfma_f32_16x16x32_bf16 v[20:23], v[64:67], v[88:91], v[20:23]\n"
    "ds_read_b64_tr_b16 v[64:65], v105 offset:10240\n"
    "ds_read_b64_tr_b16 v[66:67], v105 offset:15360\n"
    "s_waitcnt lgkmcnt(11)\n"
    "v_mfma_f32_16x16x32_bf16 v[24:27], v[68:71], v[80:83], v[24:27]\n"
    "buffer_load_dwordx4 v113, s[44:47], s65 offen offset:1024 sc1 lds\n"
    "v_mfma_f32_16x16x32_bf16 v[28:31], v[68:71], v[84:87], v[28:31]\n"
    "v_mfma_f32_16x16x32_bf16 v[32:35], v[68:71], v[88:91], v[32:35]\n"
    "ds_read_b64_tr_b16 v[68:69], v104 offset:10304\n"
    "ds_read_b64_tr_b16 v[70:71], v104 offset:15424\n"
    "buffer_load_dwordx4 v113, s[44:47], s65 offen offset:2048 sc1 lds\n"
    "s_waitcnt lgkmcnt(11)\n"
    "v_mfma_f32_16x16x32_bf16 v[36:39], v[72:75], v[80:83], v[36:39]\n"
    "v_mfma_f32_16x16x32_bf16 v[40:43], v[72:75], v[84:87], v[40:43]\n"
    "v_mfma_f32_16x16x32_bf16 v[44:47], v[72:75], v[88:91], v[44:47]\n"
    "ds_read_b64_tr_b16 v[72:73], v105 offset:10304\n"
    "ds_read_b64_tr_b16 v[74:75], v105 offset:15424\n"
    "s_waitcnt lgkmcnt(11)\n"
    "v_mfma_f32_16x16x32_bf16 v[48:51], v[76:79], v[80:83], v[48:51]\n"
    "v_mfma_f32_16x16x32_bf16 v[52:55], v[76:79], v[84:87], v[52:55]\n"
    "v_mfma_f32_16x16x32_bf16 v[56:59], v[76:79], v[88:91], v[56:59]\n"
    "ds_read_b64_tr_b16 v[76:77], v104 offset:10368\n"
    "ds_read_b64_tr_b16 v[78:79], v104 offset:15488\n"
    "s_waitcnt lgkmcnt(9)\n"
    "v_mfma_f32_16x16x32_bf16 v[0:3], v[60:63], v[92:95], v[0:3]\n"
    "v_mfma_f32_16x16x32_bf16 v[4:7], v[60:63], v[96:99], v[4:7]\n"
    "s_waitcnt lgkmcnt(8)\n"
    "v_mfma_f32_16x16x32_bf16 v[8:11], v[60:63], v[100:103], v[8:11]\n"
    "s_waitcnt lgkmcnt(6)\n"
    "v_mfma_f32_16x16x32_bf16 v[12:15], v[64:67], v[92:95], v[12:15]\n"
    "v_mfma_f32_16x16x32_bf16 v[16:19], v[64:67], v[96:99], v[16:19]\n"
    "v_mfma_f32_16x16x32_bf16 v[20:23], v[64:67], v[100:103], v[20:23]\n"
    "s_waitcnt vmcnt(17) lgkmcnt(0)\n"
    "s_barrier\n"
    "ds_read_b64_tr_b16 v[60:61], v104 offset:20480\n"
    "ds_read_b64_tr_b16 v[62:63], v104 offset:25600\n"
    "v_mfma_f32_16x16x32_bf16 v[24:27], v[68:71], v[92:95], v[24:27]\n"
    "ds_read_b128 v[80:83], v106 offset:24576\n"
    "ds_read_b64_tr_b16 v[64:65], v105 offset:20480\n"
    "ds_read_b64_tr_b16 v[66:67], v105 offset:25600\n"
    "s_add_u32 m0, s52, 0\n"
    "s_add_u32 s64, s61, 245760\n"
    "buffer_load_dwordx4 v108, s[40:43], s64 offen offset:0 lds\n"
    "v_mfma_f32_16x16x32_bf16 v[28:31], v[68:71], v[96:99], v[28:31]\n"
    "ds_read_b128 v[84:87], v106 offset:25600\n"
    "v_mfma_f32_16x16x32_bf16 v[32:35], v[68:71], v[100:103], v[32:35]\n"
    "ds_read_b128 v[88:91], v106 offset:26624\n"
    "ds_read_b64_tr_b16 v[68:69], v104 offset:20544\n"
    "ds_read_b64_tr_b16 v[70:71], v104 offset:25664\n"
    "buffer_load_dwordx4 v109, s[40:43], s64 offen offset:1024 lds\n"
    "v_mfma_f32_16x16x32_bf16 v[36:39], v[72:75], v[92:95], v[36:39]\n"
    "v_mfma_f32_16x16x32_bf16 v[40:43], v[72:75], v[96:99], v[40:43]\n"
    "buffer_load_dwordx4 v110, s[40:43], s64 offen offset:2048 lds\n"
    "v_mfma_f32_16x16x32_bf16 v[44:47], v[72:75], v[100:103], v[44:47]\n"
    "ds_read_b64_tr_b16 v[72:73], v105 offset:20544\n"
    "ds_read_b64_tr_b16 v[74:75], v105 offset:25664\n"
    "v_mfma_f32_16x16x32_bf16 v[48:51], v[76:79], v[92:95], v[48:51]\n"
    "buffer_load_dwordx4 v111, s[40:43], s64 offen offset:3072 lds\n"
    "s_add_u32 m0, s52, 4096\n"
    "v_mfma_f32_16x16x32_bf16 v[52:55], v[76:79], v[96:99], v[52:55]\n"
    "v_mfma_f32_16x16x32_bf16 v[56:59], v[76:79], v[100:103], v[56:59]\n"
    "ds_read_b64_tr_b16 v[76:77], v104 offset:20608\n"
    "ds_read_b64_tr_b16 v[78:79], v104 offset:25728\n"
    "buffer_load_dwordx4 v112, s[40:43], s64 offen offset:0 lds\n"
    "s_waitcnt lgkmcnt(10)\n"
    "v_mfma_f32_16x16x32_bf16 v[0:3], v[60:63], v[80:83], v[0:3]\n"
    "ds_read_b128 v[92:95], v106 offset:36864\n"
    "s_waitcnt lgkmcnt(8)\n"
    "v_mfma_f32_16x16x32_bf16 v[4:7], v[60:63], v[84:87], v[4:7]\n"
    "ds_read_b128 v[96:99], v106 offset:37888\n"
    "s_waitcnt lgkmcnt(8)\n"
    "v_mfma_f32_16x16x32_bf16 v[8:11], v[60:63], v[88:91], v[8:11]\n"
    "ds_read_b64_tr_b16 v[60:61], v104 offset:30720\n"
    "ds_read_b64_tr_b16 v[62:63], v104 offset:35840\n"
    "v_mfma_f32_16x16x32_bf16 v[12:15], v[64:67], v[80:83], v[12:15]\n"
    "ds_read_b128 v[100:103], v106 offset:38912\n"
    "v_mfma_f32_16x16x32_bf16 v[16:19], v[64:67], v[84:87], v[16:19]\n"
    "v_mfma_f32_16x16x32_bf16 v[20:23], v[64:67], v[88:91], v[20:23]\n"
    "ds_read_b64_tr_b16 v[64:65], v105 offset:30720\n"
    "ds_read_b64_tr_b16 v[66:67], v105 offset:35840\n"
    "s_waitcnt lgkmcnt(11)\n"
    "v_mfma_f32_16x16x32_bf16 v[24:27], v[68:71], v[80:83], v[24:27]\n"
    "v_mfma_f32_16x16x32_bf16 v[28:31], v[68:71], v[84:87], v[28:31]\n"
    "v_mfma_f32_16x16x32_bf16 v[32:35], v[68:71], v[88:91], v[32:35]\n"
    "ds_read_b64_tr_b16 v[68:69], v104 offset:30784\n"
    "ds_read_b64_tr_b16 v[70:71], v104 offset:35904\n"
    "s_waitcnt lgkmcnt(11)\n"
    "v_mfma_f32_16x16x32_bf16 v[36:39], v[72:75], v[80:83], v[36:39]\n"
    "v_mfma_f32_16x16x32_bf16 v[40:43], v[72:75], v[84:87], v[40:43]\n"
    "v_mfma_f32_16x16x32_bf16 v[44:47], v[72:75], v[88:91], v[44:47]\n"
    "ds_read_b64_tr_b16 v[72:73], v105 offset:30784\n"
    "ds_read_b64_tr_b16 v[74:75], v105 offset:35904\n"
    "s_waitcnt lgkmcnt(11)\n"
    "v_mfma_f32_16x16x32_bf16 v[48:51], v[76:79], v[80:83], v[48:51]\n"
    "v_mfma_f32_16x16x32_bf16 v[52:55], v[76:79], v[84:87], v[52:55]\n"
    "v_mfma_f32_16x16x32_bf16 v[56:59], v[76:79], v[88:91], v[56:59]\n"
    "ds_read_b64_tr_b16 v[76:77], v104 offset:30848\n"
    "ds_read_b64_tr_b16 v[78:79], v104 offset:35968\n"
    "s_waitcnt lgkmcnt(9)\n"
    "v_mfma_f32_16x16x32_bf16 v[0:3], v[60:63], v[92:95], v[0:3]\n"
    "v_mfma_f32_16x16x32_bf16 v[4:7], v[60:63], v[96:99], v[4:7]\n"
    "s_waitcnt lgkmcnt(8)\n"
    "v_mfma_f32_16x16x32_bf16 v[8:11], v[60:63], v[100:103], v[8:11]\n"
    "s_waitcnt lgkmcnt(6)\n"
    "v_mfma_f32_16x16x32_bf16 v[12:15], v[64:67], v[92:95], v[12:15]\n"
    "v_mfma_f32_16x16x32_bf16 v[16:19], v[64:67], v[96:99], v[16:19]\n"
    "v_mfma_f32_16x16x32_bf16 v[20:23], v[64:67], v[100:103], v[20:23]\n"
    "s_waitcnt vmcnt(11) lgkmcnt(0)\n"
    "s_barrier\n"
    "ds_read_b64_tr_b16 v[60:61], v104 offset:40960\n"
    "ds_read_b64_tr_b16 v[62:63], v104 offset:46080\n"
    "v_mfma_f32_16x16x32_bf16 v[24:27], v[68:71], v[92:95], v[24:27]\n"
    "ds_read_b128 v[80:83], v107 offset:0\n"
    "ds_read_b64_tr_b16 v[64:65], v105 offset:40960\n"
    "ds_read_b64_tr_b16 v[66:67], v105 offset:46080\n"
    "v_mfma_f32_16x16x32_bf16 v[28:31], v[68:71], v[96:99], v[28:31]\n"
    "ds_read_b128 v[84:87], v107 offset:1024\n"
    "v_mfma_f32_16x16x32_bf16 v[32:35], v[68:71], v[100:103], v[32:35]\n"
    "ds_read_b128 v[88:91], v107 offset:2048\n"
    "ds_read_b64_tr_b16 v[68:69], v104 offset:41024\n"
    "ds_read_b64_tr_b16 v[70:71], v104 offset:46144\n"
    "v_mfma_f32_16x16x32_bf16 v[36:39], v[72:75], v[92:95], v[36:39]\n"
    "v_mfma_f32_16x16x32_bf16 v[40:43], v[72:75], v[96:99], v[40:43]\n"
    "v_mfma_f32_16x16x32_bf16 v[44:47], v[72:75], v[100:103], v[44:47]\n"
    "ds_read_b64_tr_b16 v[72:73], v105 offset:41024\n"
    "ds_read_b64_tr_b16 v[74:75], v105 offset:46144\n"
    "v_mfma_f32_16x16x32_bf16 v[48:51], v[76:79], v[92:95], v[48:51]\n"
    "v_mfma_f32_16x16x32_bf16 v[52:55], v[76:79], v[96:99], v[52:55]\n"
    "v_mfma_f32_16x16x32_bf16 v[56:59], v[76:79], v[100:103], v[56:59]\n"
    "ds_read_b64_tr_b16 v[76:77], v104 offset:41088\n"
    "ds_read_b64_tr_b16 v[78:79], v104 offset:46208\n"
    "s_waitcnt lgkmcnt(10)\n"
    "v_mfma_f32_16x16x32_bf16 v[0:3], v[60:63], v[80:83], v[0:3]\n"
    "ds_read_b128 v[92:95], v107 offset:12288\n"
    "s_waitcnt lgkmcnt(8)\n"
    "v_mfma_f32_16x16x32_bf16 v[4:7], v[60:63], v[84:87], v[4:7]\n"
    "ds_read_b128 v[96:99], v107 offset:13312\n"
    "s_waitcnt lgkmcnt(8)\n"
    "v_mfma_f32_16x16x32_bf16 v[8:11], v[60:63], v[88:91], v[8:11]\n"
    "ds_read_b64_tr_b16 v[60:61], v104 offset:51200\n"
    "ds_read_b64_tr_b16 v[62:63], v104 offset:56320\n"
    "v_mfma_f32_16x16x32_bf16 v[12:15], v[64:67], v[80:83], v[12:15]\n"
    "ds_read_b128 v[100:103], v107 offset:14336\n"
    "v_mfma_f32_16x16x32_bf16 v[16:19], v[64:67], v[84:87], v[16:19]\n"
    "v_mfma_f32_16x16x32_bf16 v[20:23], v[64:67], v[88:91], v[20:23]\n"
    "ds_read_b64_tr_b16 v[64:65], v105 offset:51200\n"
    "ds_read_b64_tr_b16 v[66:67], v105 offset:56320\n"
    "s_waitcnt lgkmcnt(11)\n"
    "v_mfma_f32_16x16x32_bf16 v[24:27], v[68:71], v[80:83], v[24:27]\n"
    "v_mfma_f32_16x16x32_bf16 v[28:31], v[68:71], v[84:87], v[28:31]\n"
    "v_mfma_f32_16x16x32_bf16 v[32:35], v[68:71], v[88:91], v[32:35]\n"
    "ds_read_b64_tr_b16 v[68:69], v104 offset:51264\n"
    "ds_read_b64_tr_b16 v[70:71], v104 offset:56384\n"
    "s_waitcnt lgkmcnt(11)\n"
    "v_mfma_f32_16x16x32_bf16 v[36:39], v[72:75], v[80:83], v[36:39]\n"
    "v_mfma_f32_16x16x32_bf16 v[40:43], v[72:75], v[84:87], v[40:43]\n"
    "v_mfma_f32_16x16x32_bf16 v[44:47], v[72:75], v[88:91], v[44:47]\n"
    "ds_read_b64_tr_b16 v[72:73], v105 offset:51264\n"
    "ds_read_b64_tr_b16 v[74:75], v105 offset:56384\n"
    "s_waitcnt lgkmcnt(11)\n"
    "v_mfma_f32_16x16x32_bf16 v[48:51], v[76:79], v[80:83], v[48:51]\n"
    "v_mfma_f32_16x16x32_bf16 v[52:55], v[76:79], v[84:87], v[52:55]\n"
    "v_mfma_f32_16x16x32_bf16 v[56:59], v[76:79], v[88:91], v[56:59]\n"
    "ds_read_b64_tr_b16 v[76:77], v104 offset:51328\n"
    "ds_read_b64_tr_b16 v[78:79], v104 offset:56448\n"
    "s_waitcnt lgkmcnt(9)\n"
    "v_mfma_f32_16x16x32_bf16 v[0:3], v[60:63], v[92:95], v[0:3]\n"
    "v_mfma_f32_16x16x32_bf16 v[4:7], v[60:63], v[96:99], v[4:7]\n"
    "s_waitcnt lgkmcnt(8)\n"
    "v_mfma_f32_16x16x32_bf16 v[8:11], v[60:63], v[100:103], v[8:11]\n"
    "s_waitcnt lgkmcnt(6)\n"
    "v_mfma_f32_16x16x32_bf16 v[12:15], v[64:67], v[92:95], v[12:15]\n"
    "v_mfma_f32_16x16x32_bf16 v[16:19], v[64:67], v[96:99], v[16:19]\n"
    "v_mfma_f32_16x16x32_bf16 v[20:23], v[64:67], v[100:103], v[20:23]\n"
    "s_waitcnt vmcnt(0) lgkmcnt(0)\n"
    "s_barrier\n"
    "ds_read_b64_tr_b16 v[60:61], v104 offset:0\n"
    "ds_read_b64_tr_b16 v[62:63], v104 offset:5120\n"
    "v_mfma_f32_16x16x32_bf16 v[24:27], v[68:71], v[92:95], v[24:27]\n"
    "ds_read_b128 v[80:83], v107 offset:24576\n"
    "ds_read_b64_tr_b16 v[64:65], v105 offset:0\n"
    "ds_read_b64_tr_b16 v[66:67], v105 offset:5120\n"
    "v_mfma_f32_16x16x32_bf16 v[28:31], v[68:71], v[96:99], v[28:31]\n"
    "ds_read_b128 v[84:87], v107 offset:25600\n"
    "v_mfma_f32_16x16x32_bf16 v[32:35], v[68:71], v[100:103], v[32:35]\n"
    "ds_read_b128 v[88:91], v107 offset:26624\n"
    "ds_read_b64_tr_b16 v[68:69], v104 offset:64\n"
    "ds_read_b64_tr_b16 v[70:71], v104 offset:5184\n"
    "v_mfma_f32_16x16x32_bf16 v[36:39], v[72:75], v[92:95], v[36:39]\n"
    "v_mfma_f32_16x16x32_bf16 v[40:43], v[72:75], v[96:99], v[40:43]\n"
    "v_mfma_f32_16x16x32_bf16 v[44:47], v[72:75], v[100:103], v[44:47]\n"
    "ds_read_b64_tr_b16 v[72:73], v105 offset:64\n"
    "ds_read_b64_tr_b16 v[74:75], v105 offset:5184\n"
    "v_mfma_f32_16x16x32_bf16 v[48:51], v[76:79], v[92:95], v[48:51]\n"
    "v_mfma_f32_16x16x32_bf16 v[52:55], v[76:79], v[96:99], v[52:55]\n"
    "v_mfma_f32_16x16x32_bf16 v[56:59], v[76:79], v[100:103], v[56:59]\n"
    "ds_read_b64_tr_b16 v[76:77], v104 offset:128\n"
    "ds_read_b64_tr_b16 v[78:79], v104 offset:5248\n"
    "s_waitcnt lgkmcnt(10)\n"
    "v_mfma_f32_16x16x32_bf16 v[0:3], v[60:63], v[80:83], v[0:3]\n"
    "ds_read_b128 v[92:95], v107 offset:36864\n"
    "s_waitcnt lgkmcnt(8)\n"
    "v_mfma_f32_16x16x32_bf16 v[4:7], v[60:63], v[84:87], v[4:7]\n"
    "ds_read_b128 v[96:99], v107 offset:37888\n"
    "s_waitcnt lgkmcnt(8)\n"
    "v_mfma_f32_16x16x32_bf16 v[8:11], v[60:63], v[88:91], v[8:11]\n"
    "ds_read_b64_tr_b16 v[60:61], v104 offset:10240\n"
    "ds_read_b64_tr_b16 v[62:63], v104 offset:15360\n"
    "v_mfma_f32_16x16x32_bf16 v[12:15], v[64:67], v[80:83], v[12:15]\n"
    "ds_read_b128 v[100:103], v107 offset:38912\n"
    "v_mfma_f32_16x16x32_bf16 v[16:19], v[64:67], v[84:87], v[16:19]\n"
    "v_mfma_f32_16x16x32_bf16 v[20:23], v[64:67], v[88:91], v[20:23]\n"
    "ds_read_b64_tr_b16 v[64:65], v105 offset:10240\n"
    "ds_read_b64_tr_b16 v[66:67], v105 offset:15360\n"
    "s_waitcnt lgkmcnt(11)\n"
    "v_mfma_f32_16x16x32_bf16 v[24:27], v[68:71], v[80:83], v[24:27]\n"
    "v_mfma_f32_16x16x32_bf16 v[28:31], v[68:71], v[84:87], v[28:31]\n"
    "v_mfma_f32_16x16x32_bf16 v[32:35], v[68:71], v[88:91], v[32:35]\n"
    "ds_read_b64_tr_b16 v[68:69], v104 offset:10304\n"
    "ds_read_b64_tr_b16 v[70:71], v104 offset:15424\n"
    "s_waitcnt lgkmcnt(11)\n"
    "v_mfma_f32_16x16x32_bf16 v[36:39], v[72:75], v[80:83], v[36:39]\n"
    "v_mfma_f32_16x16x32_bf16 v[40:43], v[72:75], v[84:87], v[40:43]\n"
    "v_mfma_f32_16x16x32_bf16 v[44:47], v[72:75], v[88:91], v[44:47]\n"
    "ds_read_b64_tr_b16 v[72:73], v105 offset:10304\n"
    "ds_read_b64_tr_b16 v[74:75], v105 offset:15424\n"
    "s_waitcnt lgkmcnt(11)\n"
    "v_mfma_f32_16x16x32_bf16 v[48:51], v[76:79], v[80:83], v[48:51]\n"
    "v_mfma_f32_16x16x32_bf16 v[52:55], v[76:79], v[84:87], v[52:55]\n"
    "v_mfma_f32_16x16x32_bf16 v[56:59], v[76:79], v[88:91], v[56:59]\n"
    "ds_read_b64_tr_b16 v[76:77], v104 offset:10368\n"
    "ds_read_b64_tr_b16 v[78:79], v104 offset:15488\n"
    "s_waitcnt lgkmcnt(9)\n"
    "v_mfma_f32_16x16x32_bf16 v[0:3], v[60:63], v[92:95], v[0:3]\n"
    "v_mfma_f32_16x16x32_bf16 v[4:7], v[60:63], v[96:99], v[4:7]\n"
    "s_waitcnt lgkmcnt(8)\n"
    "v_mfma_f32_16x16x32_bf16 v[8:11], v[60:63], v[100:103], v[8:11]\n"
    "s_waitcnt lgkmcnt(6)\n"
    "v_mfma_f32_16x16x32_bf16 v[12:15], v[64:67], v[92:95], v[12:15]\n"
    "v_mfma_f32_16x16x32_bf16 v[16:19], v[64:67], v[96:99], v[16:19]\n"
    "v_mfma_f32_16x16x32_bf16 v[20:23], v[64:67], v[100:103], v[20:23]\n"
    "s_waitcnt lgkmcnt(4)\n"
    "v_mfma_f32_16x16x32_bf16 v[24:27], v[68:71], v[92:95], v[24:27]\n"
    "v_mfma_f32_16x16x32_bf16 v[28:31], v[68:71], v[96:99], v[28:31]\n"
    "v_mfma_f32_16x16x32_bf16 v[32:35], v[68:71], v[100:103], v[32:35]\n"
    "s_waitcnt lgkmcnt(2)\n"
    "v_mfma_f32_16x16x32_bf16 v[36:39], v[72:75], v[92:95], v[36:39]\n"
    "v_mfma_f32_16x16x32_bf16 v[40:43], v[72:75], v[96:99], v[40:43]\n"
    "v_mfma_f32_16x16x32_bf16 v[44:47], v[72:75], v[100:103], v[44:47]\n"
    "s_waitcnt lgkmcnt(0)\n"
    "v_mfma_f32_16x16x32_bf16 v[48:51], v[76:79], v[92:95], v[48:51]\n"
    "v_mfma_f32_16x16x32_bf16 v[52:55], v[76:79], v[96:99], v[52:55]\n"
    "v_mfma_f32_16x16x32_bf16 v[56:59], v[76:79], v[100:103], v[56:59]\n"
    "s_branch .Lend_%=\n"
    ".Lslow1_%=:\n"
    "s_mov_b32 s66, 0x40000\n"
    ".Lsl1_%=:\n"
    "v_mov_b32_e32 v115, v117\n"
    "s_cmp_lg_u32 s63, 4\n"
    "s_cbranch_scc1 .Lnps1_%=\n"
    "v_lshl_or_b32 v118, v115, 8, 1\n"
    "buffer_store_dword v118, v119, s[48:51], 0 offen sc1\n"
    ".Lnps1_%=:\n"
    "s_sleep 2\n"
    "buffer_load_dword v116, v120, s[48:51], 0 offen sc1\n"
    "buffer_load_dword v117, v114, s[48:51], 0 offen sc1\n"
    "s_sub_u32 s66, s66, 1\n"
    "s_cbranch_scc1 .Lok1_%=\n"
    "s_waitcnt vmcnt(0)\n"
    "v_subrev_u32_e32 v118, s62, v116\n"
    "v_subrev_u32_e32 v118, 1, v118\n"
    "v_cmp_lt_u32_e32 vcc, 1, v118\n"
    "s_cbranch_vccnz .Lsl1_%=\n"
    "v_cmp_ne_u32_e32 vcc, v117, v115\n"
    "s_cbranch_vccnz .Lsl1_%=\n"
    "s_branch .Lok1_%=\n"
    ".Lslow5_%=:\n"
    "s_mov_b32 s66, 0x40000\n"
    ".Lsl5_%=:\n"
    "v_mov_b32_e32 v115, v117\n"
    "s_cmp_lg_u32 s63, 4\n"
    "s_cbranch_scc1 .Lnps5_%=\n"
    "v_lshl_or_b32 v118, v115, 8, 2\n"
    "buffer_store_dword v118, v119, s[48:51], 0 offen sc1\n"
    ".Lnps5_%=:\n"
    "s_sleep 2\n"
    "buffer_load_dword v116, v120, s[48:51], 0 offen sc1\n"
    "buffer_load_dword v117, v114, s[48:51], 0 offen sc1\n"
    "s_sub_u32 s66, s66, 1\n"
    "s_cbranch_scc1 .Lok5_%=\n"
    "s_waitcnt vmcnt(0)\n"
    "v_subrev_u32_e32 v118, s62, v116\n"
    "v_subrev_u32_e32 v118, 2, v118\n"
    "v_cmp_lt_u32_e32 vcc, 1, v118\n"
    "s_cbranch_vccnz .Lsl5_%=\n"
    "v_cmp_ne_u32_e32 vcc, v117, v115\n"
    "s_cbranch_vccnz .Lsl5_%=\n"
    "s_branch .Lok5_%=\n"
    ".LS_%=:\n"
    "v_lshrrev_b32_e32 v121, 4, v241\n"
    "v_add_u32_e32 v106, s37, v241\n"
    "v_and_b32_e32 v122, 3, v121\n"
    "v_lshrrev_b32_e32 v123, 2, v121\n"
    "v_mul_u32_u24_e32 v123, 0x140, v123\n"
    "v_lshl_add_u32 v123, v122, 3, v123\n"
    "v_add_u32_e32 v123, s69, v123\n"
    "v_lshlrev_b32_e32 v105, 1, v121\n"
    "v_and_b32_e32 v105, 32, v105\n"
    "v_xor_b32_e32 v104, s36, v105\n"
    "s_add_u32 s64, s36, 32\n"
    "v_add_u32_e32 v104, v104, v123\n"
    "v_xor_b32_e32 v105, s64, v105\n"
    "v_add_u32_e32 v105, v105, v123\n"
    "v_add_u32_e32 v107, 0xc000, v106\n"
    "v_add_u32_e32 v113, s38, v241\n"
    "s_waitcnt lgkmcnt(0)\n"
    "s_barrier\n"
    "ds_read_b128 v[80:83], v106 offset:0\n"
    "ds_read_b128 v[84:87], v106 offset:1024\n"
    "ds_read_b128 v[88:91], v106 offset:2048\n"
    "ds_read_b64_tr_b16 v[60:61], v104 offset:0\n"
    "ds_read_b64_tr_b16 v[62:63], v104 offset:5120\n"
    "ds_read_b64_tr_b16 v[64:65], v105 offset:0\n"
    "ds_read_b64_tr_b16 v[66:67], v105 offset:5120\n"
    "ds_read_b64_tr_b16 v[68:69], v104 offset:64\n"
    "ds_read_b64_tr_b16 v[70:71], v104 offset:5184\n"
    "ds_read_b64_tr_b16 v[72:73], v105 offset:64\n"
    "ds_read_b64_tr_b16 v[74:75], v105 offset:5184\n"
    "ds_read_b64_tr_b16 v[76:77], v104 offset:128\n"
    "ds_read_b64_tr_b16 v[78:79], v104 offset:5248\n"
    "s_waitcnt lgkmcnt(8)\n"
    "v_mfma_f32_16x16x32_bf16 v[0:3], v[60:63], v[80:83], 0\n"
    "ds_read_b128 v[92:95], v106 offset:12288\n"
    "v_mfma_f32_16x16x32_bf16 v[4:7], v[60:63], v[84:87], 0\n"
    "ds_read_b128 v[96:99], v106 offset:13312\n"
    "v_mfma_f32_16x16x32_bf16 v[8:11], v[60:63], v[88:91], 0\n"
    "ds_read_b64_tr_b16 v[60:61], v104 offset:10240\n"
    "ds_read_b64_tr_b16 v[62:63], v104 offset:15360\n"
    "s_waitcnt lgkmcnt(10)\n"
    "v_mfma_f32_16x16x32_bf16 v[12:15], v[64:67], v[80:83], 0\n"
    "ds_read_b128 v[100:103], v106 offset:14336\n"
    "v_mfma_f32_16x16x32_bf16 v[16:19], v[64:67], v[84:87], 0\n"
    "v_mfma_f32_16x16x32_bf16 v[20:23], v[64:67], v[88:91], 0\n"
    "ds_read_b64_tr_b16 v[64:65], v105 offset:10240\n"
    "ds_read_b64_tr_b16 v[66:67], v105 offset:15360\n"
    "s_waitcnt lgkmcnt(11)\n"
    "v_mfma_f32_16x16x32_bf16 v[24:27], v[68:71], v[80:83], 0\n"
    "v_mfma_f32_16x16x32_bf16 v[28:31], v[68:71], v[84:87], 0\n"
    "v_mfma_f32_16x16x32_bf16 v[32:35], v[68:71], v[88:91], 0\n"
    "ds_read_b64_tr_b16 v[68:69], v104 offset:10304\n"
    "ds_read_b64_tr_b16 v[70:71], v104 offset:15424\n"
    "s_waitcnt lgkmcnt(11)\n"
    "v_mfma_f32_16x16x32_bf16 v[36:39], v[72:75], v[80:83], 0\n"
    "v_mfma_f32_16x16x32_bf16 v[40:43], v[72:75], v[84:87], 0\n"
    "v_mfma_f32_16x16x32_bf16 v[44:47], v[72:75], v[88:91], 0\n"
    "ds_read_b64_tr_b16 v[72:73], v105 offset:10304\n"
    "ds_read_b64_tr_b16 v[74:75], v105 offset:15424\n"
    "s_waitcnt lgkmcnt(11)\n"
    "v_mfma_f32_16x16x32_bf16 v[48:51], v[76:79], v[80:83], 0\n"
    "v_mfma_f32_16x16x32_bf16 v[52:55], v[76:79], v[84:87], 0\n"
    "v_mfma_f32_16x16x32_bf16 v[56:59], v[76:79], v[88:91], 0\n"
    "ds_read_b64_tr_b16 v[76:77], v104 offset:10368\n"
    "ds_read_b64_tr_b16 v[78:79], v104 offset:15488\n"
    "s_waitcnt lgkmcnt(9)\n"
    "v_mfma_f32_16x16x32_bf16 v[0:3], v[60:63], v[92:95], v[0:3]\n"
    "v_mfma_f32_16x16x32_bf16 v[4:7], v[60:63], v[96:99], v[4:7]\n"
    "s_waitcnt lgkmcnt(8)\n"
    "v_mfma_f32_16x16x32_bf16 v[8:11], v[60:63], v[100:103], v[8:11]\n"
    "s_waitcnt lgkmcnt(6)\n"
    "v_mfma_f32_16x16x32_bf16 v[12:15], v[64:67], v[92:95], v[12:15]\n"
    "v_mfma_f32_16x16x32_bf16 v[16:19], v[64:67], v[96:99], v[16:19]\n"
    "v_mfma_f32_16x16x32_bf16 v[20:23], v[64:67], v[100:103], v[20:23]\n"
    "s_waitcnt vmcnt(0) lgkmcnt(0)\n"
    "s_barrier\n"
    "ds_read_b64_tr_b16 v[60:61], v104 offset:20480\n"
    "ds_read_b64_tr_b16 v[62:63], v104 offset:25600\n"
    "v_mfma_f32_16x16x32_bf16 v[24:27], v[68:71], v[92:95], v[24:27]\n"
    "ds_read_b128 v[80:83], v106 offset:24576\n"
    "ds_read_b64_tr_b16 v[64:65], v105 offset:20480\n"
    "ds_read_b64_tr_b16 v[66:67], v105 offset:25600\n"
    "v_mfma_f32_16x16x32_bf16 v[28:31], v[68:71], v[96:99], v[28:31]\n"
    "ds_read_b128 v[84:87], v106 offset:25600\n"
    "v_mfma_f32_16x16x32_bf16 v[32:35], v[68:71], v[100:103], v[32:35]\n"
    "ds_read_b128 v[88:91], v106 offset:26624\n"
    "ds_read_b64_tr_b16 v[68:69], v104 offset:20544\n"
    "ds_read_b64_tr_b16 v[70:71], v104 offset:25664\n"
    "v_mfma_f32_16x16x32_bf16 v[36:39], v[72:75], v[92:95], v[36:39]\n"
    "v_mfma_f32_16x16x32_bf16 v[40:43], v[72:75], v[96:99], v[40:43]\n"
    "v_mfma_f32_16x16x32_bf16 v[44:47], v[72:75], v[100:103], v[44:47]\n"
    "ds_read_b64_tr_b16 v[72:73], v105 offset:20544\n"
    "ds_read_b64_tr_b16 v[74:75], v105 offset:25664\n"
    "v_mfma_f32_16x16x32_bf16 v[48:51], v[76:79], v[92:95], v[48:51]\n"
    "v_mfma_f32_16x16x32_bf16 v[52:55], v[76:79], v[96:99], v[52:55]\n"
    "v_mfma_f32_16x16x32_bf16 v[56:59], v[76:79], v[100:103], v[56:59]\n"
    "ds_read_b64_tr_b16 v[76:77], v104 offset:20608\n"
    "ds_read_b64_tr_b16 v[78:79], v104 offset:25728\n"
    "s_waitcnt lgkmcnt(10)\n"
    "v_mfma_f32_16x16x32_bf16 v[0:3], v[60:63], v[80:83], v[0:3]\n"
    "ds_read_b128 v[92:95], v106 offset:36864\n"
    "s_waitcnt lgkmcnt(8)\n"
    "v_mfma_f32_16x16x32_bf16 v[4:7], v[60:63], v[84:87], v[4:7]\n"
    "ds_read_b128 v[96:99], v106 offset:37888\n"
    "s_waitcnt lgkmcnt(8)\n"
    "v_mfma_f32_16x16x32_bf16 v[8:11], v[60:63], v[88:91], v[8:11]\n"
    "ds_read_b64_tr_b16 v[60:61], v104 offset:30720\n"
    "ds_read_b64_tr_b16 v[62:63], v104 offset:35840\n"
    "v_mfma_f32_16x16x32_bf16 v[12:15], v[64:67], v[80:83], v[12:15]\n"
    "s_sleep 1\n"
    "ds_read_b128 v[100:103], v106 offset:38912\n"
    "v_mfma_f32_16x16x32_bf16 v[16:19], v[64:67], v[84:87], v[16:19]\n"
    "v_mfma_f32_16x16x32_bf16 v[20:23], v[64:67], v[88:91], v[20:23]\n"
    "ds_read_b64_tr_b16 v[64:65], v105 offset:30720\n"
    "ds_read_b64_tr_b16 v[66:67], v105 offset:35840\n"
    "s_waitcnt lgkmcnt(11)\n"
    "v_mfma_f32_16x16x32_bf16 v[24:27], v[68:71], v[80:83], v[24:27]\n"
    "v_mfma_f32_16x16x32_bf16 v[28:31], v[68:71], v[84:87], v[28:31]\n"
    "v_mfma_f32_16x16x32_bf16 v[32:35], v[68:71], v[88:91], v[32:35]\n"
    "ds_read_b64_tr_b16 v[68:69], v104 offset:30784\n"
    "ds_read_b64_tr_b16 v[70:71], v104 offset:35904\n"
    "s_waitcnt lgkmcnt(11)\n"
    "v_mfma_f32_16x16x32_bf16 v[36:39], v[72:75], v[80:83], v[36:39]\n"
    "v_mfma_f32_16x16x32_bf16 v[40:43], v[72:75], v[84:87], v[40:43]\n"
    "s_sleep 1\n"
    "v_mfma_f32_16x16x32_bf16 v[44:47], v[72:75], v[88:91], v[44:47]\n"
    "ds_read_b64_tr_b16 v[72:73], v105 offset:30784\n"
    "ds_read_b64_tr_b16 v[74:75], v105 offset:35904\n"
    "s_waitcnt lgkmcnt(11)\n"
    "v_mfma_f32_16x16x32_bf16 v[48:51], v[76:79], v[80:83], v[48:51]\n"
    "v_mfma_f32_16x16x32_bf16 v[52:55], v[76:79], v[84:87], v[52:55]\n"
    "v_mfma_f32_16x16x32_bf16 v[56:59], v[76:79], v[88:91], v[56:59]\n"
    "ds_read_b64_tr_b16 v[76:77], v104 offset:30848\n"
    "ds_read_b64_tr_b16 v[78:79], v104 offset:35968\n"
    "s_waitcnt lgkmcnt(9)\n"
    "v_mfma_f32_16x16x32_bf16 v[0:3], v[60:63], v[92:95], v[0:3]\n"
    "v_mfma_f32_16x16x32_bf16 v[4:7], v[60:63], v[96:99], v[4:7]\n"
    "s_waitcnt lgkmcnt(8)\n"
    "v_mfma_f32_16x16x32_bf16 v[8:11], v[60:63], v[100:103], v[8:11]\n"
    "s_waitcnt lgkmcnt(6)\n"
    "v_mfma_f32_16x16x32_bf16 v[12:15], v[64:67], v[92:95], v[12:15]\n"
    "s_sleep 1\n"
    "v_mfma_f32_16x16x32_bf16 v[16:19], v[64:67], v[96:99], v[16:19]\n"
    "v_mfma_f32_16x16x32_bf16 v[20:23], v[64:67], v[100:103], v[20:23]\n"
    "s_waitcnt lgkmcnt(0)\n"
    "s_barrier\n"
    "ds_read_b64_tr_b16 v[60:61], v104 offset:40960\n"
    "ds_read_b64_tr_b16 v[62:63], v104 offset:46080\n"
    "v_mfma_f32_16x16x32_bf16 v[24:27], v[68:71], v[92:95], v[24:27]\n"
    "ds_read_b128 v[80:83], v107 offset:0\n"
    "ds_read_b64_tr_b16 v[64:65], v105 offset:40960\n"
    "ds_read_b64_tr_b16 v[66:67], v105 offset:46080\n"
    "v_mfma_f32_16x16x32_bf16 v[28:31], v[68:71], v[96:99], v[28:31]\n"
    "ds_read_b128 v[84:87], v107 offset:1024\n"
    "v_mfma_f32_16x16x32_bf16 v[32:35], v[68:71], v[100:103], v[32:35]\n"
    "ds_read_b128 v[88:91], v107 offset:2048\n"
    "ds_read_b64_tr_b16 v[68:69], v104 offset:41024\n"
    "ds_read_b64_tr_b16 v[70:71], v104 offset:46144\n"
    "v_mfma_f32_16x16x32_bf16 v[36:39], v[72:75], v[92:95], v[36:39]\n"
    "v_mfma_f32_16x16x32_bf16 v[40:43], v[72:75], v[96:99], v[40:43]\n"
    "s_sleep 1\n"
    "v_mfma_f32_16x16x32_bf16 v[44:47], v[72:75], v[100:103], v[44:47]\n"
    "ds_read_b64_tr_b16 v[72:73], v105 offset:41024\n"
    "ds_read_b64_tr_b16 v[74:75], v105 offset:46144\n"
    "v_mfma_f32_16x16x32_bf16 v[48:51], v[76:79], v[92:95], v[48:51]\n"
    "v_mfma_f32_16x16x32_bf16 v[52:55], v[76:79], v[96:99], v[52:55]\n"
    "v_mfma_f32_16x16x32_bf16 v[56:59], v[76:79], v[100:103], v[56:59]\n"
    "ds_read_b64_tr_b16 v[76:77], v104 offset:41088\n"
    "ds_read_b64_tr_b16 v[78:79], v104 offset:46208\n"
    "s_waitcnt lgkmcnt(10)\n"
    "v_mfma_f32_16x16x32_bf16 v[0:3], v[60:63], v[80:83], v[0:3]\n"
    "ds_read_b128 v[92:95], v107 offset:12288\n"
    "s_waitcnt lgkmcnt(8)\n"
    "v_mfma_f32_16x16x32_bf16 v[4:7], v[60:63], v[84:87], v[4:7]\n"
    "ds_read_b128 v[96:99], v107 offset:13312\n"
    "s_waitcnt lgkmcnt(8)\n"
    "v_mfma_f32_16x16x32_bf16 v[8:11], v[60:63], v[88:91], v[8:11]\n"
    "ds_read_b64_tr_b16 v[60:61], v104 offset:51200\n"
    "ds_read_b64_tr_b16 v[62:63], v104 offset:56320\n"
    "s_add_u32 s64, s54, 49152\n"
    "buffer_store_dwordx4 v[80:83], v113, s[44:47], s64 offen offset:0 sc1\n"
    "v_mfma_f32_16x16x32_bf16 v[12:15], v[64:67], v[80:83], v[12:15]\n"
    "s_sleep 1\n"
    "ds_read_b128 v[100:103], v107 offset:14336\n"
    "v_mfma_f32_16x16x32_bf16 v[16:19], v[64:67], v[84:87], v[16:19]\n"
    "v_mfma_f32_16x16x32_bf16 v[20:23], v[64:67], v[88:91], v[20:23]\n"
    "ds_read_b64_tr_b16 v[64:65], v105 offset:51200\n"
    "ds_read_b64_tr_b16 v[66:67], v105 offset:56320\n"
    "s_waitcnt lgkmcnt(11)\n"
    "v_mfma_f32_16x16x32_bf16 v[24:27], v[68:71], v[80:83], v[24:27]\n"
    "buffer_store_dwordx4 v[84:87], v113, s[44:47], s64 offen offset:1024 sc1\n"
    "v_mfma_f32_16x16x32_bf16 v[28:31], v[68:71], v[84:87], v[28:31]\n"
    "v_mfma_f32_16x16x32_bf16 v[32:35], v[68:71], v[88:91], v[32:35]\n"
    "ds_read_b64_tr_b16 v[68:69], v104 offset:51264\n"
    "ds_read_b64_tr_b16 v[70:71], v104 offset:56384\n"
    "s_waitcnt lgkmcnt(11)\n"
    "v_mfma_f32_16x16x32_bf16 v[36:39], v[72:75], v[80:83], v[36:39]\n"
    "v_mfma_f32_16x16x32_bf16 v[40:43], v[72:75], v[84:87], v[40:43]\n"
    "s_sleep 1\n"
    "buffer_store_dwordx4 v[88:91], v113, s[44:47], s64 offen offset:2048 sc1\n"
    "v_mfma_f32_16x16x32_bf16 v[44:47], v[72:75], v[88:91], v[44:47]\n"
    "ds_read_b64_tr_b16 v[72:73], v105 offset:51264\n"
    "ds_read_b64_tr_b16 v[74:75], v105 offset:56384\n"
    "s_waitcnt lgkmcnt(11)\n"
    "v_mfma_f32_16x16x32_bf16 v[48:51], v[76:79], v[80:83], v[48:51]\n"
    "v_mfma_f32_16x16x32_bf16 v[52:55], v[76:79], v[84:87], v[52:55]\n"
    "v_mfma_f32_16x16x32_bf16 v[56:59], v[76:79], v[88:91], v[56:59]\n"
    "ds_read_b64_tr_b16 v[76:77], v104 offset:51328\n"
    "ds_read_b64_tr_b16 v[78:79], v104 offset:56448\n"
    "s_waitcnt lgkmcnt(9)\n"
    "v_mfma_f32_16x16x32_bf16 v[0:3], v[60:63], v[92:95], v[0:3]\n"
    "v_mfma_f32_16x16x32_bf16 v[4:7], v[60:63], v[96:99], v[4:7]\n"
    "s_waitcnt lgkmcnt(8)\n"
    "v_mfma_f32_16x16x32_bf16 v[8:11], v[60:63], v[100:103], v[8:11]\n"
    "s_add_u32 s64, s54, 61440\n"
    "buffer_store_dwordx4 v[92:95], v113, s[44:47], s64 offen offset:0 sc1\n"
    "s_waitcnt lgkmcnt(6)\n"
    "v_mfma_f32_16x16x32_bf16 v[12:15], v[64:67], v[92:95], v[12:15]\n"
    "s_sleep 1\n"
    "v_mfma_f32_16x16x32_bf16 v[16:19], v[64:67], v[96:99], v[16:19]\n"
    "v_mfma_f32_16x16x32_bf16 v[20:23], v[64:67], v[100:103], v[20:23]\n"
    "s_waitcnt lgkmcnt(0)\n"
    "s_barrier\n"
    "ds_read_b64_tr_b16 v[60:61], v104 offset:0\n"
    "ds_read_b64_tr_b16 v[62:63], v104 offset:5120\n"
    "v_mfma_f32_16x16x32_bf16 v[24:27], v[68:71], v[92:95], v[24:27]\n"
    "ds_read_b128 v[80:83], v107 offset:24576\n"
    "ds_read_b64_tr_b16 v[64:65], v105 offset:0\n"
    "ds_read_b64_tr_b16 v[66:67], v105 offset:5120\n"
    "buffer_store_dwordx4 v[96:99], v113, s[44:47], s64 offen offset:1024 sc1\n"
    "v_mfma_f32_16x16x32_bf16 v[28:31], v[68:71], v[96:99], v[28:31]\n"
    "ds_read_b128 v[84:87], v107 offset:25600\n"
    "v_mfma_f32_16x16x32_bf16 v[32:35], v[68:71], v[100:103], v[32:35]\n"
    "ds_read_b128 v[88:91], v107 offset:26624\n"
    "ds_read_b64_tr_b16 v[68:69], v104 offset:64\n"
    "ds_read_b64_tr_b16 v[70:71], v104 offset:5184\n"
    "v_mfma_f32_16x16x32_bf16 v[36:39], v[72:75], v[92:95], v[36:39]\n"
    "v_mfma_f32_16x16x32_bf16 v[40:43], v[72:75], v[96:99], v[40:43]\n"
    "s_sleep 1\n"
    "buffer_store_dwordx4 v[100:103], v113, s[44:47], s64 offen offset:2048 sc1\n"
    "v_mfma_f32_16x16x32_bf16 v[44:47], v[72:75], v[100:103], v[44:47]\n"
    "ds_read_b64_tr_b16 v[72:73], v105 offset:64\n"
    "ds_read_b64_tr_b16 v[74:75], v105 offset:5184\n"
    "v_mfma_f32_16x16x32_bf16 v[48:51], v[76:79], v[92:95], v[48:51]\n"
    "v_mfma_f32_16x16x32_bf16 v[52:55], v[76:79], v[96:99], v[52:55]\n"
    "v_mfma_f32_16x16x32_bf16 v[56:59], v[76:79], v[100:103], v[56:59]\n"
    "ds_read_b64_tr_b16 v[76:77], v104 offset:128\n"
    "ds_read_b64_tr_b16 v[78:79], v104 offset:5248\n"
    "s_waitcnt lgkmcnt(10)\n"
    "v_mfma_f32_16x16x32_bf16 v[0:3], v[60:63], v[80:83], v[0:3]\n"
    "ds_read_b128 v[92:95], v107 offset:36864\n"
    "s_waitcnt lgkmcnt(8)\n"
    "v_mfma_f32_16x16x32_bf16 v[4:7], v[60:63], v[84:87], v[4:7]\n"
    "ds_read_b128 v[96:99], v107 offset:37888\n"
    "s_waitcnt lgkmcnt(8)\n"
    "v_mfma_f32_16x16x32_bf16 v[8:11], v[60:63], v[88:91], v[8:11]\n"
    "ds_read_b64_tr_b16 v[60:61], v104 offset:10240\n"
    "ds_read_b64_tr_b16 v[62:63], v104 offset:15360\n"
    "s_add_u32 s64, s54, 73728\n"
    "buffer_store_dwordx4 v[80:83], v113, s[44:47], s64 offen offset:0 sc1\n"
    "v_mfma_f32_16x16x32_bf16 v[12:15], v[64:67], v[80:83], v[12:15]\n"
    "s_sleep 1\n"
    "ds_read_b128 v[100:103], v107 offset:38912\n"
    "v_mfma_f32_16x16x32_bf16 v[16:19], v[64:67], v[84:87], v[16:19]\n"
    "v_mfma_f32_16x16x32_bf16 v[20:23], v[64:67], v[88:91], v[20:23]\n"
    "ds_read_b64_tr_b16 v[64:65], v105 offset:10240\n"
    "ds_read_b64_tr_b16 v[66:67], v105 offset:15360\n"
    "s_waitcnt lgkmcnt(11)\n"
    "v_mfma_f32_16x16x32_bf16 v[24:27], v[68:71], v[80:83], v[24:27]\n"
    "buffer_store_dwordx4 v[84:87], v113, s[44:47], s64 offen offset:1024 sc1\n"
    "v_mfma_f32_16x16x32_bf16 v[28:31], v[68:71], v[84:87], v[28:31]\n"
    "v_mfma_f32_16x16x32_bf16 v[32:35], v[68:71], v[88:91], v[32:35]\n"
    "ds_read_b64_tr_b16 v[68:69], v104 offset:10304\n"
    "ds_read_b64_tr_b16 v[70:71], v104 offset:15424\n"
    "s_waitcnt lgkmcnt(11)\n"
    "v_mfma_f32_16x16x32_bf16 v[36:39], v[72:75], v[80:83], v[36:39]\n"
    "v_mfma_f32_16x16x32_bf16 v[40:43], v[72:75], v[84:87], v[40:43]\n"
    "s_sleep 1\n"
    "buffer_store_dwordx4 v[88:91], v113, s[44:47], s64 offen offset:2048 sc1\n"
    "v_mfma_f32_16x16x32_bf16 v[44:47], v[72:75], v[88:91], v[44:47]\n"
    "ds_read_b64_tr_b16 v[72:73], v105 offset:10304\n"
    "ds_read_b64_tr_b16 v[74:75], v105 offset:15424\n"
    "s_waitcnt lgkmcnt(11)\n"
    "v_mfma_f32_16x16x32_bf16 v[48:51], v[76:79], v[80:83], v[48:51]\n"
    "v_mfma_f32_16x16x32_bf16 v[52:55], v[76:79], v[84:87], v[52:55]\n"
    "v_mfma_f32_16x16x32_bf16 v[56:59], v[76:79], v[88:91], v[56:59]\n"
    "ds_read_b64_tr_b16 v[76:77], v104 offset:10368\n"
    "ds_read_b64_tr_b16 v[78:79], v104 offset:15488\n"
    "s_waitcnt lgkmcnt(9)\n"
    "v_mfma_f32_16x16x32_bf16 v[0:3], v[60:63], v[92:95], v[0:3]\n"
    "v_mfma_f32_16x16x32_bf16 v[4:7], v[60:63], v[96:99], v[4:7]\n"
    "s_waitcnt lgkmcnt(8)\n"
    "v_mfma_f32_16x16x32_bf16 v[8:11], v[60:63], v[100:103], v[8:11]\n"
    "s_add_u32 s64, s54, 86016\n"
    "buffer_store_dwordx4 v[92:95], v113, s[44:47], s64 offen offset:0 sc1\n"
    "s_waitcnt lgkmcnt(6)\n"
    "v_mfma_f32_16x16x32_bf16 v[12:15], v[64:67], v[92:95], v[12:15]\n"
    "s_sleep 1\n"
    "v_mfma_f32_16x16x32_bf16 v[16:19], v[64:67], v[96:99], v[16:19]\n"
    "v_mfma_f32_16x16x32_bf16 v[20:23], v[64:67], v[100:103], v[20:23]\n"
    "s_waitcnt lgkmcnt(0)\n"
    "s_barrier\n"
    "ds_read_b64_tr_b16 v[60:61], v104 offset:20480\n"
    "ds_read_b64_tr_b16 v[62:63], v104 offset:25600\n"
    "v_mfma_f32_16x16x32_bf16 v[24:27], v[68:71], v[92:95], v[24:27]\n"
    "ds_read_b128 v[80:83], v106 offset:0\n"
    "ds_read_b64_tr_b16 v[64:65], v105 offset:20480\n"
    "ds_read_b64_tr_b16 v[66:67], v105 offset:25600\n"
    "buffer_store_dwordx4 v[96:99], v113, s[44:47], s64 offen offset:1024 sc1\n"
    "v_mfma_f32_16x16x32_bf16 v[28:31], v[68:71], v[96:99], v[28:31]\n"
    "ds_read_b128 v[84:87], v106 offset:1024\n"
    "v_mfma_f32_16x16x32_bf16 v[32:35], v[68:71], v[100:103], v[32:35]\n"
    "ds_read_b128 v[88:91], v106 offset:2048\n"
    "ds_read_b64_tr_b16 v[68:69], v104 offset:20544\n"
    "ds_read_b64_tr_b16 v[70:71], v104 offset:25664\n"
    "v_mfma_f32_16x16x32_bf16 v[36:39], v[72:75], v[92:95], v[36:39]\n"
    "v_mfma_f32_16x16x32_bf16 v[40:43], v[72:75], v[96:99], v[40:43]\n"
    "s_sleep 1\n"
    "buffer_store_dwordx4 v[100:103], v113, s[44:47], s64 offen offset:2048 sc1\n"
    "v_mfma_f32_16x16x32_bf16 v[44:47], v[72:75], v[100:103], v[44:47]\n"
    "ds_read_b64_tr_b16 v[72:73], v105 offset:20544\n"
    "ds_read_b64_tr_b16 v[74:75], v105 offset:25664\n"
    "v_mfma_f32_16x16x32_bf16 v[48:51], v[76:79], v[92:95], v[48:51]\n"
    "v_mfma_f32_16x16x32_bf16 v[52:55], v[76:79], v[96:99], v[52:55]\n"
    "v_mfma_f32_16x16x32_bf16 v[56:59], v[76:79], v[100:103], v[56:59]\n"
    "ds_read_b64_tr_b16 v[76:77], v104 offset:20608\n"
    "ds_read_b64_tr_b16 v[78:79], v104 offset:25728\n"
    "s_waitcnt lgkmcnt(10)\n"
    "v_mfma_f32_16x16x32_bf16 v[0:3], v[60:63], v[80:83], v[0:3]\n"
    "ds_read_b128 v[92:95], v106 offset:12288\n"
    "s_waitcnt lgkmcnt(8)\n"
    "v_mfma_f32_16x16x32_bf16 v[4:7], v[60:63], v[84:87], v[4:7]\n"
    "ds_read_b128 v[96:99], v106 offset:13312\n"
    "s_waitcnt lgkmcnt(8)\n"
    "v_mfma_f32_16x16x32_bf16 v[8:11], v[60:63], v[88:91], v[8:11]\n"
    "ds_read_b64_tr_b16 v[60:61], v104 offset:30720\n"
    "ds_read_b64_tr_b16 v[62:63], v104 offset:35840\n"
    "v_mfma_f32_16x16x32_bf16 v[12:15], v[64:67], v[80:83], v[12:15]\n"
    "s_sleep 1\n"
    "ds_read_b128 v[100:103], v106 offset:14336\n"
    "v_mfma_f32_16x16x32_bf16 v[16:19], v[64:67], v[84:87], v[16:19]\n"
    "v_mfma_f32_16x16x32_bf16 v[20:23], v[64:67], v[88:91], v[20:23]\n"
    "ds_read_b64_tr_b16 v[64:65], v105 offset:30720\n"
    "ds_read_b64_tr_b16 v[66:67], v105 offset:35840\n"
    "s_waitcnt lgkmcnt(11)\n"
    "v_mfma_f32_16x16x32_bf16 v[24:27], v[68:71], v[80:83], v[24:27]\n"
    "v_mfma_f32_16x16x32_bf16 v[28:31], v[68:71], v[84:87], v[28:31]\n"
    "v_mfma_f32_16x16x32_bf16 v[32:35], v[68:71], v[88:91], v[32:35]\n"
    "ds_read_b64_tr_b16 v[68:69], v104 offset:30784\n"
    "ds_read_b64_tr_b16 v[70:71], v104 offset:35904\n"
    "s_waitcnt lgkmcnt(11)\n"
    "v_mfma_f32_16x16x32_bf16 v[36:39], v[72:75], v[80:83], v[36:39]\n"
    "v_mfma_f32_16x16x32_bf16 v[40:43], v[72:75], v[84:87], v[40:43]\n"
    "s_sleep 1\n"
    "v_mfma_f32_16x16x32_bf16 v[44:47], v[72:75], v[88:91], v[44:47]\n"
    "ds_read_b64_tr_b16 v[72:73], v105 offset:30784\n"
    "ds_read_b64_tr_b16 v[74:75], v105 offset:35904\n"
    "s_waitcnt lgkmcnt(11)\n"
    "v_mfma_f32_16x16x32_bf16 v[48:51], v[76:79], v[80:83], v[48:51]\n"
    "v_mfma_f32_16x16x32_bf16 v[52:55], v[76:79], v[84:87], v[52:55]\n"
    "v_mfma_f32_16x16x32_bf16 v[56:59], v[76:79], v[88:91], v[56:59]\n"
    "ds_read_b64_tr_b16 v[76:77], v104 offset:30848\n"
    "ds_read_b64_tr_b16 v[78:79], v104 offset:35968\n"
    "s_waitcnt lgkmcnt(9)\n"
    "v_mfma_f32_16x16x32_bf16 v[0:3], v[60:63], v[92:95], v[0:3]\n"
    "v_mfma_f32_16x16x32_bf16 v[4:7], v[60:63], v[96:99], v[4:7]\n"
    "s_waitcnt lgkmcnt(8)\n"
    "v_mfma_f32_16x16x32_bf16 v[8:11], v[60:63], v[100:103], v[8:11]\n"
    "s_waitcnt lgkmcnt(6)\n"
    "v_mfma_f32_16x16x32_bf16 v[12:15], v[64:67], v[92:95], v[12:15]\n"
    "s_sleep 1\n"
    "v_mfma_f32_16x16x32_bf16 v[16:19], v[64:67], v[96:99], v[16:19]\n"
    "v_mfma_f32_16x16x32_bf16 v[20:23], v[64:67], v[100:103], v[20:23]\n"
    "s_waitcnt vmcnt(0) lgkmcnt(0)\n"
    "s_barrier\n"
    "ds_read_b64_tr_b16 v[60:61], v104 offset:40960\n"
    "ds_read_b64_tr_b16 v[62:63], v104 offset:46080\n"
    "v_mfma_f32_16x16x32_bf16 v[24:27], v[68:71], v[92:95], v[24:27]\n"
    "ds_read_b128 v[80:83], v106 offset:24576\n"
    "ds_read_b64_tr_b16 v[64:65], v105 offset:40960\n"
    "ds_read_b64_tr_b16 v[66:67], v105 offset:46080\n"
    "v_mfma_f32_16x16x32_bf16 v[28:31], v[68:71], v[96:99], v[28:31]\n"
    "ds_read_b128 v[84:87], v106 offset:25600\n"
    "v_mfma_f32_16x16x32_bf16 v[32:35], v[68:71], v[100:103], v[32:35]\n"
    "ds_read_b128 v[88:91], v106 offset:26624\n"
    "ds_read_b64_tr_b16 v[68:69], v104 offset:41024\n"
    "ds_read_b64_tr_b16 v[70:71], v104 offset:46144\n"
    "v_mfma_f32_16x16x32_bf16 v[36:39], v[72:75], v[92:95], v[36:39]\n"
    "v_mfma_f32_16x16x32_bf16 v[40:43], v[72:75], v[96:99], v[40:43]\n"
    "s_sleep 1\n"
    "v_mfma_f32_16x16x32_bf16 v[44:47], v[72:75], v[100:103], v[44:47]\n"
    "ds_read_b64_tr_b16 v[72:73], v105 offset:41024\n"
    "ds_read_b64_tr_b16 v[74:75], v105 offset:46144\n"
    "v_mfma_f32_16x16x32_bf16 v[48:51], v[76:79], v[92:95], v[48:51]\n"
    "v_mfma_f32_16x16x32_bf16 v[52:55], v[76:79], v[96:99], v[52:55]\n"
    "v_mfma_f32_16x16x32_bf16 v[56:59], v[76:79], v[100:103], v[56:59]\n"
    "ds_read_b64_tr_b16 v[76:77], v104 offset:41088\n"
    "ds_read_b64_tr_b16 v[78:79], v104 offset:46208\n"
    "s_waitcnt lgkmcnt(10)\n"
    "v_mfma_f32_16x16x32_bf16 v[0:3], v[60:63], v[80:83], v[0:3]\n"
    "ds_read_b128 v[92:95], v106 offset:36864\n"
    "s_waitcnt lgkmcnt(8)\n"
    "v_mfma_f32_16x16x32_bf16 v[4:7], v[60:63], v[84:87], v[4:7]\n"
    "ds_read_b128 v[96:99], v106 offset:37888\n"
    "s_waitcnt lgkmcnt(8)\n"
    "v_mfma_f32_16x16x32_bf16 v[8:11], v[60:63], v[88:91], v[8:11]\n"
    "ds_read_b64_tr_b16 v[60:61], v104 offset:51200\n"
    "ds_read_b64_tr_b16 v[62:63], v104 offset:56320\n"
    "v_mfma_f32_16x16x32_bf16 v[12:15], v[64:67], v[80:83], v[12:15]\n"
    "ds_read_b128 v[100:103], v106 offset:38912\n"
    "v_mfma_f32_16x16x32_bf16 v[16:19], v[64:67], v[84:87], v[16:19]\n"
    "v_mfma_f32_16x16x32_bf16 v[20:23], v[64:67], v[88:91], v[20:23]\n"
    "ds_read_b64_tr_b16 v[64:65], v105 offset:51200\n"
    "ds_read_b64_tr_b16 v[66:67], v105 offset:56320\n"
    "s_waitcnt lgkmcnt(11)\n"
    "v_mfma_f32_16x16x32_bf16 v[24:27], v[68:71], v[80:83], v[24:27]\n"
    "v_mfma_f32_16x16x32_bf16 v[28:31], v[68:71], v[84:87], v[28:31]\n"
    "v_mfma_f32_16x16x32_bf16 v[32:35], v[68:71], v[88:91], v[32:35]\n"
    "ds_read_b64_tr_b16 v[68:69], v104 offset:51264\n"
    "ds_read_b64_tr_b16 v[70:71], v104 offset:56384\n"
    "s_waitcnt lgkmcnt(11)\n"
    "v_mfma_f32_16x16x32_bf16 v[36:39], v[72:75], v[80:83], v[36:39]\n"
    "v_mfma_f32_16x16x32_bf16 v[40:43], v[72:75], v[84:87], v[40:43]\n"
    "v_mfma_f32_16x16x32_bf16 v[44:47], v[72:75], v[88:91], v[44:47]\n"
    "ds_read_b64_tr_b16 v[72:73], v105 offset:51264\n"
    "ds_read_b64_tr_b16 v[74:75], v105 offset:56384\n"
    "s_waitcnt lgkmcnt(11)\n"
    "v_mfma_f32_16x16x32_bf16 v[48:51], v[76:79], v[80:83], v[48:51]\n"
    "v_mfma_f32_16x16x32_bf16 v[52:55], v[76:79], v[84:87], v[52:55]\n"
    "v_mfma_f32_16x16x32_bf16 v[56:59], v[76:79], v[88:91], v[56:59]\n"
    "ds_read_b64_tr_b16 v[76:77], v104 offset:51328\n"
    "ds_read_b64_tr_b16 v[78:79], v104 offset:56448\n"
    "s_waitcnt lgkmcnt(9)\n"
    "v_mfma_f32_16x16x32_bf16 v[0:3], v[60:63], v[92:95], v[0:3]\n"
    "v_mfma_f32_16x16x32_bf16 v[4:7], v[60:63], v[96:99], v[4:7]\n"
    "s_waitcnt lgkmcnt(8)\n"
    "v_mfma_f32_16x16x32_bf16 v[8:11], v[60:63], v[100:103], v[8:11]\n"
    "s_waitcnt lgkmcnt(6)\n"
    "v_mfma_f32_16x16x32_bf16 v[12:15], v[64:67], v[92:95], v[12:15]\n"
    "v_mfma_f32_16x16x32_bf16 v[16:19], v[64:67], v[96:99], v[16:19]\n"
    "v_mfma_f32_16x16x32_bf16 v[20:23], v[64:67], v[100:103], v[20:23]\n"
    "s_waitcnt lgkmcnt(0)\n"
    "s_barrier\n"
    "ds_read_b64_tr_b16 v[60:61], v104 offset:0\n"
    "ds_read_b64_tr_b16 v[62:63], v104 offset:5120\n"
    "v_mfma_f32_16x16x32_bf16 v[24:27], v[68:71], v[92:95], v[24:27]\n"
    "ds_read_b128 v[80:83], v107 offset:0\n"
    "ds_read_b64_tr_b16 v[64:65], v105 offset:0\n"
    "ds_read_b64_tr_b16 v[66:67], v105 offset:5120\n"
    "v_mfma_f32_16x16x32_bf16 v[28:31], v[68:71], v[96:99], v[28:31]\n"
    "ds_read_b128 v[84:87], v107 offset:1024\n"
    "v_mfma_f32_16x16x32_bf16 v[32:35], v[68:71], v[100:103], v[32:35]\n"
    "ds_read_b128 v[88:91], v107 offset:2048\n"
    "ds_read_b64_tr_b16 v[68:69], v104 offset:64\n"
    "ds_read_b64_tr_b16 v[70:71], v104 offset:5184\n"
    "v_mfma_f32_16x16x32_bf16 v[36:39], v[72:75], v[92:95], v[36:39]\n"
    "v_mfma_f32_16x16x32_bf16 v[40:43], v[72:75], v[96:99], v[40:43]\n"
    "v_mfma_f32_16x16x32_bf16 v[44:47], v[72:75], v[100:103], v[44:47]\n"
    "ds_read_b64_tr_b16 v[72:73], v105 offset:64\n"
    "ds_read_b64_tr_b16 v[74:75], v105 offset:5184\n"
    "v_mfma_f32_16x16x32_bf16 v[48:51], v[76:79], v[92:95], v[48:51]\n"
    "v_mfma_f32_16x16x32_bf16 v[52:55], v[76:79], v[96:99], v[52:55]\n"
    "v_mfma_f32_16x16x32_bf16 v[56:59], v[76:79], v[100:103], v[56:59]\n"
    "ds_read_b64_tr_b16 v[76:77], v104 offset:128\n"
    "ds_read_b64_tr_b16 v[78:79], v104 offset:5248\n"
    "s_waitcnt lgkmcnt(10)\n"
    "v_mfma_f32_16x16x32_bf16 v[0:3], v[60:63], v[80:83], v[0:3]\n"
    "ds_read_b128 v[92:95], v107 offset:12288\n"
    "s_waitcnt lgkmcnt(8)\n"
    "v_mfma_f32_16x16x32_bf16 v[4:7], v[60:63], v[84:87], v[4:7]\n"
    "ds_read_b128 v[96:99], v107 offset:13312\n"
    "s_waitcnt lgkmcnt(8)\n"
    "v_mfma_f32_16x16x32_bf16 v[8:11], v[60:63], v[88:91], v[8:11]\n"
    "ds_read_b64_tr_b16 v[60:61], v104 offset:10240\n"
    "ds_read_b64_tr_b16 v[62:63], v104 offset:15360\n"
    "v_mfma_f32_16x16x32_bf16 v[12:15], v[64:67], v[80:83], v[12:15]\n"
    "ds_read_b128 v[100:103], v107 offset:14336\n"
    "v_mfma_f32_16x16x32_bf16 v[16:19], v[64:67], v[84:87], v[16:19]\n"
    "v_mfma_f32_16x16x32_bf16 v[20:23], v[64:67], v[88:91], v[20:23]\n"
    "ds_read_b64_tr_b16 v[64:65], v105 offset:10240\n"
    "ds_read_b64_tr_b16 v[66:67], v105 offset:15360\n"
    "s_waitcnt lgkmcnt(11)\n"
    "v_mfma_f32_16x16x32_bf16 v[24:27], v[68:71], v[80:83], v[24:27]\n"
    "v_mfma_f32_16x16x32_bf16 v[28:31], v[68:71], v[84:87], v[28:31]\n"
    "v_mfma_f32_16x16x32_bf16 v[32:35], v[68:71], v[88:91], v[32:35]\n"
    "ds_read_b64_tr_b16 v[68:69], v104 offset:10304\n"
    "ds_read_b64_tr_b16 v[70:71], v104 offset:15424\n"
    "s_waitcnt lgkmcnt(11)\n"
    "v_mfma_f32_16x16x32_bf16 v[36:39], v[72:75], v[80:83], v[36:39]\n"
    "v_mfma_f32_16x16x32_bf16 v[40:43], v[72:75], v[84:87], v[40:43]\n"
    "v_mfma_f32_16x16x32_bf16 v[44:47], v[72:75], v[88:91], v[44:47]\n"
    "ds_read_b64_tr_b16 v[72:73], v105 offset:10304\n"
    "ds_read_b64_tr_b16 v[74:75], v105 offset:15424\n"
    "s_waitcnt lgkmcnt(11)\n"
    "v_mfma_f32_16x16x32_bf16 v[48:51], v[76:79], v[80:83], v[48:51]\n"
    "v_mfma_f32_16x16x32_bf16 v[52:55], v[76:79], v[84:87], v[52:55]\n"
    "v_mfma_f32_16x16x32_bf16 v[56:59], v[76:79], v[88:91], v[56:59]\n"
    "ds_read_b64_tr_b16 v[76:77], v104 offset:10368\n"
    "ds_read_b64_tr_b16 v[78:79], v104 offset:15488\n"
    "s_waitcnt lgkmcnt(9)\n"
    "v_mfma_f32_16x16x32_bf16 v[0:3], v[60:63], v[92:95], v[0:3]\n"
    "v_mfma_f32_16x16x32_bf16 v[4:7], v[60:63], v[96:99], v[4:7]\n"
    "s_waitcnt lgkmcnt(8)\n"
    "v_mfma_f32_16x16x32_bf16 v[8:11], v[60:63], v[100:103], v[8:11]\n"
    "s_waitcnt lgkmcnt(6)\n"
    "v_mfma_f32_16x16x32_bf16 v[12:15], v[64:67], v[92:95], v[12:15]\n"
    "v_mfma_f32_16x16x32_bf16 v[16:19], v[64:67], v[96:99], v[16:19]\n"
    "v_mfma_f32_16x16x32_bf16 v[20:23], v[64:67], v[100:103], v[20:23]\n"
    "s_waitcnt lgkmcnt(0)\n"
    "s_barrier\n"
    "ds_read_b64_tr_b16 v[60:61], v104 offset:20480\n"
    "ds_read_b64_tr_b16 v[62:63], v104 offset:25600\n"
    "v_mfma_f32_16x16x32_bf16 v[24:27], v[68:71], v[92:95], v[24:27]\n"
    "ds_read_b128 v[80:83], v107 offset:24576\n"
    "ds_read_b64_tr_b16 v[64:65], v105 offset:20480\n"
    "ds_read_b64_tr_b16 v[66:67], v105 offset:25600\n"
    "v_mfma_f32_16x16x32_bf16 v[28:31], v[68:71], v[96:99], v[28:31]\n"
    "ds_read_b128 v[84:87], v107 offset:25600\n"
    "v_mfma_f32_16x16x32_bf16 v[32:35], v[68:71], v[100:103], v[32:35]\n"
    "ds_read_b128 v[88:91], v107 offset:26624\n"
    "ds_read_b64_tr_b16 v[68:69], v104 offset:20544\n"
    "ds_read_b64_tr_b16 v[70:71], v104 offset:25664\n"
    "v_mfma_f32_16x16x32_bf16 v[36:39], v[72:75], v[92:95], v[36:39]\n"
    "v_mfma_f32_16x16x32_bf16 v[40:43], v[72:75], v[96:99], v[40:43]\n"
    "v_mfma_f32_16x16x32_bf16 v[44:47], v[72:75], v[100:103], v[44:47]\n"
    "ds_read_b64_tr_b16 v[72:73], v105 offset:20544\n"
    "ds_read_b64_tr_b16 v[74:75], v105 offset:25664\n"
    "v_mfma_f32_16x16x32_bf16 v[48:51], v[76:79], v[92:95], v[48:51]\n"
    "v_mfma_f32_16x16x32_bf16 v[52:55], v[76:79], v[96:99], v[52:55]\n"
    "v_mfma_f32_16x16x32_bf16 v[56:59], v[76:79], v[100:103], v[56:59]\n"
    "ds_read_b64_tr_b16 v[76:77], v104 offset:20608\n"
    "ds_read_b64_tr_b16 v[78:79], v104 offset:25728\n"
    "s_waitcnt lgkmcnt(10)\n"
    "v_mfma_f32_16x16x32_bf16 v[0:3], v[60:63], v[80:83], v[0:3]\n"
    "ds_read_b128 v[92:95], v107 offset:36864\n"
    "s_waitcnt lgkmcnt(8)\n"
    "v_mfma_f32_16x16x32_bf16 v[4:7], v[60:63], v[84:87], v[4:7]\n"
    "ds_read_b128 v[96:99], v107 offset:37888\n"
    "s_waitcnt lgkmcnt(8)\n"
    "v_mfma_f32_16x16x32_bf16 v[8:11], v[60:63], v[88:91], v[8:11]\n"
    "ds_read_b64_tr_b16 v[60:61], v104 offset:30720\n"
    "ds_read_b64_tr_b16 v[62:63], v104 offset:35840\n"
    "v_mfma_f32_16x16x32_bf16 v[12:15], v[64:67], v[80:83], v[12:15]\n"
    "ds_read_b128 v[100:103], v107 offset:38912\n"
    "v_mfma_f32_16x16x32_bf16 v[16:19], v[64:67], v[84:87], v[16:19]\n"
    "v_mfma_f32_16x16x32_bf16 v[20:23], v[64:67], v[88:91], v[20:23]\n"
    "ds_read_b64_tr_b16 v[64:65], v105 offset:30720\n"
    "ds_read_b64_tr_b16 v[66:67], v105 offset:35840\n"
    "s_waitcnt lgkmcnt(11)\n"
    "v_mfma_f32_16x16x32_bf16 v[24:27], v[68:71], v[80:83], v[24:27]\n"
    "v_mfma_f32_16x16x32_bf16 v[28:31], v[68:71], v[84:87], v[28:31]\n"
    "v_mfma_f32_16x16x32_bf16 v[32:35], v[68:71], v[88:91], v[32:35]\n"
    "ds_read_b64_tr_b16 v[68:69], v104 offset:30784\n"
    "ds_read_b64_tr_b16 v[70:71], v104 offset:35904\n"
    "s_waitcnt lgkmcnt(11)\n"
    "v_mfma_f32_16x16x32_bf16 v[36:39], v[72:75], v[80:83], v[36:39]\n"
    "v_mfma_f32_16x16x32_bf16 v[40:43], v[72:75], v[84:87], v[40:43]\n"
    "v_mfma_f32_16x16x32_bf16 v[44:47], v[72:75], v[88:91], v[44:47]\n"
    "ds_read_b64_tr_b16 v[72:73], v105 offset:30784\n"
    "ds_read_b64_tr_b16 v[74:75], v105 offset:35904\n"
    "s_waitcnt lgkmcnt(11)\n"
    "v_mfma_f32_16x16x32_bf16 v[48:51], v[76:79], v[80:83], v[48:51]\n"
    "v_mfma_f32_16x16x32_bf16 v[52:55], v[76:79], v[84:87], v[52:55]\n"
    "v_mfma_f32_16x16x32_bf16 v[56:59], v[76:79], v[88:91], v[56:59]\n"
    "ds_read_b64_tr_b16 v[76:77], v104 offset:30848\n"
    "ds_read_b64_tr_b16 v[78:79], v104 offset:35968\n"
    "s_waitcnt lgkmcnt(9)\n"
    "v_mfma_f32_16x16x32_bf16 v[0:3], v[60:63], v[92:95], v[0:3]\n"
    "v_mfma_f32_16x16x32_bf16 v[4:7], v[60:63], v[96:99], v[4:7]\n"
    "s_waitcnt lgkmcnt(8)\n"
    "v_mfma_f32_16x16x32_bf16 v[8:11], v[60:63], v[100:103], v[8:11]\n"
    "s_waitcnt lgkmcnt(6)\n"
    "v_mfma_f32_16x16x32_bf16 v[12:15], v[64:67], v[92:95], v[12:15]\n"
    "v_mfma_f32_16x16x32_bf16 v[16:19], v[64:67], v[96:99], v[16:19]\n"
    "v_mfma_f32_16x16x32_bf16 v[20:23], v[64:67], v[100:103], v[20:23]\n"
    "s_waitcnt lgkmcnt(0)\n"
    "s_barrier\n"
    "ds_read_b64_tr_b16 v[60:61], v104 offset:40960\n"
    "ds_read_b64_tr_b16 v[62:63], v104 offset:46080\n"
    "v_mfma_f32_16x16x32_bf16 v[24:27], v[68:71], v[92:95], v[24:27]\n"
    "ds_read_b128 v[80:83], v106 offset:0\n"
    "ds_read_b64_tr_b16 v[64:65], v105 offset:40960\n"
    "ds_read_b64_tr_b16 v[66:67], v105 offset:46080\n"
    "v_mfma_f32_16x16x32_bf16 v[28:31], v[68:71], v[96:99], v[28:31]\n"
    "ds_read_b128 v[84:87], v106 offset:1024\n"
    "v_mfma_f32_16x16x32_bf16 v[32:35], v[68:71], v[100:103], v[32:35]\n"
    "ds_read_b128 v[88:91], v106 offset:2048\n"
    "ds_read_b64_tr_b16 v[68:69], v104 offset:41024\n"
    "ds_read_b64_tr_b16 v[70:71], v104 offset:46144\n"
    "v_mfma_f32_16x16x32_bf16 v[36:39], v[72:75], v[92:95], v[36:39]\n"
    "v_mfma_f32_16x16x32_bf16 v[40:43], v[72:75], v[96:99], v[40:43]\n"
    "v_mfma_f32_16x16x32_bf16 v[44:47], v[72:75], v[100:103], v[44:47]\n"
    "ds_read_b64_tr_b16 v[72:73], v105 offset:41024\n"
    "ds_read_b64_tr_b16 v[74:75], v105 offset:46144\n"
    "v_mfma_f32_16x16x32_bf16 v[48:51], v[76:79], v[92:95], v[48:51]\n"
    "v_mfma_f32_16x16x32_bf16 v[52:55], v[76:79], v[96:99], v[52:55]\n"
    "v_mfma_f32_16x16x32_bf16 v[56:59], v[76:79], v[100:103], v[56:59]\n"
    "ds_read_b64_tr_b16 v[76:77], v104 offset:41088\n"
    "ds_read_b64_tr_b16 v[78:79], v104 offset:46208\n"
    "s_waitcnt lgkmcnt(10)\n"
    "v_mfma_f32_16x16x32_bf16 v[0:3], v[60:63], v[80:83], v[0:3]\n"
    "ds_read_b128 v[92:95], v106 offset:12288\n"
    "s_waitcnt lgkmcnt(8)\n"
    "v_mfma_f32_16x16x32_bf16 v[4:7], v[60:63], v[84:87], v[4:7]\n"
    "ds_read_b128 v[96:99], v106 offset:13312\n"
    "s_waitcnt lgkmcnt(8)\n"
    "v_mfma_f32_16x16x32_bf16 v[8:11], v[60:63], v[88:91], v[8:11]\n"
    "ds_read_b64_tr_b16 v[60:61], v104 offset:51200\n"
    "ds_read_b64_tr_b16 v[62:63], v104 offset:56320\n"
    "v_mfma_f32_16x16x32_bf16 v[12:15], v[64:67], v[80:83], v[12:15]\n"
    "ds_read_b128 v[100:103], v106 offset:14336\n"
    "v_mfma_f32_16x16x32_bf16 v[16:19], v[64:67], v[84:87], v[16:19]\n"
    "v_mfma_f32_16x16x32_bf16 v[20:23], v[64:67], v[88:91], v[20:23]\n"
    "ds_read_b64_tr_b16 v[64:65], v105 offset:51200\n"
    "ds_read_b64_tr_b16 v[66:67], v105 offset:56320\n"
    "s_waitcnt lgkmcnt(11)\n"
    "v_mfma_f32_16x16x32_bf16 v[24:27], v[68:71], v[80:83], v[24:27]\n"
    "v_mfma_f32_16x16x32_bf16 v[28:31], v[68:71], v[84:87], v[28:31]\n"
    "v_mfma_f32_16x16x32_bf16 v[32:35], v[68:71], v[88:91], v[32:35]\n"
    "ds_read_b64_tr_b16 v[68:69], v104 offset:51264\n"
    "ds_read_b64_tr_b16 v[70:71], v104 offset:56384\n"
    "s_waitcnt lgkmcnt(11)\n"
    "v_mfma_f32_16x16x32_bf16 v[36:39], v[72:75], v[80:83], v[36:39]\n"
    "v_mfma_f32_16x16x32_bf16 v[40:43], v[72:75], v[84:87], v[40:43]\n"
    "v_mfma_f32_16x16x32_bf16 v[44:47], v[72:75], v[88:91], v[44:47]\n"
    "ds_read_b64_tr_b16 v[72:73], v105 offset:51264\n"
    "ds_read_b64_tr_b16 v[74:75], v105 offset:56384\n"
    "s_waitcnt lgkmcnt(11)\n"
    "v_mfma_f32_16x16x32_bf16 v[48:51], v[76:79], v[80:83], v[48:51]\n"
    "v_mfma_f32_16x16x32_bf16 v[52:55], v[76:79], v[84:87], v[52:55]\n"
    "v_mfma_f32_16x16x32_bf16 v[56:59], v[76:79], v[88:91], v[56:59]\n"
    "ds_read_b64_tr_b16 v[76:77], v104 offset:51328\n"
    "ds_read_b64_tr_b16 v[78:79], v104 offset:56448\n"
    "s_waitcnt lgkmcnt(9)\n"
    "v_mfma_f32_16x16x32_bf16 v[0:3], v[60:63], v[92:95], v[0:3]\n"
    "v_mfma_f32_16x16x32_bf16 v[4:7], v[60:63], v[96:99], v[4:7]\n"
    "s_waitcnt lgkmcnt(8)\n"
    "v_mfma_f32_16x16x32_bf16 v[8:11], v[60:63], v[100:103], v[8:11]\n"
    "s_waitcnt lgkmcnt(6)\n"
    "v_mfma_f32_16x16x32_bf16 v[12:15], v[64:67], v[92:95], v[12:15]\n"
    "v_mfma_f32_16x16x32_bf16 v[16:19], v[64:67], v[96:99], v[16:19]\n"
    "v_mfma_f32_16x16x32_bf16 v[20:23], v[64:67], v[100:103], v[20:23]\n"
    "s_waitcnt lgkmcnt(0)\n"
    "s_barrier\n"
    "ds_read_b64_tr_b16 v[60:61], v104 offset:0\n"
    "ds_read_b64_tr_b16 v[62:63], v104 offset:5120\n"
    "v_mfma_f32_16x16x32_bf16 v[24:27], v[68:71], v[92:95], v[24:27]\n"
    "ds_read_b128 v[80:83], v106 offset:24576\n"
    "ds_read_b64_tr_b16 v[64:65], v105 offset:0\n"
    "ds_read_b64_tr_b16 v[66:67], v105 offset:5120\n"
    "v_mfma_f32_16x16x32_bf16 v[28:31], v[68:71], v[96:99], v[28:31]\n"
    "ds_read_b128 v[84:87], v106 offset:25600\n"
    "v_mfma_f32_16x16x32_bf16 v[32:35], v[68:71], v[100:103], v[32:35]\n"
    "ds_read_b128 v[88:91], v106 offset:26624\n"
    "ds_read_b64_tr_b16 v[68:69], v104 offset:64\n"
    "ds_read_b64_tr_b16 v[70:71], v104 offset:5184\n"
    "v_mfma_f32_16x16x32_bf16 v[36:39], v[72:75], v[92:95], v[36:39]\n"
    "v_mfma_f32_16x16x32_bf16 v[40:43], v[72:75], v[96:99], v[40:43]\n"
    "v_mfma_f32_16x16x32_bf16 v[44:47], v[72:75], v[100:103], v[44:47]\n"
    "ds_read_b64_tr_b16 v[72:73], v105 offset:64\n"
    "ds_read_b64_tr_b16 v[74:75], v105 offset:5184\n"
    "v_mfma_f32_16x16x32_bf16 v[48:51], v[76:79], v[92:95], v[48:51]\n"
    "v_mfma_f32_16x16x32_bf16 v[52:55], v[76:79], v[96:99], v[52:55]\n"
    "v_mfma_f32_16x16x32_bf16 v[56:59], v[76:79], v[100:103], v[56:59]\n"
    "ds_read_b64_tr_b16 v[76:77], v104 offset:128\n"
    "ds_read_b64_tr_b16 v[78:79], v104 offset:5248\n"
    "s_waitcnt lgkmcnt(10)\n"
    "v_mfma_f32_16x16x32_bf16 v[0:3], v[60:63], v[80:83], v[0:3]\n"
    "ds_read_b128 v[92:95], v106 offset:36864\n"
    "s_waitcnt lgkmcnt(8)\n"
    "v_mfma_f32_16x16x32_bf16 v[4:7], v[60:63], v[84:87], v[4:7]\n"
    "ds_read_b128 v[96:99], v106 offset:37888\n"
    "s_waitcnt lgkmcnt(8)\n"
    "v_mfma_f32_16x16x32_bf16 v[8:11], v[60:63], v[88:91], v[8:11]\n"
    "ds_read_b64_tr_b16 v[60:61], v104 offset:10240\n"
    "ds_read_b64_tr_b16 v[62:63], v104 offset:15360\n"
    "v_mfma_f32_16x16x32_bf16 v[12:15], v[64:67], v[80:83], v[12:15]\n"
    "ds_read_b128 v[100:103], v106 offset:38912\n"
    "v_mfma_f32_16x16x32_bf16 v[16:19], v[64:67], v[84:87], v[16:19]\n"
    "v_mfma_f32_16x16x32_bf16 v[20:23], v[64:67], v[88:91], v[20:23]\n"
    "ds_read_b64_tr_b16 v[64:65], v105 offset:10240\n"
    "ds_read_b64_tr_b16 v[66:67], v105 offset:15360\n"
    "s_waitcnt lgkmcnt(11)\n"
    "v_mfma_f32_16x16x32_bf16 v[24:27], v[68:71], v[80:83], v[24:27]\n"
    "v_mfma_f32_16x16x32_bf16 v[28:31], v[68:71], v[84:87], v[28:31]\n"
    "v_mfma_f32_16x16x32_bf16 v[32:35], v[68:71], v[88:91], v[32:35]\n"
    "ds_read_b64_tr_b16 v[68:69], v104 offset:10304\n"
    "ds_read_b64_tr_b16 v[70:71], v104 offset:15424\n"
    "s_waitcnt lgkmcnt(11)\n"
    "v_mfma_f32_16x16x32_bf16 v[36:39], v[72:75], v[80:83], v[36:39]\n"
    "v_mfma_f32_16x16x32_bf16 v[40:43], v[72:75], v[84:87], v[40:43]\n"
    "v_mfma_f32_16x16x32_bf16 v[44:47], v[72:75], v[88:91], v[44:47]\n"
    "ds_read_b64_tr_b16 v[72:73], v105 offset:10304\n"
    "ds_read_b64_tr_b16 v[74:75], v105 offset:15424\n"
    "s_waitcnt lgkmcnt(11)\n"
    "v_mfma_f32_16x16x32_bf16 v[48:51], v[76:79], v[80:83], v[48:51]\n"
    "v_mfma_f32_16x16x32_bf16 v[52:55], v[76:79], v[84:87], v[52:55]\n"
    "v_mfma_f32_16x16x32_bf16 v[56:59], v[76:79], v[88:91], v[56:59]\n"
    "ds_read_b64_tr_b16 v[76:77], v104 offset:10368\n"
    "ds_read_b64_tr_b16 v[78:79], v104 offset:15488\n"
    "s_waitcnt lgkmcnt(9)\n"
    "v_mfma_f32_16x16x32_bf16 v[0:3], v[60:63], v[92:95], v[0:3]\n"
    "v_mfma_f32_16x16x32_bf16 v[4:7], v[60:63], v[96:99], v[4:7]\n"
    "s_waitcnt lgkmcnt(8)\n"
    "v_mfma_f32_16x16x32_bf16 v[8:11], v[60:63], v[100:103], v[8:11]\n"
    "s_waitcnt lgkmcnt(6)\n"
    "v_mfma_f32_16x16x32_bf16 v[12:15], v[64:67], v[92:95], v[12:15]\n"
    "v_mfma_f32_16x16x32_bf16 v[16:19], v[64:67], v[96:99], v[16:19]\n"
    "v_mfma_f32_16x16x32_bf16 v[20:23], v[64:67], v[100:103], v[20:23]\n"
    "s_waitcnt lgkmcnt(0)\n"
    "s_barrier\n"
    "ds_read_b64_tr_b16 v[60:61], v104 offset:20480\n"
    "ds_read_b64_tr_b16 v[62:63], v104 offset:25600\n"
    "v_mfma_f32_16x16x32_bf16 v[24:27], v[68:71], v[92:95], v[24:27]\n"
    "ds_read_b128 v[80:83], v107 offset:0\n"
    "ds_read_b64_tr_b16 v[64:65], v105 offset:20480\n"
    "ds_read_b64_tr_b16 v[66:67], v105 offset:25600\n"
    "v_mfma_f32_16x16x32_bf16 v[28:31], v[68:71], v[96:99], v[28:31]\n"
    "ds_read_b128 v[84:87], v107 offset:1024\n"
    "v_mfma_f32_16x16x32_bf16 v[32:35], v[68:71], v[100:103], v[32:35]\n"
    "ds_read_b128 v[88:91], v107 offset:2048\n"
    "ds_read_b64_tr_b16 v[68:69], v104 offset:20544\n"
    "ds_read_b64_tr_b16 v[70:71], v104 offset:25664\n"
    "v_mfma_f32_16x16x32_bf16 v[36:39], v[72:75], v[92:95], v[36:39]\n"
    "v_mfma_f32_16x16x32_bf16 v[40:43], v[72:75], v[96:99], v[40:43]\n"
    "v_mfma_f32_16x16x32_bf16 v[44:47], v[72:75], v[100:103], v[44:47]\n"
    "ds_read_b64_tr_b16 v[72:73], v105 offset:20544\n"
    "ds_read_b64_tr_b16 v[74:75], v105 offset:25664\n"
    "v_mfma_f32_16x16x32_bf16 v[48:51], v[76:79], v[92:95], v[48:51]\n"
    "v_mfma_f32_16x16x32_bf16 v[52:55], v[76:79], v[96:99], v[52:55]\n"
    "v_mfma_f32_16x16x32_bf16 v[56:59], v[76:79], v[100:103], v[56:59]\n"
    "ds_read_b64_tr_b16 v[76:77], v104 offset:20608\n"
    "ds_read_b64_tr_b16 v[78:79], v104 offset:25728\n"
    "s_waitcnt lgkmcnt(10)\n"
    "v_mfma_f32_16x16x32_bf16 v[0:3], v[60:63], v[80:83], v[0:3]\n"
    "ds_read_b128 v[92:95], v107 offset:12288\n"
    "s_waitcnt lgkmcnt(8)\n"
    "v_mfma_f32_16x16x32_bf16 v[4:7], v[60:63], v[84:87], v[4:7]\n"
    "ds_read_b128 v[96:99], v107 offset:13312\n"
    "s_waitcnt lgkmcnt(8)\n"
    "v_mfma_f32_16x16x32_bf16 v[8:11], v[60:63], v[88:91], v[8:11]\n"
    "ds_read_b64_tr_b16 v[60:61], v104 offset:30720\n"
    "ds_read_b64_tr_b16 v[62:63], v104 offset:35840\n"
    "v_mfma_f32_16x16x32_bf16 v[12:15], v[64:67], v[80:83], v[12:15]\n"
    "ds_read_b128 v[100:103], v107 offset:14336\n"
    "v_mfma_f32_16x16x32_bf16 v[16:19], v[64:67], v[84:87], v[16:19]\n"
    "v_mfma_f32_16x16x32_bf16 v[20:23], v[64:67], v[88:91], v[20:23]\n"
    "ds_read_b64_tr_b16 v[64:65], v105 offset:30720\n"
    "ds_read_b64_tr_b16 v[66:67], v105 offset:35840\n"
    "s_waitcnt lgkmcnt(11)\n"
    "v_mfma_f32_16x16x32_bf16 v[24:27], v[68:71], v[80:83], v[24:27]\n"
    "v_mfma_f32_16x16x32_bf16 v[28:31], v[68:71], v[84:87], v[28:31]\n"
    "v_mfma_f32_16x16x32_bf16 v[32:35], v[68:71], v[88:91], v[32:35]\n"
    "ds_read_b64_tr_b16 v[68:69], v104 offset:30784\n"
    "ds_read_b64_tr_b16 v[70:71], v104 offset:35904\n"
    "s_waitcnt lgkmcnt(11)\n"
    "v_mfma_f32_16x16x32_bf16 v[36:39], v[72:75], v[80:83], v[36:39]\n"
    "v_mfma_f32_16x16x32_bf16 v[40:43], v[72:75], v[84:87], v[40:43]\n"
    "v_mfma_f32_16x16x32_bf16 v[44:47], v[72:75], v[88:91], v[44:47]\n"
    "ds_read_b64_tr_b16 v[72:73], v105 offset:30784\n"
    "ds_read_b64_tr_b16 v[74:75], v105 offset:35904\n"
    "s_waitcnt lgkmcnt(11)\n"
    "v_mfma_f32_16x16x32_bf16 v[48:51], v[76:79], v[80:83], v[48:51]\n"
    "v_mfma_f32_16x16x32_bf16 v[52:55], v[76:79], v[84:87], v[52:55]\n"
    "v_mfma_f32_16x16x32_bf16 v[56:59], v[76:79], v[88:91], v[56:59]\n"
    "ds_read_b64_tr_b16 v[76:77], v104 offset:30848\n"
    "ds_read_b64_tr_b16 v[78:79], v104 offset:35968\n"
    "s_waitcnt lgkmcnt(9)\n"
    "v_mfma_f32_16x16x32_bf16 v[0:3], v[60:63], v[92:95], v[0:3]\n"
    "v_mfma_f32_16x16x32_bf16 v[4:7], v[60:63], v[96:99], v[4:7]\n"
    "s_waitcnt lgkmcnt(8)\n"
    "v_mfma_f32_16x16x32_bf16 v[8:11], v[60:63], v[100:103], v[8:11]\n"
    "s_waitcnt lgkmcnt(6)\n"
    "v_mfma_f32_16x16x32_bf16 v[12:15], v[64:67], v[92:95], v[12:15]\n"
    "v_mfma_f32_16x16x32_bf16 v[16:19], v[64:67], v[96:99], v[16:19]\n"
    "v_mfma_f32_16x16x32_bf16 v[20:23], v[64:67], v[100:103], v[20:23]\n"
    "s_waitcnt lgkmcnt(0)\n"
    "s_barrier\n"
    "ds_read_b64_tr_b16 v[60:61], v104 offset:40960\n"
    "ds_read_b64_tr_b16 v[62:63], v104 offset:46080\n"
    "v_mfma_f32_16x16x32_bf16 v[24:27], v[68:71], v[92:95], v[24:27]\n"
    "ds_read_b128 v[80:83], v107 offset:24576\n"
    "ds_read_b64_tr_b16 v[64:65], v105 offset:40960\n"
    "ds_read_b64_tr_b16 v[66:67], v105 offset:46080\n"
    "v_mfma_f32_16x16x32_bf16 v[28:31], v[68:71], v[96:99], v[28:31]\n"
    "ds_read_b128 v[84:87], v107 offset:25600\n"
    "v_mfma_f32_16x16x32_bf16 v[32:35], v[68:71], v[100:103], v[32:35]\n"
    "ds_read_b128 v[88:91], v107 offset:26624\n"
    "ds_read_b64_tr_b16 v[68:69], v104 offset:41024\n"
    "ds_read_b64_tr_b16 v[70:71], v104 offset:46144\n"
    "v_mfma_f32_16x16x32_bf16 v[36:39], v[72:75], v[92:95], v[36:39]\n"
    "v_mfma_f32_16x16x32_bf16 v[40:43], v[72:75], v[96:99], v[40:43]\n"
    "v_mfma_f32_16x16x32_bf16 v[44:47], v[72:75], v[100:103], v[44:47]\n"
    "ds_read_b64_tr_b16 v[72:73], v105 offset:41024\n"
    "ds_read_b64_tr_b16 v[74:75], v105 offset:46144\n"
    "v_mfma_f32_16x16x32_bf16 v[48:51], v[76:79], v[92:95], v[48:51]\n"
    "v_mfma_f32_16x16x32_bf16 v[52:55], v[76:79], v[96:99], v[52:55]\n"
    "v_mfma_f32_16x16x32_bf16 v[56:59], v[76:79], v[100:103], v[56:59]\n"
    "ds_read_b64_tr_b16 v[76:77], v104 offset:41088\n"
    "ds_read_b64_tr_b16 v[78:79], v104 offset:46208\n"
    "s_waitcnt lgkmcnt(10)\n"
    "v_mfma_f32_16x16x32_bf16 v[0:3], v[60:63], v[80:83], v[0:3]\n"
    "ds_read_b128 v[92:95], v107 offset:36864\n"
    "s_waitcnt lgkmcnt(8)\n"
    "v_mfma_f32_16x16x32_bf16 v[4:7], v[60:63], v[84:87], v[4:7]\n"
    "ds_read_b128 v[96:99], v107 offset:37888\n"
    "s_waitcnt lgkmcnt(8)\n"
    "v_mfma_f32_16x16x32_bf16 v[8:11], v[60:63], v[88:91], v[8:11]\n"
    "ds_read_b64_tr_b16 v[60:61], v104 offset:51200\n"
    "ds_read_b64_tr_b16 v[62:63], v104 offset:56320\n"
    "v_mfma_f32_16x16x32_bf16 v[12:15], v[64:67], v[80:83], v[12:15]\n"
    "ds_read_b128 v[100:103], v107 offset:38912\n"
    "v_mfma_f32_16x16x32_bf16 v[16:19], v[64:67], v[84:87], v[16:19]\n"
    "v_mfma_f32_16x16x32_bf16 v[20:23], v[64:67], v[88:91], v[20:23]\n"
    "ds_read_b64_tr_b16 v[64:65], v105 offset:51200\n"
    "ds_read_b64_tr_b16 v[66:67], v105 offset:56320\n"
    "s_waitcnt lgkmcnt(11)\n"
    "v_mfma_f32_16x16x32_bf16 v[24:27], v[68:71], v[80:83], v[24:27]\n"
    "v_mfma_f32_16x16x32_bf16 v[28:31], v[68:71], v[84:87], v[28:31]\n"
    "v_mfma_f32_16x16x32_bf16 v[32:35], v[68:71], v[88:91], v[32:35]\n"
    "ds_read_b64_tr_b16 v[68:69], v104 offset:51264\n"
    "ds_read_b64_tr_b16 v[70:71], v104 offset:56384\n"
    "s_waitcnt lgkmcnt(11)\n"
    "v_mfma_f32_16x16x32_bf16 v[36:39], v[72:75], v[80:83], v[36:39]\n"
    "v_mfma_f32_16x16x32_bf16 v[40:43], v[72:75], v[84:87], v[40:43]\n"
    "v_mfma_f32_16x16x32_bf16 v[44:47], v[72:75], v[88:91], v[44:47]\n"
    "ds_read_b64_tr_b16 v[72:73], v105 offset:51264\n"
    "ds_read_b64_tr_b16 v[74:75], v105 offset:56384\n"
    "s_waitcnt lgkmcnt(11)\n"
    "v_mfma_f32_16x16x32_bf16 v[48:51], v[76:79], v[80:83], v[48:51]\n"
    "v_mfma_f32_16x16x32_bf16 v[52:55], v[76:79], v[84:87], v[52:55]\n"
    "v_mfma_f32_16x16x32_bf16 v[56:59], v[76:79], v[88:91], v[56:59]\n"
    "ds_read_b64_tr_b16 v[76:77], v104 offset:51328\n"
    "ds_read_b64_tr_b16 v[78:79], v104 offset:56448\n"
    "s_waitcnt lgkmcnt(9)\n"
    "v_mfma_f32_16x16x32_bf16 v[0:3], v[60:63], v[92:95], v[0:3]\n"
    "v_mfma_f32_16x16x32_bf16 v[4:7], v[60:63], v[96:99], v[4:7]\n"
    "s_waitcnt lgkmcnt(8)\n"
    "v_mfma_f32_16x16x32_bf16 v[8:11], v[60:63], v[100:103], v[8:11]\n"
    "s_waitcnt lgkmcnt(6)\n"
    "v_mfma_f32_16x16x32_bf16 v[12:15], v[64:67], v[92:95], v[12:15]\n"
    "v_mfma_f32_16x16x32_bf16 v[16:19], v[64:67], v[96:99], v[16:19]\n"
    "v_mfma_f32_16x16x32_bf16 v[20:23], v[64:67], v[100:103], v[20:23]\n"
    "s_waitcnt lgkmcnt(0)\n"
    "s_barrier\n"
    "ds_read_b64_tr_b16 v[60:61], v104 offset:0\n"
    "ds_read_b64_tr_b16 v[62:63], v104 offset:5120\n"
    "v_mfma_f32_16x16x32_bf16 v[24:27], v[68:71], v[92:95], v[24:27]\n"
    "ds_read_b128 v[80:83], v106 offset:0\n"
    "ds_read_b64_tr_b16 v[64:65], v105 offset:0\n"
    "ds_read_b64_tr_b16 v[66:67], v105 offset:5120\n"
    "v_mfma_f32_16x16x32_bf16 v[28:31], v[68:71], v[96:99], v[28:31]\n"
    "ds_read_b128 v[84:87], v106 offset:1024\n"
    "v_mfma_f32_16x16x32_bf16 v[32:35], v[68:71], v[100:103], v[32:35]\n"
    "ds_read_b128 v[88:91], v106 offset:2048\n"
    "ds_read_b64_tr_b16 v[68:69], v104 offset:64\n"
    "ds_read_b64_tr_b16 v[70:71], v104 offset:5184\n"
    "v_mfma_f32_16x16x32_bf16 v[36:39], v[72:75], v[92:95], v[36:39]\n"
    "v_mfma_f32_16x16x32_bf16 v[40:43], v[72:75], v[96:99], v[40:43]\n"
    "v_mfma_f32_16x16x32_bf16 v[44:47], v[72:75], v[100:103], v[44:47]\n"
    "ds_read_b64_tr_b16 v[72:73], v105 offset:64\n"
    "ds_read_b64_tr_b16 v[74:75], v105 offset:5184\n"
    "v_mfma_f32_16x16x32_bf16 v[48:51], v[76:79], v[92:95], v[48:51]\n"
    "v_mfma_f32_16x16x32_bf16 v[52:55], v[76:79], v[96:99], v[52:55]\n"
    "v_mfma_f32_16x16x32_bf16 v[56:59], v[76:79], v[100:103], v[56:59]\n"
    "ds_read_b64_tr_b16 v[76:77], v104 offset:128\n"
    "ds_read_b64_tr_b16 v[78:79], v104 offset:5248\n"
    "s_waitcnt lgkmcnt(10)\n"
    "v_mfma_f32_16x16x32_bf16 v[0:3], v[60:63], v[80:83], v[0:3]\n"
    "ds_read_b128 v[92:95], v106 offset:12288\n"
    "s_waitcnt lgkmcnt(8)\n"
    "v_mfma_f32_16x16x32_bf16 v[4:7], v[60:63], v[84:87], v[4:7]\n"
    "ds_read_b128 v[96:99], v106 offset:13312\n"
    "s_waitcnt lgkmcnt(8)\n"
    "v_mfma_f32_16x16x32_bf16 v[8:11], v[60:63], v[88:91], v[8:11]\n"
    "ds_read_b64_tr_b16 v[60:61], v104 offset:10240\n"
    "ds_read_b64_tr_b16 v[62:63], v104 offset:15360\n"
    "v_mfma_f32_16x16x32_bf16 v[12:15], v[64:67], v[80:83], v[12:15]\n"
    "ds_read_b128 v[100:103], v106 offset:14336\n"
    "v_mfma_f32_16x16x32_bf16 v[16:19], v[64:67], v[84:87], v[16:19]\n"
    "v_mfma_f32_16x16x32_bf16 v[20:23], v[64:67], v[88:91], v[20:23]\n"
    "ds_read_b64_tr_b16 v[64:65], v105 offset:10240\n"
    "ds_read_b64_tr_b16 v[66:67], v105 offset:15360\n"
    "s_waitcnt lgkmcnt(11)\n"
    "v_mfma_f32_16x16x32_bf16 v[24:27], v[68:71], v[80:83], v[24:27]\n"
    "v_mfma_f32_16x16x32_bf16 v[28:31], v[68:71], v[84:87], v[28:31]\n"
    "v_mfma_f32_16x16x32_bf16 v[32:35], v[68:71], v[88:91], v[32:35]\n"
    "ds_read_b64_tr_b16 v[68:69], v104 offset:10304\n"
    "ds_read_b64_tr_b16 v[70:71], v104 offset:15424\n"
    "s_waitcnt lgkmcnt(11)\n"
    "v_mfma_f32_16x16x32_bf16 v[36:39], v[72:75], v[80:83], v[36:39]\n"
    "v_mfma_f32_16x16x32_bf16 v[40:43], v[72:75], v[84:87], v[40:43]\n"
    "v_mfma_f32_16x16x32_bf16 v[44:47], v[72:75], v[88:91], v[44:47]\n"
    "ds_read_b64_tr_b16 v[72:73], v105 offset:10304\n"
    "ds_read_b64_tr_b16 v[74:75], v105 offset:15424\n"
    "s_waitcnt lgkmcnt(11)\n"
    "v_mfma_f32_16x16x32_bf16 v[48:51], v[76:79], v[80:83], v[48:51]\n"
    "v_mfma_f32_16x16x32_bf16 v[52:55], v[76:79], v[84:87], v[52:55]\n"
    "v_mfma_f32_16x16x32_bf16 v[56:59], v[76:79], v[88:91], v[56:59]\n"
    "ds_read_b64_tr_b16 v[76:77], v104 offset:10368\n"
    "ds_read_b64_tr_b16 v[78:79], v104 offset:15488\n"
    "s_waitcnt lgkmcnt(9)\n"
    "v_mfma_f32_16x16x32_bf16 v[0:3], v[60:63], v[92:95], v[0:3]\n"
    "v_mfma_f32_16x16x32_bf16 v[4:7], v[60:63], v[96:99], v[4:7]\n"
    "s_waitcnt lgkmcnt(8)\n"
    "v_mfma_f32_16x16x32_bf16 v[8:11], v[60:63], v[100:103], v[8:11]\n"
    "s_waitcnt lgkmcnt(6)\n"
    "v_mfma_f32_16x16x32_bf16 v[12:15], v[64:67], v[92:95], v[12:15]\n"
    "v_mfma_f32_16x16x32_bf16 v[16:19], v[64:67], v[96:99], v[16:19]\n"
    "v_mfma_f32_16x16x32_bf16 v[20:23], v[64:67], v[100:103], v[20:23]\n"
    "s_waitcnt lgkmcnt(0)\n"
    "s_barrier\n"
    "ds_read_b64_tr_b16 v[60:61], v104 offset:20480\n"
    "ds_read_b64_tr_b16 v[62:63], v104 offset:25600\n"
    "v_mfma_f32_16x16x32_bf16 v[24:27], v[68:71], v[92:95], v[24:27]\n"
    "ds_read_b128 v[80:83], v106 offset:24576\n"
    "ds_read_b64_tr_b16 v[64:65], v105 offset:20480\n"
    "ds_read_b64_tr_b16 v[66:67], v105 offset:25600\n"
    "v_mfma_f32_16x16x32_bf16 v[28:31], v[68:71], v[96:99], v[28:31]\n"
    "ds_read_b128 v[84:87], v106 offset:25600\n"
    "v_mfma_f32_16x16x32_bf16 v[32:35], v[68:71], v[100:103], v[32:35]\n"
    "ds_read_b128 v[88:91], v106 offset:26624\n"
    "ds_read_b64_tr_b16 v[68:69], v104 offset:20544\n"
    "ds_read_b64_tr_b16 v[70:71], v104 offset:25664\n"
    "v_mfma_f32_16x16x32_bf16 v[36:39], v[72:75], v[92:95], v[36:39]\n"
    "v_mfma_f32_16x16x32_bf16 v[40:43], v[72:75], v[96:99], v[40:43]\n"
    "v_mfma_f32_16x16x32_bf16 v[44:47], v[72:75], v[100:103], v[44:47]\n"
    "ds_read_b64_tr_b16 v[72:73], v105 offset:20544\n"
    "ds_read_b64_tr_b16 v[74:75], v105 offset:25664\n"
    "v_mfma_f32_16x16x32_bf16 v[48:51], v[76:79], v[92:95], v[48:51]\n"
    "v_mfma_f32_16x16x32_bf16 v[52:55], v[76:79], v[96:99], v[52:55]\n"
    "v_mfma_f32_16x16x32_bf16 v[56:59], v[76:79], v[100:103], v[56:59]\n"
    "ds_read_b64_tr_b16 v[76:77], v104 offset:20608\n"
    "ds_read_b64_tr_b16 v[78:79], v104 offset:25728\n"
    "s_waitcnt lgkmcnt(10)\n"
    "v_mfma_f32_16x16x32_bf16 v[0:3], v[60:63], v[80:83], v[0:3]\n"
    "ds_read_b128 v[92:95], v106 offset:36864\n"
    "s_waitcnt lgkmcnt(8)\n"
    "v_mfma_f32_16x16x32_bf16 v[4:7], v[60:63], v[84:87], v[4:7]\n"
    "ds_read_b128 v[96:99], v106 offset:37888\n"
    "s_waitcnt lgkmcnt(8)\n"
    "v_mfma_f32_16x16x32_bf16 v[8:11], v[60:63], v[88:91], v[8:11]\n"
    "ds_read_b64_tr_b16 v[60:61], v104 offset:30720\n"
    "ds_read_b64_tr_b16 v[62:63], v104 offset:35840\n"
    "v_mfma_f32_16x16x32_bf16 v[12:15], v[64:67], v[80:83], v[12:15]\n"
    "ds_read_b128 v[100:103], v106 offset:38912\n"
    "v_mfma_f32_16x16x32_bf16 v[16:19], v[64:67], v[84:87], v[16:19]\n"
    "v_mfma_f32_16x16x32_bf16 v[20:23], v[64:67], v[88:91], v[20:23]\n"
    "ds_read_b64_tr_b16 v[64:65], v105 offset:30720\n"
    "ds_read_b64_tr_b16 v[66:67], v105 offset:35840\n"
    "s_waitcnt lgkmcnt(11)\n"
    "v_mfma_f32_16x16x32_bf16 v[24:27], v[68:71], v[80:83], v[24:27]\n"
    "v_mfma_f32_16x16x32_bf16 v[28:31], v[68:71], v[84:87], v[28:31]\n"
    "v_mfma_f32_16x16x32_bf16 v[32:35], v[68:71], v[88:91], v[32:35]\n"
    "ds_read_b64_tr_b16 v[68:69], v104 offset:30784\n"
    "ds_read_b64_tr_b16 v[70:71], v104 offset:35904\n"
    "s_waitcnt lgkmcnt(11)\n"
    "v_mfma_f32_16x16x32_bf16 v[36:39], v[72:75], v[80:83], v[36:39]\n"
    "v_mfma_f32_16x16x32_bf16 v[40:43], v[72:75], v[84:87], v[40:43]\n"
    "v_mfma_f32_16x16x32_bf16 v[44:47], v[72:75], v[88:91], v[44:47]\n"
    "ds_read_b64_tr_b16 v[72:73], v105 offset:30784\n"
    "ds_read_b64_tr_b16 v[74:75], v105 offset:35904\n"
    "s_waitcnt lgkmcnt(11)\n"
    "v_mfma_f32_16x16x32_bf16 v[48:51], v[76:79], v[80:83], v[48:51]\n"
    "v_mfma_f32_16x16x32_bf16 v[52:55], v[76:79], v[84:87], v[52:55]\n"
    "v_mfma_f32_16x16x32_bf16 v[56:59], v[76:79], v[88:91], v[56:59]\n"
    "ds_read_b64_tr_b16 v[76:77], v104 offset:30848\n"
    "ds_read_b64_tr_b16 v[78:79], v104 offset:35968\n"
    "s_waitcnt lgkmcnt(9)\n"
    "v_mfma_f32_16x16x32_bf16 v[0:3], v[60:63], v[92:95], v[0:3]\n"
    "v_mfma_f32_16x16x32_bf16 v[4:7], v[60:63], v[96:99], v[4:7]\n"
    "s_waitcnt lgkmcnt(8)\n"
    "v_mfma_f32_16x16x32_bf16 v[8:11], v[60:63], v[100:103], v[8:11]\n"
    "s_waitcnt lgkmcnt(6)\n"
    "v_mfma_f32_16x16x32_bf16 v[12:15], v[64:67], v[92:95], v[12:15]\n"
    "v_mfma_f32_16x16x32_bf16 v[16:19], v[64:67], v[96:99], v[16:19]\n"
    "v_mfma_f32_16x16x32_bf16 v[20:23], v[64:67], v[100:103], v[20:23]\n"
    "s_waitcnt lgkmcnt(0)\n"
    "s_barrier\n"
    "ds_read_b64_tr_b16 v[60:61], v104 offset:40960\n"
    "ds_read_b64_tr_b16 v[62:63], v104 offset:46080\n"
    "v_mfma_f32_16x16x32_bf16 v[24:27], v[68:71], v[92:95], v[24:27]\n"
    "ds_read_b128 v[80:83], v107 offset:0\n"
    "ds_read_b64_tr_b16 v[64:65], v105 offset:40960\n"
    "ds_read_b64_tr_b16 v[66:67], v105 offset:46080\n"
    "v_mfma_f32_16x16x32_bf16 v[28:31], v[68:71], v[96:99], v[28:31]\n"
    "ds_read_b128 v[84:87], v107 offset:1024\n"
    "v_mfma_f32_16x16x32_bf16 v[32:35], v[68:71], v[100:103], v[32:35]\n"
    "ds_read_b128 v[88:91], v107 offset:2048\n"
    "ds_read_b64_tr_b16 v[68:69], v104 offset:41024\n"
    "ds_read_b64_tr_b16 v[70:71], v104 offset:46144\n"
    "v_mfma_f32_16x16x32_bf16 v[36:39], v[72:75], v[92:95], v[36:39]\n"
    "v_mfma_f32_16x16x32_bf16 v[40:43], v[72:75], v[96:99], v[40:43]\n"
    "v_mfma_f32_16x16x32_bf16 v[44:47], v[72:75], v[100:103], v[44:47]\n"
    "ds_read_b64_tr_b16 v[72:73], v105 offset:41024\n"
    "ds_read_b64_tr_b16 v[74:75], v105 offset:46144\n"
    "v_mfma_f32_16x16x32_bf16 v[48:51], v[76:79], v[92:95], v[48:51]\n"
    "v_mfma_f32_16x16x32_bf16 v[52:55], v[76:79], v[96:99], v[52:55]\n"
    "v_mfma_f32_16x16x32_bf16 v[56:59], v[76:79], v[100:103], v[56:59]\n"
    "ds_read_b64_tr_b16 v[76:77], v104 offset:41088\n"
    "ds_read_b64_tr_b16 v[78:79], v104 offset:46208\n"
    "s_waitcnt lgkmcnt(10)\n"
    "v_mfma_f32_16x16x32_bf16 v[0:3], v[60:63], v[80:83], v[0:3]\n"
    "ds_read_b128 v[92:95], v107 offset:12288\n"
    "s_waitcnt lgkmcnt(8)\n"
    "v_mfma_f32_16x16x32_bf16 v[4:7], v[60:63], v[84:87], v[4:7]\n"
    "ds_read_b128 v[96:99], v107 offset:13312\n"
    "s_waitcnt lgkmcnt(8)\n"
    "v_mfma_f32_16x16x32_bf16 v[8:11], v[60:63], v[88:91], v[8:11]\n"
    "ds_read_b64_tr_b16 v[60:61], v104 offset:51200\n"
    "ds_read_b64_tr_b16 v[62:63], v104 offset:56320\n"
    "v_mfma_f32_16x16x32_bf16 v[12:15], v[64:67], v[80:83], v[12:15]\n"
    "ds_read_b128 v[100:103], v107 offset:14336\n"
    "v_mfma_f32_16x16x32_bf16 v[16:19], v[64:67], v[84:87], v[16:19]\n"
    "v_mfma_f32_16x16x32_bf16 v[20:23], v[64:67], v[88:91], v[20:23]\n"
    "ds_read_b64_tr_b16 v[64:65], v105 offset:51200\n"
    "ds_read_b64_tr_b16 v[66:67], v105 offset:56320\n"
    "s_waitcnt lgkmcnt(11)\n"
    "v_mfma_f32_16x16x32_bf16 v[24:27], v[68:71], v[80:83], v[24:27]\n"
    "v_mfma_f32_16x16x32_bf16 v[28:31], v[68:71], v[84:87], v[28:31]\n"
    "v_mfma_f32_16x16x32_bf16 v[32:35], v[68:71], v[88:91], v[32:35]\n"
    "ds_read_b64_tr_b16 v[68:69], v104 offset:51264\n"
    "ds_read_b64_tr_b16 v[70:71], v104 offset:56384\n"
    "s_waitcnt lgkmcnt(11)\n"
    "v_mfma_f32_16x16x32_bf16 v[36:39], v[72:75], v[80:83], v[36:39]\n"
    "v_mfma_f32_16x16x32_bf16 v[40:43], v[72:75], v[84:87], v[40:43]\n"
    "v_mfma_f32_16x16x32_bf16 v[44:47], v[72:75], v[88:91], v[44:47]\n"
    "ds_read_b64_tr_b16 v[72:73], v105 offset:51264\n"
    "ds_read_b64_tr_b16 v[74:75], v105 offset:56384\n"
    "s_waitcnt lgkmcnt(11)\n"
    "v_mfma_f32_16x16x32_bf16 v[48:51], v[76:79], v[80:83], v[48:51]\n"
    "v_mfma_f32_16x16x32_bf16 v[52:55], v[76:79], v[84:87], v[52:55]\n"
    "v_mfma_f32_16x16x32_bf16 v[56:59], v[76:79], v[88:91], v[56:59]\n"
    "ds_read_b64_tr_b16 v[76:77], v104 offset:51328\n"
    "ds_read_b64_tr_b16 v[78:79], v104 offset:56448\n"
    "s_waitcnt lgkmcnt(9)\n"
    "v_mfma_f32_16x16x32_bf16 v[0:3], v[60:63], v[92:95], v[0:3]\n"
    "v_mfma_f32_16x16x32_bf16 v[4:7], v[60:63], v[96:99], v[4:7]\n"
    "s_waitcnt lgkmcnt(8)\n"
    "v_mfma_f32_16x16x32_bf16 v[8:11], v[60:63], v[100:103], v[8:11]\n"
    "s_waitcnt lgkmcnt(6)\n"
    "v_mfma_f32_16x16x32_bf16 v[12:15], v[64:67], v[92:95], v[12:15]\n"
    "v_mfma_f32_16x16x32_bf16 v[16:19], v[64:67], v[96:99], v[16:19]\n"
    "v_mfma_f32_16x16x32_bf16 v[20:23], v[64:67], v[100:103], v[20:23]\n"
    "s_waitcnt lgkmcnt(0)\n"
    "s_barrier\n"
    "ds_read_b64_tr_b16 v[60:61], v104 offset:0\n"
    "ds_read_b64_tr_b16 v[62:63], v104 offset:5120\n"
    "v_mfma_f32_16x16x32_bf16 v[24:27], v[68:71], v[92:95], v[24:27]\n"
    "ds_read_b128 v[80:83], v107 offset:24576\n"
    "ds_read_b64_tr_b16 v[64:65], v105 offset:0\n"
    "ds_read_b64_tr_b16 v[66:67], v105 offset:5120\n"
    "v_mfma_f32_16x16x32_bf16 v[28:31], v[68:71], v[96:99], v[28:31]\n"
    "ds_read_b128 v[84:87], v107 offset:25600\n"
    "v_mfma_f32_16x16x32_bf16 v[32:35], v[68:71], v[100:103], v[32:35]\n"
    "ds_read_b128 v[88:91], v107 offset:26624\n"
    "ds_read_b64_tr_b16 v[68:69], v104 offset:64\n"
    "ds_read_b64_tr_b16 v[70:71], v104 offset:5184\n"
    "v_mfma_f32_16x16x32_bf16 v[36:39], v[72:75], v[92:95], v[36:39]\n"
    "v_mfma_f32_16x16x32_bf16 v[40:43], v[72:75], v[96:99], v[40:43]\n"
    "v_mfma_f32_16x16x32_bf16 v[44:47], v[72:75], v[100:103], v[44:47]\n"
    "ds_read_b64_tr_b16 v[72:73], v105 offset:64\n"
    "ds_read_b64_tr_b16 v[74:75], v105 offset:5184\n"
    "v_mfma_f32_16x16x32_bf16 v[48:51], v[76:79], v[92:95], v[48:51]\n"
    "v_mfma_f32_16x16x32_bf16 v[52:55], v[76:79], v[96:99], v[52:55]\n"
    "v_mfma_f32_16x16x32_bf16 v[56:59], v[76:79], v[100:103], v[56:59]\n"
    "ds_read_b64_tr_b16 v[76:77], v104 offset:128\n"
    "ds_read_b64_tr_b16 v[78:79], v104 offset:5248\n"
    "s_waitcnt lgkmcnt(10)\n"
    "v_mfma_f32_16x16x32_bf16 v[0:3], v[60:63], v[80:83], v[0:3]\n"
    "ds_read_b128 v[92:95], v107 offset:36864\n"
    "s_waitcnt lgkmcnt(8)\n"
    "v_mfma_f32_16x16x32_bf16 v[4:7], v[60:63], v[84:87], v[4:7]\n"
    "ds_read_b128 v[96:99], v107 offset:37888\n"
    "s_waitcnt lgkmcnt(8)\n"
    "v_mfma_f32_16x16x32_bf16 v[8:11], v[60:63], v[88:91], v[8:11]\n"
    "ds_read_b64_tr_b16 v[60:61], v104 offset:10240\n"
    "ds_read_b64_tr_b16 v[62:63], v104 offset:15360\n"
    "v_mfma_f32_16x16x32_bf16 v[12:15], v[64:67], v[80:83], v[12:15]\n"
    "ds_read_b128 v[100:103], v107 offset:38912\n"
    "v_mfma_f32_16x16x32_bf16 v[16:19], v[64:67], v[84:87], v[16:19]\n"
    "v_mfma_f32_16x16x32_bf16 v[20:23], v[64:67], v[88:91], v[20:23]\n"
    "ds_read_b64_tr_b16 v[64:65], v105 offset:10240\n"
    "ds_read_b64_tr_b16 v[66:67], v105 offset:15360\n"
    "s_waitcnt lgkmcnt(11)\n"
    "v_mfma_f32_16x16x32_bf16 v[24:27], v[68:71], v[80:83], v[24:27]\n"
    "v_mfma_f32_16x16x32_bf16 v[28:31], v[68:71], v[84:87], v[28:31]\n"
    "v_mfma_f32_16x16x32_bf16 v[32:35], v[68:71], v[88:91], v[32:35]\n"
    "ds_read_b64_tr_b16 v[68:69], v104 offset:10304\n"
    "ds_read_b64_tr_b16 v[70:71], v104 offset:15424\n"
    "s_waitcnt lgkmcnt(11)\n"
    "v_mfma_f32_16x16x32_bf16 v[36:39], v[72:75], v[80:83], v[36:39]\n"
    "v_mfma_f32_16x16x32_bf16 v[40:43], v[72:75], v[84:87], v[40:43]\n"
    "v_mfma_f32_16x16x32_bf16 v[44:47], v[72:75], v[88:91], v[44:47]\n"
    "ds_read_b64_tr_b16 v[72:73], v105 offset:10304\n"
    "ds_read_b64_tr_b16 v[74:75], v105 offset:15424\n"
    "s_waitcnt lgkmcnt(11)\n"
    "v_mfma_f32_16x16x32_bf16 v[48:51], v[76:79], v[80:83], v[48:51]\n"
    "v_mfma_f32_16x16x32_bf16 v[52:55], v[76:79], v[84:87], v[52:55]\n"
    "v_mfma_f32_16x16x32_bf16 v[56:59], v[76:79], v[88:91], v[56:59]\n"
    "ds_read_b64_tr_b16 v[76:77], v104 offset:10368\n"
    "ds_read_b64_tr_b16 v[78:79], v104 offset:15488\n"
    "s_waitcnt lgkmcnt(9)\n"
    "v_mfma_f32_16x16x32_bf16 v[0:3], v[60:63], v[92:95], v[0:3]\n"
    "v_mfma_f32_16x16x32_bf16 v[4:7], v[60:63], v[96:99], v[4:7]\n"
    "s_waitcnt lgkmcnt(8)\n"
    "v_mfma_f32_16x16x32_bf16 v[8:11], v[60:63], v[100:103], v[8:11]\n"
    "s_waitcnt lgkmcnt(6)\n"
    "v_mfma_f32_16x16x32_bf16 v[12:15], v[64:67], v[92:95], v[12:15]\n"
    "v_mfma_f32_16x16x32_bf16 v[16:19], v[64:67], v[96:99], v[16:19]\n"
    "v_mfma_f32_16x16x32_bf16 v[20:23], v[64:67], v[100:103], v[20:23]\n"
    "s_waitcnt lgkmcnt(4)\n"
    "v_mfma_f32_16x16x32_bf16 v[24:27], v[68:71], v[92:95], v[24:27]\n"
    "v_mfma_f32_16x16x32_bf16 v[28:31], v[68:71], v[96:99], v[28:31]\n"
    "v_mfma_f32_16x16x32_bf16 v[32:35], v[68:71], v[100:103], v[32:35]\n"
    "s_waitcnt lgkmcnt(2)\n"
    "v_mfma_f32_16x16x32_bf16 v[36:39], v[72:75], v[92:95], v[36:39]\n"
    "v_mfma_f32_16x16x32_bf16 v[40:43], v[72:75], v[96:99], v[40:43]\n"
    "v_mfma_f32_16x16x32_bf16 v[44:47], v[72:75], v[100:103], v[44:47]\n"
    "s_waitcnt lgkmcnt(0)\n"
    "v_mfma_f32_16x16x32_bf16 v[48:51], v[76:79], v[92:95], v[48:51]\n"
    "v_mfma_f32_16x16x32_bf16 v[52:55], v[76:79], v[96:99], v[52:55]\n"
    "v_mfma_f32_16x16x32_bf16 v[56:59], v[76:79], v[100:103], v[56:59]\n"
    ".Lend_%=:\n"
    "s_setprio 0\n"
    "s_nop 7\n"
    "s_nop 3\n"
      : "={v[0:31]}"(ac0), "={v[32:63]}"(ac1)
      : "{v241}"(ln16), "{s69}"(ldsb), "{s36}"(160u * hh), "{s37}"(ldsb + P3O + 3072u * rg), "{s38}"((w < 4u ? 3072u : 6144u) * rg),
        "{s39}"((bid & ~24u) * 128u), "{s88}"(bid * 128u + 16u), "{s89}"(rg * 20480u),
        "{s[40:43]}"(srdWo), "{s[44:47]}"(srdO), "{s[48:51]}"(srdM), "{s52}"(ldsb + 5120u * rg), "{s53}"(ldsb + P3O + 6144u * rg),
        "{s54}"(sob[0]), "{s55}"(sob[1]), "{s56}"(sob[2]), "{s57}"(sob[3]), "{s58}"(swb[0]), "{s59}"(swb[1]), "{s60}"(swb[2]), "{s61}"(swb[3]),
        "{s63}"(w), "{s67}"(16u + 4u * jq), "{s68}"(jq)
      : "v64", "v65", "v66", "v67", "v68", "v69", "v70", "v71", "v72", "v73", "v74", "v75", "v76", "v77", "v78", "v79", "v80", "v81", "v82", "v83", "v84", "v85", "v86", "v87", "v88", "v89", "v90", "v91", "v92", "v93", "v94", "v95", "v96", "v97", "v98", "v99", "v100", "v101", "v102", "v103", "v104", "v105", "v106", "v107", "v108", "v109", "v110", "v111", "v112", "v113", "v114", "v115", "v116", "v117", "v118", "v119", "v120", "v121", "v122", "v123", "v124", "v125", "s62", "s64", "s65", "s66", "vcc", "scc", "memory");
  }
  const unsigned ln = ln16 >> 4, g3 = ln >> 4;
  f32x4 acc[5][3];
#pragma unroll
  for (int i = 0; i < 15; ++i)
#pragma unroll
    for (int e = 0; e < 4; ++e) acc[i / 3][i % 3][e] = 4 * i + e < 32 ? ac0[4 * i + e] : ac1[4 * i + e - 32];
  unsigned ech2 = mb_load(Ows, peer_bid(bid, 4u + (ln & 3u)), 0u);
  auto fraga = [&](int i) { return i < 6 ? acc[2 * (i / 3)][i % 3] : (i == 6 ? acc[4][0] : acc[4][2]); };
  auto fragb = [&](int i) { return i < 6 ? acc[2 * (i / 3) + 1][i % 3] : (i == 6 ? acc[4][1] : acc[4][2]); };
  if (rg != hg) {
    char* px = Ows + WS_PX + ((size_t)peer_bid(bid, 4u + rg) * 4u + hg) * 16384u + hh * 8192u + 16u * ln;
#pragma unroll
    for (int i = 0; i < 8; ++i) {
      const f32x4 x = fraga(i), y = fragb(i);
      store16_sc1(px + i * 1024, u32x4{cvtpk(x[0], x[1]), cvtpk(x[2], x[3]), cvtpk(y[0], y[1]), cvtpk(y[2], y[3])});
    }
  }
  wait_bar<0>();
  asm volatile("" : "+v"(ech2));
  if (w == 0u && ln < 4u) mb_store(Ows, bid, 8u + ln, (ech2 << 8) | 1u);
  if (rg == hg) {
#pragma unroll
    for (int i = 0; i < 8; ++i) {
      *(LAS f32x4*)(size_t)(ldsb + (hh * 8u + i) * 2048u + 16u * ln) = fraga(i);
      *(LAS f32x4*)(size_t)(ldsb + (hh * 8u + i) * 2048u + 1024u + 16u * ln) = fragb(i);
    }
  }
  if (w == 0u) {
    handoff_wait<true>(Ows, bid, 1u, hg, nonce, ln, ech2);
  }
  barrier();
  {
    const char* pin = Ows + WS_PX + (size_t)bid * (4u * 16384u) + w * 2048u + 16u * ln;
    u32x4 r[2][3];
#pragma unroll
    for (int k = 0; k < 3; ++k) {
      const char* s = pin + (size_t)((hg + 1u + k) & 3u) * 16384u;
      asm volatile("global_load_dwordx4 %0, %1, off sc1" : "=v"(r[0][k]) : "v"(s) : "memory");
      asm volatile("global_load_dwordx4 %0, %1, off offset:1024 sc1" : "=v"(r[1][k]) : "v"(s) : "memory");
    }
    asm volatile("s_waitcnt vmcnt(0)" : "+v"(r[0][0]), "+v"(r[0][1]), "+v"(r[0][2]), "+v"(r[1][0]), "+v"(r[1][1]), "+v"(r[1][2]) :: "memory");
    char* ob = out + ((size_t)(b * 192u + 48u * hg + (ln & 15u)) * 1280u + br * 640u + 160u * jq + 80u * hh + 4u * g3) * 2u;
#pragma unroll
    for (int rd = 0; rd < 2; ++rd) {
      const unsigned i = 2u * rg + rd;
      f32x4 s0 = *(const LAS f32x4*)(size_t)(ldsb + (2u * w + rd) * 2048u + 16u * ln), s1 = *(const LAS f32x4*)(size_t)(ldsb + (2u * w + rd) * 2048u + 1024u + 16u * ln);
#pragma unroll
      for (int k = 0; k < 3; ++k) {
        const u32x4 x = r[rd][k];
        s0 += f32x4{__uint_as_float(x[0] << 16), __uint_as_float(x[0] & 0xffff0000u), __uint_as_float(x[1] << 16), __uint_as_float(x[1] & 0xffff0000u)};
        s1 += f32x4{__uint_as_float(x[2] << 16), __uint_as_float(x[2] & 0xffff0000u), __uint_as_float(x[3] << 16), __uint_as_float(x[3] & 0xffff0000u)};
      }
      const unsigned nba = i < 6u ? 2u * (i / 3u) : 4u, qba = i < 6u ? i % 3u : (i == 6u ? 0u : 2u);
      const unsigned nbb = i < 6u ? nba + 1u : 4u, qbb = i < 6u ? qba : 1u;
      *(u32x2*)(ob + (size_t)qba * (16u * 2560u) + nba * 32u) = u32x2{cvtpk(s0[0], s0[1]), cvtpk(s0[2], s0[3])};
      if (i != 7u) *(u32x2*)(ob + (size_t)qbb * (16u * 2560u) + nbb * 32u) = u32x2{cvtpk(s1[0], s1[1]), cvtpk(s1[2], s1[3])};
    }
  }
}

extern "C" void kernel_launch(void* const* d_in, const int* in_sizes, int n_in,
                              void* d_out, int out_size, void* d_ws, size_t ws_size,
                              hipStream_t stream) {
  fused_attn_kernel<<<256, 512, 0, stream>>>((const char*)d_in[0], (const char*)d_in[1], (const char*)d_in[2], (const char*)d_in[3], (const char*)d_in[4],
                                             (const char*)d_in[5], (const char*)d_in[6], (const char*)d_in[7], (const char*)d_in[8], (const char*)d_in[9],
                                             (char*)d_out, (char*)d_ws);
}
```
